```python
import torch

def _softmax(x, axis=-1):
    return torch.softmax(x, dim=axis)
def _gelu(x):
    return torch.nn.functional.gelu(x, approximate="tanh")
def _silu(x):
    return torch.nn.functional.silu(x)
def _sigmoid(x):
    return torch.sigmoid(x)
def _layernorm(x, axis=-1, eps=1e-5):
    m = x.mean(dim=axis, keepdim=True)
    v = x.var(dim=axis, keepdim=True, unbiased=False)
    return (x - m) / torch.sqrt(v + eps)
def _scatter_add(tgt, idx, src, axis=0):
    return tgt.clone().index_add_(axis, idx, src)
def _segment_sum(src, seg, n):
    out = torch.zeros((n,) + src.shape[1:], dtype=src.dtype)
    return out.index_add_(0, seg, src)
def _pad(x, pw):
    flat = tuple(v for pair in reversed(pw) for v in pair)
    return torch.nn.functional.pad(x, flat)

kernel_name = 'recurrent_scan__mha_block__dense_activation_relu__prompt'

def setup_inputs():

    _w = (lambda a: (lambda t: t.to(torch.bfloat16) if t.is_floating_point() else t)(torch.as_tensor(a)))
    def _A_setup():
        B, S, D, H = 1, 2048, 640, 32
        return {
            "x": _w((lambda *s: torch.randn(*s, dtype=torch.bfloat16) * 0.05)(B, S, D)),
            "wqkv": _w((lambda *s: torch.randn(*s, dtype=torch.bfloat16) * 0.05)(D, 3 * D)),
            "wo": _w((lambda *s: torch.randn(*s, dtype=torch.bfloat16) * 0.05)(D, D)),
        }
    _A_in = _A_setup()

    def _B_setup():
        B, S, D, Dff = 1, 2048, 640, 1536
        return {
            "x": _w((lambda *s: torch.randn(*s, dtype=torch.bfloat16) * 0.05)(B, S, D)),
            "w1": _w((lambda *s: torch.randn(*s, dtype=torch.bfloat16) * 0.05)(D, Dff)),
            "w2": _w((lambda *s: torch.randn(*s, dtype=torch.bfloat16) * 0.05)(Dff, D)),
        }
    _B_in = _B_setup()

    return {
        "A_x": _A_in["x"],
        "A_wqkv": _A_in["wqkv"],
        "A_wo": _A_in["wo"],
        "B_x": _B_in["x"],
        "B_w1": _B_in["w1"],
        "B_w2": _B_in["w2"],
    }

def reference(A_x, A_wqkv, A_wo, B_x, B_w1, B_w2):
    def _A_ref(x, wqkv, wo):
        B, S, D = x.shape
        H = 32
        Dh = D // H
        qkv = (lambda x, *d: x.permute(*d))((x @ wqkv).reshape(B, S, 3, H, Dh), 2, 0, 3, 1, 4)
        q, k, v = qkv[0], qkv[1], qkv[2]
        scores = torch.einsum("bhqd,bhkd->bhqk", q, k) * (Dh ** -0.5)
        p = _softmax(scores, axis=-1)
        o = torch.einsum("bhqk,bhkd->bhqd", p, v)
        return (lambda x, *d: x.permute(*d))(o, 0, 2, 1, 3).reshape(B, S, D) @ wo

    def _B_ref(x, w1, w2):
        h = torch.einsum("bsd,df->bsf", x, w1)
        zero = torch.zeros_like(h)
        h = torch.maximum(h, zero)
        return torch.einsum("bsf,fd->bsd", h, w2)

    x = A_x
    xn = _layernorm(x, axis=-1)
    s = _A_ref(xn, A_wqkv, A_wo)
    g = _silu(_B_ref(xn, B_w1, B_w2))
    return x + s * g
```

```cpp
#include <hip/hip_runtime.h>
#include <hip/hip_bf16.h>

typedef __attribute__((ext_vector_type(8))) __bf16 bf16x8;
typedef __attribute__((ext_vector_type(16))) __bf16 bf16x16;
typedef __attribute__((ext_vector_type(4))) short s16x4;
typedef __attribute__((ext_vector_type(8))) short s16x8;
typedef __attribute__((ext_vector_type(16))) float f32x16;
typedef __attribute__((ext_vector_type(8))) float f32x8;
typedef __attribute__((ext_vector_type(4))) float f32x4;
typedef __attribute__((ext_vector_type(4))) unsigned u32x4;
typedef __attribute__((ext_vector_type(2))) unsigned u32x2;
#define LAS __attribute__((address_space(3)))
#define GAS __attribute__((address_space(1)))

constexpr int S_ = 2048, D_ = 640, F_ = 1536;
constexpr int QUAD = 10752;
constexpr size_t WS_XN = 0;
constexpr size_t WS_QM = WS_XN + (size_t)S_ * D_ * 2;
constexpr size_t WS_QR = WS_QM + 32 * 64 * 1024;
constexpr size_t WS_KV = WS_QR + 32 * 2048 * 8;
constexpr size_t WS_HP = WS_KV + (size_t)32 * 16 * QUAD;
constexpr size_t WS_OM = WS_HP + (size_t)192 * 2048 * 16;
constexpr size_t WS_OR = WS_OM + (size_t)64 * 2048 * 16;

__device__ __forceinline__ unsigned pk2(float lo, float hi) {
  typedef __attribute__((ext_vector_type(2))) __bf16 bf16x2;
  typedef __attribute__((ext_vector_type(2))) float f32x2;
  f32x2 v = {lo, hi};
  return __builtin_bit_cast(unsigned, __builtin_convertvector(v, bf16x2));
}
__device__ __forceinline__ float blo(unsigned u) { return __uint_as_float(u << 16); }
__device__ __forceinline__ float bhi(unsigned u) { return __uint_as_float(u & 0xffff0000u); }
__device__ __forceinline__ void glds16(const void* g, void* l) {
  __builtin_amdgcn_global_load_lds((const GAS void*)g, (LAS void*)l, 16, 0, 0);
}
__device__ __forceinline__ s16x4 trread(const char* p) {
  return __builtin_amdgcn_ds_read_tr16_b64_v4i16((LAS s16x4*)p);
}

__global__ void __launch_bounds__(512) k_ln(const char* x, char* ws) {
  int w = threadIdx.x >> 6, l = threadIdx.x & 63, row = blockIdx.x * 8 + w;
  u32x4 a = {0, 0, 0, 0}, b = {0, 0, 0, 0};
  if (l < 40) { a = *(const u32x4*)(x + (size_t)row * 1280 + l * 32); b = *(const u32x4*)(x + (size_t)row * 1280 + l * 32 + 16); }
  float v[16];
  for (int i = 0; i < 4; ++i) { v[2*i] = blo(a[i]); v[2*i+1] = bhi(a[i]); v[8+2*i] = blo(b[i]); v[9+2*i] = bhi(b[i]); }
  float s = 0; for (int i = 0; i < 16; ++i) s += v[i];
  for (int o = 32; o; o >>= 1) s += __shfl_xor(s, o);
  float m = s * (1.f / 640);
  float q = 0; if (l < 40) for (int i = 0; i < 16; ++i) q += (v[i] - m) * (v[i] - m);
  for (int o = 32; o; o >>= 1) q += __shfl_xor(q, o);
  float r = rsqrtf(q * (1.f / 640) + 1e-5f);
  if (l < 40) {
    for (int i = 0; i < 4; ++i) { a[i] = pk2((v[2*i] - m) * r, (v[2*i+1] - m) * r); b[i] = pk2((v[8+2*i] - m) * r, (v[9+2*i] - m) * r); }
    *(u32x4*)(ws + WS_XN + (size_t)row * 1280 + l * 32) = a; *(u32x4*)(ws + WS_XN + (size_t)row * 1280 + l * 32 + 16) = b;
  }
}

template <bool ISV>
__device__ __forceinline__ void k1_loop(const char* X, const char* W, int wpitch, int ntiles, char* lds, f32x16 (&acc)[4], int w, int l) {
  const int rt = w & 3, ch = w >> 2, r = l & 31, h = l >> 5;
  const int nmine = ch == 0 ? 4 : ntiles - 4;
  auto stage = [&](int c, char* buf) {
    for (int j = 0; j < 2; ++j) {
      int p = w * 2 + j, row = p * 8 + (l >> 3);
      glds16(X + (size_t)row * 1280 + c * 128 + (((l & 7) ^ ((row >> 1) & 7)) << 4), buf + p * 1024);
    }
    if (w < ntiles)
      for (int j = 0; j < 4; ++j)
        glds16(W + (size_t)(c * 64 + j * 16 + (l >> 2)) * wpitch + w * 64 + (l & 3) * 16, buf + 16384 + w * 4096 + j * 1024);
  };
  stage(0, lds);
  for (int c = 0; c < 10; ++c) {
    __syncthreads();
    char* buf = lds + (c & 1) * 45056;
    if (c + 1 < 10) stage(c + 1, lds + ((c + 1) & 1) * 45056);
#pragma unroll
    for (int s = 0; s < 4; ++s) {
      int row = rt * 32 + r;
      bf16x8 xa = *(const bf16x8*)(buf + row * 128 + (((2 * s + h) ^ ((row >> 1) & 7)) << 4));
#pragma unroll
      for (int t = 0; t < 4; ++t) {
        if (t < nmine) {
          const char* wp = buf + 16384 + (ch * 4 + t) * 4096 + (s * 16 + 8 * h + ((l & 15) >> 2)) * 64 + ((l >> 4) & 1) * 32 + (l & 3) * 8;
          s16x4 lo = trread(wp), hi = trread(wp + 256);
          s16x8 wv = {lo[0], lo[1], lo[2], lo[3], hi[0], hi[1], hi[2], hi[3]};
          bf16x8 wb = __builtin_bit_cast(bf16x8, wv);
          if (ISV) acc[t] = __builtin_amdgcn_mfma_f32_32x32x16_bf16(xa, wb, acc[t], 0, 0, 0);
          else     acc[t] = __builtin_amdgcn_mfma_f32_32x32x16_bf16(wb, xa, acc[t], 0, 0, 0);
        }
      }
    }
  }
}

__global__ void __launch_bounds__(512) k_gemm1(const char* wqkv, const char* w1, char* ws) {
  extern __shared__ __attribute__((aligned(16))) char lds[];
  int b = blockIdx.x, xcd = b & 7, i = b >> 3;
  int cb = (xcd & 3) * 4 + (i & 3), rb = (xcd >> 2) * 8 + (i >> 2);
  int w = __builtin_amdgcn_readfirstlane(threadIdx.x >> 6), l = threadIdx.x & 63;
  int kind, t0, nt;
  if (cb < 9) { kind = cb / 3; int j = cb % 3; t0 = j * 7; nt = j == 2 ? 6 : 7; }
  else { kind = 3; int j = cb - 9; t0 = j * 7; nt = j == 6 ? 6 : 7; }
  const char* W = kind < 3 ? wqkv + (kind * 20 + t0) * 64 : w1 + t0 * 64;
  int wpitch = kind < 3 ? 3840 : 3072;
  const char* X = ws + WS_XN + (size_t)rb * 128 * 1280;
  f32x16 acc[4];
  for (int t = 0; t < 4; ++t) for (int j = 0; j < 16; ++j) acc[t][j] = 0.f;
  if (kind == 2) k1_loop<true>(X, W, wpitch, nt, lds, acc, w, l);
  else k1_loop<false>(X, W, wpitch, nt, lds, acc, w, l);
  const int rt = w & 3, ch = w >> 2, r = l & 31, h = l >> 5;
  const int nmine = ch == 0 ? 4 : nt - 4;
  const int T = rb * 4 + rt;
  const float qs = 0.22360679774997896f * 1.4426950408889634f;
#pragma unroll
  for (int t = 0; t < 4; ++t) {
    if (t >= nmine) continue;
    int ct = t0 + ch * 4 + t;
#pragma unroll
    for (int gi = 0; gi < 4; ++gi) {
      float a0 = acc[t][4*gi], a1 = acc[t][4*gi+1], a2 = acc[t][4*gi+2], a3 = acc[t][4*gi+3];
      char* dst;
      if (kind == 0) {
        a0 *= qs; a1 *= qs; a2 *= qs; a3 *= qs;
        int G = ct * 8 + 2 * gi + h, head = G / 5, sub = G % 5;
        dst = sub < 4 ? ws + WS_QM + ((size_t)(head * 64 + T) * 64 + (sub >> 1) * 32 + r) * 16 + (sub & 1) * 8
                      : ws + WS_QR + ((size_t)head * 2048 + T * 32 + r) * 8;
      } else if (kind == 1) {
        int G = ct * 8 + 2 * gi + h, head = G / 5, sub = G % 5;
        char* base = ws + WS_KV + (size_t)(head * 16 + (T >> 2)) * QUAD;
        dst = sub < 4 ? base + ((T & 3) * 64 + (sub >> 1) * 32 + r) * 16 + (sub & 1) * 8
                      : base + 4096 + ((T & 1) * 32 + r) * 16 + ((T >> 1) & 1) * 8;
      } else if (kind == 2) {
        int n = ct * 32 + r, head = n / 20, d = n % 20;
        char* base = ws + WS_KV + (size_t)(head * 16 + (T >> 2)) * QUAD + 5120 + ((T & 3) * 4 + gi) * 352;
        dst = base + d * 16 + h * 8;
        if (d == 0) { unsigned c = h == 0 ? 0x3f803f80u : 0u; *(u32x4*)(base + 320 + h * 16) = (u32x4){c, c, c, c}; }
      } else {
        a0 = fmaxf(a0, 0.f); a1 = fmaxf(a1, 0.f); a2 = fmaxf(a2, 0.f); a3 = fmaxf(a3, 0.f);
        dst = ws + WS_HP + ((size_t)(ct * 4 + gi) * 2048 + T * 32 + r) * 16 + h * 8;
      }
      *(u32x2*)dst = (u32x2){pk2(a0, a1), pk2(a2, a3)};
    }
  }
}

__device__ __forceinline__ f32x16 qk(bf16x8 km, s16x4 kr, bf16x8 qm, s16x4 qr) {
  f32x16 z; for (int i = 0; i < 16; ++i) z[i] = 0.f;
  f32x16 s = __builtin_amdgcn_mfma_f32_32x32x16_bf16(km, qm, z, 0, 0, 0);
  return __builtin_amdgcn_mfma_f32_32x32x8bf16_1k(kr, qr, s, 0, 0, 0);
}
__device__ __forceinline__ f32x16 pv(bf16x8 v0, bf16x8 v1, f32x16 p, f32x16 o) {
  f32x8 lo = __builtin_shufflevector(p, p, 0, 1, 2, 3, 4, 5, 6, 7), hi = __builtin_shufflevector(p, p, 8, 9, 10, 11, 12, 13, 14, 15);
  o = __builtin_amdgcn_smfmac_f32_32x32x32_bf16(v0, __builtin_bit_cast(bf16x16, lo), o, (int)0xDDDDDDDD, 0, 0);
  return __builtin_amdgcn_smfmac_f32_32x32x32_bf16(v1, __builtin_bit_cast(bf16x16, hi), o, (int)0xDDDDDDDD, 0, 0);
}

__device__ __forceinline__ void attn_safe(const char* kv, bf16x8 qm, s16x4 qa, s16x4 qb, int l, f32x16& o) {
  const int r = l & 31, h = l >> 5, e = r < 21 ? r : 21;
  float m = -INFINITY;
  for (int i = 0; i < 16; ++i) o[i] = 0.f;
  for (int t = 0; t < 64; ++t) {
    const char* base = kv + (t >> 2) * QUAD; int tq = t & 3;
    bf16x8 km = *(const bf16x8*)(base + tq * 1024 + l * 16);
    s16x4 kr = *(const s16x4*)(base + 4096 + l * 16 + (tq >> 1) * 8);
    bf16x8 v0 = *(const bf16x8*)(base + 5120 + (tq * 4 + h) * 352 + e * 16), v1 = *(const bf16x8*)(base + 5120 + (tq * 4 + 2 + h) * 352 + e * 16);
    f32x16 s = qk(km, kr, qm, (tq & 1) ? qb : qa);
    float mx = s[0]; for (int i = 1; i < 16; ++i) mx = fmaxf(mx, s[i]);
    mx = fmaxf(mx, __shfl_xor(mx, 32));
    float mn = fmaxf(m, mx), al = __builtin_amdgcn_exp2f(m - mn);
    m = mn;
    for (int i = 0; i < 16; ++i) { s[i] = __builtin_amdgcn_exp2f(s[i] - mn); o[i] *= al; }
    o = pv(v0, v1, s, o);
  }
}

__global__ void __launch_bounds__(512) k_attn(char* ws) {
  extern __shared__ __attribute__((aligned(16))) char lds[];
  int b = blockIdx.x, xcd = b & 7, i = b >> 3;
  int head = xcd * 4 + (i & 3), qb = i >> 2;
  int w = __builtin_amdgcn_readfirstlane(threadIdx.x >> 6), l = threadIdx.x & 63;
  const int g = w & 3, par = w >> 2, r = l & 31, h = l >> 5, e = r < 21 ? r : 21;
  const char* kv = ws + WS_KV + (size_t)head * 16 * QUAD;
  auto stage = [&](int s, char* buf) {
    if (w < 7) for (int j = 0; j < 3; ++j) glds16(kv + (size_t)s * 2 * QUAD + (w * 3 + j) * 1024 + l * 16, buf + (w * 3 + j) * 1024);
  };
  stage(0, lds);
  bf16x8 qm[2]; s16x4 qa[2], qbb[2];
  for (int t = 0; t < 2; ++t) {
    int T = qb * 8 + g * 2 + t;
    qm[t] = *(const bf16x8*)(ws + WS_QM + ((size_t)(head * 64 + T) * 64 + l) * 16);
    s16x4 q = *(const s16x4*)(ws + WS_QR + ((size_t)head * 2048 + T * 32 + r) * 8);
    s16x4 z = {0, 0, 0, 0};
    qa[t] = h == 0 ? q : z; qbb[t] = h == 1 ? q : z;
  }
  f32x16 o[2];
  for (int t = 0; t < 2; ++t) for (int j = 0; j < 16; ++j) o[t][j] = 0.f;
  for (int s = 0; s < 8; ++s) {
    __syncthreads();
    const char* base = lds + (s & 1) * 2 * QUAD + par * QUAD;
    if (s + 1 < 8) stage(s + 1, lds + ((s + 1) & 1) * 2 * QUAD);
    s16x8 kr4 = *(const s16x8*)(base + 4096 + l * 16);
#pragma unroll
    for (int tq = 0; tq < 4; ++tq) {
      bf16x8 km = *(const bf16x8*)(base + tq * 1024 + l * 16);
      s16x4 kr = tq < 2 ? __builtin_shufflevector(kr4, kr4, 0, 1, 2, 3) : __builtin_shufflevector(kr4, kr4, 4, 5, 6, 7);
      bf16x8 v0 = *(const bf16x8*)(base + 5120 + (tq * 4 + h) * 352 + e * 16), v1 = *(const bf16x8*)(base + 5120 + (tq * 4 + 2 + h) * 352 + e * 16);
#pragma unroll
      for (int t = 0; t < 2; ++t) {
        f32x16 p = qk(km, kr, qm[t], (tq & 1) ? qbb[t] : qa[t]);
        for (int j = 0; j < 16; ++j) p[j] = __builtin_amdgcn_exp2f(p[j]);
        o[t] = pv(v0, v1, p, o[t]);
      }
    }
  }
  __syncthreads();
  f32x16 give = par ? o[0] : o[1], keep = par ? o[1] : o[0];
  f32x4* xb = (f32x4*)lds;
  for (int j = 0; j < 3; ++j) xb[(w * 3 + j) * 64 + l] = (f32x4){give[4*j], give[4*j+1], give[4*j+2], give[4*j+3]};
  __syncthreads();
  for (int j = 0; j < 3; ++j) { f32x4 v = xb[((w ^ 4) * 3 + j) * 64 + l]; for (int k = 0; k < 4; ++k) keep[4*j+k] += v[k]; }
  float lsum = __shfl(keep[8], r + 32);
  bool ok = lsum >= 0x1p-30f && lsum <= 0x1p60f;
  for (int j = 0; j < 12; ++j) ok = ok && (fabsf(keep[j]) <= 0x1p120f);
  if (__any(!ok)) {
    attn_safe(kv, par ? qm[1] : qm[0], par ? qa[1] : qa[0], par ? qbb[1] : qbb[0], l, keep);
    lsum = __shfl(keep[8], r + 32);
  }
  float inv = 1.f / lsum;
  int tok = (qb * 8 + g * 2 + par) * 32 + r;
  for (int j = 0; j < 2; ++j)
    *(u32x2*)(ws + WS_OM + ((size_t)(head * 2 + j) * 2048 + tok) * 16 + h * 8) = (u32x2){pk2(keep[4*j] * inv, keep[4*j+1] * inv), pk2(keep[4*j+2] * inv, keep[4*j+3] * inv)};
  if (h == 0) *(u32x2*)(ws + WS_OR + ((size_t)head * 2048 + tok) * 8) = (u32x2){pk2(keep[8] * inv, keep[9] * inv), pk2(keep[10] * inv, keep[11] * inv)};
}

__global__ void __launch_bounds__(256) k_out(const char* x, const char* wo, const char* w2, char* ws, char* out) {
  extern __shared__ __attribute__((aligned(16))) char lds[];
  int b = blockIdx.x, xcd = b & 7, i = b >> 3;
  int cb = i & 7, rb = xcd * 4 + (i >> 3);
  int w = __builtin_amdgcn_readfirstlane(threadIdx.x >> 6), l = threadIdx.x & 63;
  const int c = l & 15, g = l >> 4;
  const int tok = rb * 64 + w * 16 + c;
  auto stage = [&](int ch, char* buf) {
    int st = ch * 4 + w;
    for (int j = 0; j < 5; ++j) {
      int en = j * 64 + l, rho = en / 10, cc = en % 10;
      int gg = (rho & 15) >> 2, jj = (rho & 3) + 4 * (rho >> 4);
      const char* src;
      if (st < 16) { int p = st * 4 + gg; src = wo + (size_t)(20 * (p >> 1) + 8 * (p & 1) + jj) * 1280; }
      else if (st < 20) { src = wo + (size_t)(20 * (8 * (st - 16) + 2 * gg + (jj >> 2)) + 16 + (jj & 3)) * 1280; }
      else src = w2 + (size_t)((st - 20) * 32 + 8 * gg + jj) * 1280;
      glds16(src + cb * 160 + cc * 16, buf + w * 5120 + j * 1024);
    }
  };
  f32x4 acc[5], sv[5];
  for (int t = 0; t < 5; ++t) acc[t] = (f32x4){0, 0, 0, 0};
  stage(0, lds);
  for (int ch = 0; ch < 17; ++ch) {
    bf16x8 a[4];
#pragma unroll
    for (int s = 0; s < 4; ++s) {
      int st = ch * 4 + s;
      if (ch < 4) a[s] = *(const bf16x8*)(ws + WS_OM + ((size_t)(st * 4 + g) * 2048 + tok) * 16);
      else if (ch == 4) {
        int hd = 8 * s + 2 * g;
        u32x2 p = *(const u32x2*)(ws + WS_OR + ((size_t)hd * 2048 + tok) * 8), q = *(const u32x2*)(ws + WS_OR + ((size_t)(hd + 1) * 2048 + tok) * 8);
        a[s] = __builtin_bit_cast(bf16x8, (u32x4){p[0], p[1], q[0], q[1]});
      } else a[s] = *(const bf16x8*)(ws + WS_HP + ((size_t)((st - 20) * 4 + g) * 2048 + tok) * 16);
    }
    __syncthreads();
    const char* buf = lds + (ch & 1) * 20480;
    if (ch + 1 < 17) stage(ch + 1, lds + ((ch + 1) & 1) * 20480);
#pragma unroll
    for (int s = 0; s < 4; ++s)
#pragma unroll
      for (int t = 0; t < 5; ++t) {
        const char* wp = buf + s * 5120 + (4 * g + (c >> 2)) * 160 + t * 32 + (c & 3) * 8;
        s16x4 lo = trread(wp), hi = trread(wp + 2560);
        s16x8 wv = {lo[0], lo[1], lo[2], lo[3], hi[0], hi[1], hi[2], hi[3]};
        acc[t] = __builtin_amdgcn_mfma_f32_16x16x32_bf16(__builtin_bit_cast(bf16x8, wv), a[s], acc[t], 0, 0, 0);
      }
    if (ch == 4) for (int t = 0; t < 5; ++t) { sv[t] = acc[t]; acc[t] = (f32x4){0, 0, 0, 0}; }
  }
  for (int t = 0; t < 5; ++t) {
    size_t off = (size_t)tok * 1280 + cb * 160 + t * 32 + g * 8;
    u32x2 xv = *(const u32x2*)(x + off);
    float o[4];
    for (int j = 0; j < 4; ++j) {
      float gv = acc[t][j];
      float sg = gv / (1.f + __expf(-gv));
      float xx = (j & 1) ? bhi(xv[j >> 1]) : blo(xv[j >> 1]);
      o[j] = xx + sv[t][j] * sg;
    }
    *(u32x2*)(out + off) = (u32x2){pk2(o[0], o[1]), pk2(o[2], o[3])};
  }
}

extern "C" void kernel_launch(void* const* d_in, const int* in_sizes, int n_in, void* d_out, int out_size,
                              void* d_ws, size_t ws_size, hipStream_t stream) {
  const char* x = (const char*)d_in[0];
  const char* wqkv = (const char*)d_in[1];
  const char* wo = (const char*)d_in[2];
  const char* w1 = (const char*)d_in[4];
  const char* w2 = (const char*)d_in[5];
  char* ws = (char*)d_ws;
  (void)hipFuncSetAttribute((const void*)k_gemm1, hipFuncAttributeMaxDynamicSharedMemorySize, 2 * 45056);
  k_ln<<<256, 512, 0, stream>>>(x, ws);
  k_gemm1<<<256, 512, 2 * 45056, stream>>>(wqkv, w1, ws);
  k_attn<<<256, 512, 4 * QUAD, stream>>>(ws);
  k_out<<<256, 256, 2 * 20480, stream>>>(x, wo, w2, ws, (char*)d_out);
}
```

```cpp
#define QSEL 6
#define QMUL 20
#include <hip/hip_runtime.h>
#include <hip/hip_bf16.h>

typedef __attribute__((ext_vector_type(8))) __bf16 bf16x8;
typedef __attribute__((ext_vector_type(16))) __bf16 bf16x16;
typedef __attribute__((ext_vector_type(4))) short s16x4;
typedef __attribute__((ext_vector_type(8))) short s16x8;
typedef __attribute__((ext_vector_type(16))) float f32x16;
typedef __attribute__((ext_vector_type(8))) float f32x8;
typedef __attribute__((ext_vector_type(4))) float f32x4;
typedef __attribute__((ext_vector_type(4))) unsigned u32x4;
typedef __attribute__((ext_vector_type(2))) unsigned u32x2;
#define LAS __attribute__((address_space(3)))
#define GAS __attribute__((address_space(1)))

constexpr int S_ = 2048, D_ = 640, F_ = 1536;
constexpr int QUAD = 10752;
constexpr size_t WS_XN = 0;
constexpr size_t WS_QM = WS_XN + (size_t)S_ * D_ * 2;
constexpr size_t WS_QR = WS_QM + 32 * 64 * 1024;
constexpr size_t WS_KV = WS_QR + 32 * 2048 * 8;
constexpr size_t WS_HP = WS_KV + (size_t)32 * 16 * QUAD;
constexpr size_t WS_OM = WS_HP + (size_t)192 * 2048 * 16;
constexpr size_t WS_OR = WS_OM + (size_t)64 * 2048 * 16;

__device__ __forceinline__ unsigned pk2(float lo, float hi) {
  typedef __attribute__((ext_vector_type(2))) __bf16 bf16x2;
  typedef __attribute__((ext_vector_type(2))) float f32x2;
  f32x2 v = {lo, hi};
  return __builtin_bit_cast(unsigned, __builtin_convertvector(v, bf16x2));
}
__device__ __forceinline__ float blo(unsigned u) { return __uint_as_float(u << 16); }
__device__ __forceinline__ float bhi(unsigned u) { return __uint_as_float(u & 0xffff0000u); }
__device__ __forceinline__ void glds16(const void* g, void* l) {
  __builtin_amdgcn_global_load_lds((const GAS void*)g, (LAS void*)l, 16, 0, 0);
}
__device__ __forceinline__ s16x4 trread(const char* p) {
  return __builtin_amdgcn_ds_read_tr16_b64_v4i16((LAS s16x4*)p);
}

constexpr size_t WS_ST = 64u << 20;
__device__ __forceinline__ void stamp(char* ws, int k, int which) {
  if (threadIdx.x == 0) { unsigned long long* p = (unsigned long long*)(ws + WS_ST) + ((k * 2 + which) * 256 + blockIdx.x) * 2; p[0] = __builtin_amdgcn_s_memrealtime(); p[1] = __builtin_amdgcn_s_memtime(); }
}
__global__ void k_report(char* ws) {
  const unsigned long long* p = (const unsigned long long*)(ws + WS_ST);
  unsigned long long mn[4], mx[4];
  for (int k = 0; k < 4; ++k) { mn[k] = ~0ull; mx[k] = 0; for (int b = 0; b < 256; ++b) { unsigned long long a = p[((k*2)*256+b)*2], e = p[((k*2+1)*256+b)*2]; mn[k] = a < mn[k] ? a : mn[k]; mx[k] = e > mx[k] ? e : mx[k]; } }
  long long q = 0;
  if (QSEL == 1) q = mx[0] - mn[0];
  if (QSEL == 2) q = mn[1] - mx[0];
  if (QSEL == 3) q = mx[1] - mn[1];
  if (QSEL == 4) q = mx[2] - mn[2];
  if (QSEL == 5) q = mx[3] - mn[3];
  if (QSEL == 6) q = (p[(5*256+7)*2+1] - p[(4*256+7)*2+1]) * 10 / (p[(5*256+7)*2] - p[(4*256+7)*2]);
  if (QSEL == 7) q = mn[2] - mx[1];
  if (QSEL == 8) q = mn[3] - mx[2];
  if (QSEL == 9) q = mx[3] - mn[0];
  if (q > 20000) q = 20000;
  unsigned long long t0 = __builtin_amdgcn_s_memrealtime();
  while ((long long)(__builtin_amdgcn_s_memrealtime() - t0) < q * QMUL) __builtin_amdgcn_s_sleep(4);
}
__global__ void __launch_bounds__(512) k_ln(const char* x, char* ws) {
  stamp(ws, 0, 0);
  int w = threadIdx.x >> 6, l = threadIdx.x & 63, row = blockIdx.x * 8 + w;
  u32x4 a = {0, 0, 0, 0}, b = {0, 0, 0, 0};
  if (l < 40) { a = *(const u32x4*)(x + (size_t)row * 1280 + l * 32); b = *(const u32x4*)(x + (size_t)row * 1280 + l * 32 + 16); }
  float v[16];
  for (int i = 0; i < 4; ++i) { v[2*i] = blo(a[i]); v[2*i+1] = bhi(a[i]); v[8+2*i] = blo(b[i]); v[9+2*i] = bhi(b[i]); }
  float s = 0; for (int i = 0; i < 16; ++i) s += v[i];
  for (int o = 32; o; o >>= 1) s += __shfl_xor(s, o);
  float m = s * (1.f / 640);
  float q = 0; if (l < 40) for (int i = 0; i < 16; ++i) q += (v[i] - m) * (v[i] - m);
  for (int o = 32; o; o >>= 1) q += __shfl_xor(q, o);
  float r = rsqrtf(q * (1.f / 640) + 1e-5f);
  if (l < 40) {
    for (int i = 0; i < 4; ++i) { a[i] = pk2((v[2*i] - m) * r, (v[2*i+1] - m) * r); b[i] = pk2((v[8+2*i] - m) * r, (v[9+2*i] - m) * r); }
    *(u32x4*)(ws + WS_XN + (size_t)row * 1280 + l * 32) = a; *(u32x4*)(ws + WS_XN + (size_t)row * 1280 + l * 32 + 16) = b;
  }
  __syncthreads(); stamp(ws, 0, 1);
}

template <bool ISV>
__device__ __forceinline__ void k1_loop(const char* X, const char* W, int wpitch, int ntiles, char* lds, f32x16 (&acc)[4], int w, int l) {
  const int rt = w & 3, ch = w >> 2, r = l & 31, h = l >> 5;
  const int nmine = ch == 0 ? 4 : ntiles - 4;
  auto stage = [&](int c, char* buf) {
    for (int j = 0; j < 2; ++j) {
      int p = w * 2 + j, row = p * 8 + (l >> 3);
      glds16(X + (size_t)row * 1280 + c * 128 + (((l & 7) ^ ((row >> 1) & 7)) << 4), buf + p * 1024);
    }
    if (w < ntiles)
      for (int j = 0; j < 4; ++j)
        glds16(W + (size_t)(c * 64 + j * 16 + (l >> 2)) * wpitch + w * 64 + (l & 3) * 16, buf + 16384 + w * 4096 + j * 1024);
  };
  stage(0, lds);
  for (int c = 0; c < 10; ++c) {
    __syncthreads();
    char* buf = lds + (c & 1) * 45056;
    if (c + 1 < 10) stage(c + 1, lds + ((c + 1) & 1) * 45056);
#pragma unroll
    for (int s = 0; s < 4; ++s) {
      int row = rt * 32 + r;
      bf16x8 xa = *(const bf16x8*)(buf + row * 128 + (((2 * s + h) ^ ((row >> 1) & 7)) << 4));
#pragma unroll
      for (int t = 0; t < 4; ++t) {
        if (t < nmine) {
          const char* wp = buf + 16384 + (ch * 4 + t) * 4096 + (s * 16 + 8 * h + ((l & 15) >> 2)) * 64 + ((l >> 4) & 1) * 32 + (l & 3) * 8;
          s16x4 lo = trread(wp), hi = trread(wp + 256);
          s16x8 wv = {lo[0], lo[1], lo[2], lo[3], hi[0], hi[1], hi[2], hi[3]};
          bf16x8 wb = __builtin_bit_cast(bf16x8, wv);
          if (ISV) acc[t] = __builtin_amdgcn_mfma_f32_32x32x16_bf16(xa, wb, acc[t], 0, 0, 0);
          else     acc[t] = __builtin_amdgcn_mfma_f32_32x32x16_bf16(wb, xa, acc[t], 0, 0, 0);
        }
      }
    }
  }
}

__global__ void __launch_bounds__(512) k_gemm1(const char* wqkv, const char* w1, char* ws) {
  stamp(ws, 1, 0);
  extern __shared__ __attribute__((aligned(16))) char lds[];
  int b = blockIdx.x, xcd = b & 7, i = b >> 3;
  int cb = (xcd & 3) * 4 + (i & 3), rb = (xcd >> 2) * 8 + (i >> 2);
  int w = __builtin_amdgcn_readfirstlane(threadIdx.x >> 6), l = threadIdx.x & 63;
  int kind, t0, nt;
  if (cb < 9) { kind = cb / 3; int j = cb % 3; t0 = j * 7; nt = j == 2 ? 6 : 7; }
  else { kind = 3; int j = cb - 9; t0 = j * 7; nt = j == 6 ? 6 : 7; }
  const char* W = kind < 3 ? wqkv + (kind * 20 + t0) * 64 : w1 + t0 * 64;
  int wpitch = kind < 3 ? 3840 : 3072;
  const char* X = ws + WS_XN + (size_t)rb * 128 * 1280;
  f32x16 acc[4];
  for (int t = 0; t < 4; ++t) for (int j = 0; j < 16; ++j) acc[t][j] = 0.f;
  if (kind == 2) k1_loop<true>(X, W, wpitch, nt, lds, acc, w, l);
  else k1_loop<false>(X, W, wpitch, nt, lds, acc, w, l);
  const int rt = w & 3, ch = w >> 2, r = l & 31, h = l >> 5;
  const int nmine = ch == 0 ? 4 : nt - 4;
  const int T = rb * 4 + rt;
  const float qs = 0.22360679774997896f * 1.4426950408889634f;
#pragma unroll
  for (int t = 0; t < 4; ++t) {
    if (t >= nmine) continue;
    int ct = t0 + ch * 4 + t;
#pragma unroll
    for (int gi = 0; gi < 4; ++gi) {
      float a0 = acc[t][4*gi], a1 = acc[t][4*gi+1], a2 = acc[t][4*gi+2], a3 = acc[t][4*gi+3];
      char* dst;
      if (kind == 0) {
        a0 *= qs; a1 *= qs; a2 *= qs; a3 *= qs;
        int G = ct * 8 + 2 * gi + h, head = G / 5, sub = G % 5;
        dst = sub < 4 ? ws + WS_QM + ((size_t)(head * 64 + T) * 64 + (sub >> 1) * 32 + r) * 16 + (sub & 1) * 8
                      : ws + WS_QR + ((size_t)head * 2048 + T * 32 + r) * 8;
      } else if (kind == 1) {
        int G = ct * 8 + 2 * gi + h, head = G / 5, sub = G % 5;
        char* base = ws + WS_KV + (size_t)(head * 16 + (T >> 2)) * QUAD;
        dst = sub < 4 ? base + ((T & 3) * 64 + (sub >> 1) * 32 + r) * 16 + (sub & 1) * 8
                      : base + 4096 + ((T & 1) * 32 + r) * 16 + ((T >> 1) & 1) * 8;
      } else if (kind == 2) {
        int n = ct * 32 + r, head = n / 20, d = n % 20;
        char* base = ws + WS_KV + (size_t)(head * 16 + (T >> 2)) * QUAD + 5120 + ((T & 3) * 4 + gi) * 352;
        dst = base + d * 16 + h * 8;
        if (d == 0) { unsigned c = h == 0 ? 0x3f803f80u : 0u; *(u32x4*)(base + 320 + h * 16) = (u32x4){c, c, c, c}; }
      } else {
        a0 = fmaxf(a0, 0.f); a1 = fmaxf(a1, 0.f); a2 = fmaxf(a2, 0.f); a3 = fmaxf(a3, 0.f);
        dst = ws + WS_HP + ((size_t)(ct * 4 + gi) * 2048 + T * 32 + r) * 16 + h * 8;
      }
      *(u32x2*)dst = (u32x2){pk2(a0, a1), pk2(a2, a3)};
    }
  }
  __syncthreads(); stamp(ws, 1, 1);
}

__device__ __forceinline__ f32x16 qk(bf16x8 km, s16x4 kr, bf16x8 qm, s16x4 qr) {
  f32x16 z; for (int i = 0; i < 16; ++i) z[i] = 0.f;
  f32x16 s = __builtin_amdgcn_mfma_f32_32x32x16_bf16(km, qm, z, 0, 0, 0);
  return __builtin_amdgcn_mfma_f32_32x32x8bf16_1k(kr, qr, s, 0, 0, 0);
}
__device__ __forceinline__ f32x16 pv(bf16x8 v0, bf16x8 v1, f32x16 p, f32x16 o) {
  f32x8 lo = __builtin_shufflevector(p, p, 0, 1, 2, 3, 4, 5, 6, 7), hi = __builtin_shufflevector(p, p, 8, 9, 10, 11, 12, 13, 14, 15);
  o = __builtin_amdgcn_smfmac_f32_32x32x32_bf16(v0, __builtin_bit_cast(bf16x16, lo), o, (int)0xDDDDDDDD, 0, 0);
  return __builtin_amdgcn_smfmac_f32_32x32x32_bf16(v1, __builtin_bit_cast(bf16x16, hi), o, (int)0xDDDDDDDD, 0, 0);
}

__device__ __forceinline__ void attn_safe(const char* kv, bf16x8 qm, s16x4 qa, s16x4 qb, int l, f32x16& o) {
  const int r = l & 31, h = l >> 5, e = r < 21 ? r : 21;
  float m = -INFINITY;
  for (int i = 0; i < 16; ++i) o[i] = 0.f;
  for (int t = 0; t < 64; ++t) {
    const char* base = kv + (t >> 2) * QUAD; int tq = t & 3;
    bf16x8 km = *(const bf16x8*)(base + tq * 1024 + l * 16);
    s16x4 kr = *(const s16x4*)(base + 4096 + l * 16 + (tq >> 1) * 8);
    bf16x8 v0 = *(const bf16x8*)(base + 5120 + (tq * 4 + h) * 352 + e * 16), v1 = *(const bf16x8*)(base + 5120 + (tq * 4 + 2 + h) * 352 + e * 16);
    f32x16 s = qk(km, kr, qm, (tq & 1) ? qb : qa);
    float mx = s[0]; for (int i = 1; i < 16; ++i) mx = fmaxf(mx, s[i]);
    mx = fmaxf(mx, __shfl_xor(mx, 32));
    float mn = fmaxf(m, mx), al = __builtin_amdgcn_exp2f(m - mn);
    m = mn;
    for (int i = 0; i < 16; ++i) { s[i] = __builtin_amdgcn_exp2f(s[i] - mn); o[i] *= al; }
    o = pv(v0, v1, s, o);
  }
}

__global__ void __launch_bounds__(512) k_attn(char* ws) {
  stamp(ws, 2, 0);
  extern __shared__ __attribute__((aligned(16))) char lds[];
  int b = blockIdx.x, xcd = b & 7, i = b >> 3;
  int head = xcd * 4 + (i & 3), qb = i >> 2;
  int w = __builtin_amdgcn_readfirstlane(threadIdx.x >> 6), l = threadIdx.x & 63;
  const int g = w & 3, par = w >> 2, r = l & 31, h = l >> 5, e = r < 21 ? r : 21;
  const char* kv = ws + WS_KV + (size_t)head * 16 * QUAD;
  auto stage = [&](int s, char* buf) {
    if (w < 7) for (int j = 0; j < 3; ++j) glds16(kv + (size_t)s * 2 * QUAD + (w * 3 + j) * 1024 + l * 16, buf + (w * 3 + j) * 1024);
  };
  stage(0, lds);
  bf16x8 qm[2]; s16x4 qa[2], qbb[2];
  for (int t = 0; t < 2; ++t) {
    int T = qb * 8 + g * 2 + t;
    qm[t] = *(const bf16x8*)(ws + WS_QM + ((size_t)(head * 64 + T) * 64 + l) * 16);
    s16x4 q = *(const s16x4*)(ws + WS_QR + ((size_t)head * 2048 + T * 32 + r) * 8);
    s16x4 z = {0, 0, 0, 0};
    qa[t] = h == 0 ? q : z; qbb[t] = h == 1 ? q : z;
  }
  f32x16 o[2];
  for (int t = 0; t < 2; ++t) for (int j = 0; j < 16; ++j) o[t][j] = 0.f;
  for (int s = 0; s < 8; ++s) {
    __syncthreads();
    const char* base = lds + (s & 1) * 2 * QUAD + par * QUAD;
    if (s + 1 < 8) stage(s + 1, lds + ((s + 1) & 1) * 2 * QUAD);
    s16x8 kr4 = *(const s16x8*)(base + 4096 + l * 16);
#pragma unroll
    for (int tq = 0; tq < 4; ++tq) {
      bf16x8 km = *(const bf16x8*)(base + tq * 1024 + l * 16);
      s16x4 kr = tq < 2 ? __builtin_shufflevector(kr4, kr4, 0, 1, 2, 3) : __builtin_shufflevector(kr4, kr4, 4, 5, 6, 7);
      bf16x8 v0 = *(const bf16x8*)(base + 5120 + (tq * 4 + h) * 352 + e * 16), v1 = *(const bf16x8*)(base + 5120 + (tq * 4 + 2 + h) * 352 + e * 16);
#pragma unroll
      for (int t = 0; t < 2; ++t) {
        f32x16 p = qk(km, kr, qm[t], (tq & 1) ? qbb[t] : qa[t]);
        for (int j = 0; j < 16; ++j) p[j] = __builtin_amdgcn_exp2f(p[j]);
        o[t] = pv(v0, v1, p, o[t]);
      }
    }
  }
  __syncthreads();
  f32x16 give = par ? o[0] : o[1], keep = par ? o[1] : o[0];
  f32x4* xb = (f32x4*)lds;
  for (int j = 0; j < 3; ++j) xb[(w * 3 + j) * 64 + l] = (f32x4){give[4*j], give[4*j+1], give[4*j+2], give[4*j+3]};
  __syncthreads();
  for (int j = 0; j < 3; ++j) { f32x4 v = xb[((w ^ 4) * 3 + j) * 64 + l]; for (int k = 0; k < 4; ++k) keep[4*j+k] += v[k]; }
  float lsum = __shfl(keep[8], r + 32);
  bool ok = lsum >= 0x1p-30f && lsum <= 0x1p60f;
  for (int j = 0; j < 12; ++j) ok = ok && (fabsf(keep[j]) <= 0x1p120f);
  if (__any(!ok)) {
    attn_safe(kv, par ? qm[1] : qm[0], par ? qa[1] : qa[0], par ? qbb[1] : qbb[0], l, keep);
    lsum = __shfl(keep[8], r + 32);
  }
  float inv = 1.f / lsum;
  int tok = (qb * 8 + g * 2 + par) * 32 + r;
  for (int j = 0; j < 2; ++j)
    *(u32x2*)(ws + WS_OM + ((size_t)(head * 2 + j) * 2048 + tok) * 16 + h * 8) = (u32x2){pk2(keep[4*j] * inv, keep[4*j+1] * inv), pk2(keep[4*j+2] * inv, keep[4*j+3] * inv)};
  if (h == 0) *(u32x2*)(ws + WS_OR + ((size_t)head * 2048 + tok) * 8) = (u32x2){pk2(keep[8] * inv, keep[9] * inv), pk2(keep[10] * inv, keep[11] * inv)};
  __syncthreads(); stamp(ws, 2, 1);
}

__global__ void __launch_bounds__(256) k_out(const char* x, const char* wo, const char* w2, char* ws, char* out) {
  stamp(ws, 3, 0);
  extern __shared__ __attribute__((aligned(16))) char lds[];
  int b = blockIdx.x, xcd = b & 7, i = b >> 3;
  int cb = i & 7, rb = xcd * 4 + (i >> 3);
  int w = __builtin_amdgcn_readfirstlane(threadIdx.x >> 6), l = threadIdx.x & 63;
  const int c = l & 15, g = l >> 4;
  const int tok = rb * 64 + w * 16 + c;
  auto stage = [&](int ch, char* buf) {
    int st = ch * 4 + w;
    for (int j = 0; j < 5; ++j) {
      int en = j * 64 + l, rho = en / 10, cc = en % 10;
      int gg = (rho & 15) >> 2, jj = (rho & 3) + 4 * (rho >> 4);
      const char* src;
      if (st < 16) { int p = st * 4 + gg; src = wo + (size_t)(20 * (p >> 1) + 8 * (p & 1) + jj) * 1280; }
      else if (st < 20) { src = wo + (size_t)(20 * (8 * (st - 16) + 2 * gg + (jj >> 2)) + 16 + (jj & 3)) * 1280; }
      else src = w2 + (size_t)((st - 20) * 32 + 8 * gg + jj) * 1280;
      glds16(src + cb * 160 + cc * 16, buf + w * 5120 + j * 1024);
    }
  };
  f32x4 acc[5], sv[5];
  for (int t = 0; t < 5; ++t) acc[t] = (f32x4){0, 0, 0, 0};
  stage(0, lds);
  for (int ch = 0; ch < 17; ++ch) {
    bf16x8 a[4];
#pragma unroll
    for (int s = 0; s < 4; ++s) {
      int st = ch * 4 + s;
      if (ch < 4) a[s] = *(const bf16x8*)(ws + WS_OM + ((size_t)(st * 4 + g) * 2048 + tok) * 16);
      else if (ch == 4) {
        int hd = 8 * s + 2 * g;
        u32x2 p = *(const u32x2*)(ws + WS_OR + ((size_t)hd * 2048 + tok) * 8), q = *(const u32x2*)(ws + WS_OR + ((size_t)(hd + 1) * 2048 + tok) * 8);
        a[s] = __builtin_bit_cast(bf16x8, (u32x4){p[0], p[1], q[0], q[1]});
      } else a[s] = *(const bf16x8*)(ws + WS_HP + ((size_t)((st - 20) * 4 + g) * 2048 + tok) * 16);
    }
    __syncthreads();
    const char* buf = lds + (ch & 1) * 20480;
    if (ch + 1 < 17) stage(ch + 1, lds + ((ch + 1) & 1) * 20480);
#pragma unroll
    for (int s = 0; s < 4; ++s)
#pragma unroll
      for (int t = 0; t < 5; ++t) {
        const char* wp = buf + s * 5120 + (4 * g + (c >> 2)) * 160 + t * 32 + (c & 3) * 8;
        s16x4 lo = trread(wp), hi = trread(wp + 2560);
        s16x8 wv = {lo[0], lo[1], lo[2], lo[3], hi[0], hi[1], hi[2], hi[3]};
        acc[t] = __builtin_amdgcn_mfma_f32_16x16x32_bf16(__builtin_bit_cast(bf16x8, wv), a[s], acc[t], 0, 0, 0);
      }
    if (ch == 4) for (int t = 0; t < 5; ++t) { sv[t] = acc[t]; acc[t] = (f32x4){0, 0, 0, 0}; }
  }
  for (int t = 0; t < 5; ++t) {
    size_t off = (size_t)tok * 1280 + cb * 160 + t * 32 + g * 8;
    u32x2 xv = *(const u32x2*)(x + off);
    float o[4];
    for (int j = 0; j < 4; ++j) {
      float gv = acc[t][j];
      float sg = gv / (1.f + __expf(-gv));
      float xx = (j & 1) ? bhi(xv[j >> 1]) : blo(xv[j >> 1]);
      o[j] = xx + sv[t][j] * sg;
    }
    *(u32x2*)(out + off) = (u32x2){pk2(o[0], o[1]), pk2(o[2], o[3])};
  }
  __syncthreads(); stamp(ws, 3, 1);
}

extern "C" void kernel_launch(void* const* d_in, const int* in_sizes, int n_in, void* d_out, int out_size,
                              void* d_ws, size_t ws_size, hipStream_t stream) {
  const char* x = (const char*)d_in[0];
  const char* wqkv = (const char*)d_in[1];
  const char* wo = (const char*)d_in[2];
  const char* w1 = (const char*)d_in[4];
  const char* w2 = (const char*)d_in[5];
  char* ws = (char*)d_ws;
  (void)hipFuncSetAttribute((const void*)k_gemm1, hipFuncAttributeMaxDynamicSharedMemorySize, 2 * 45056);
  k_ln<<<256, 512, 0, stream>>>(x, ws);
  k_gemm1<<<256, 512, 2 * 45056, stream>>>(wqkv, w1, ws);
  k_attn<<<256, 512, 4 * QUAD, stream>>>(ws);
  k_out<<<256, 256, 2 * 20480, stream>>>(x, wo, w2, ws, (char*)d_out);
  k_report<<<1, 64, 0, stream>>>(ws);
}
```

```cpp
#define QSEL 0
#define QMUL 10
#include <hip/hip_runtime.h>
#include <hip/hip_bf16.h>

typedef __attribute__((ext_vector_type(8))) __bf16 bf16x8;
typedef __attribute__((ext_vector_type(16))) __bf16 bf16x16;
typedef __attribute__((ext_vector_type(4))) short s16x4;
typedef __attribute__((ext_vector_type(8))) short s16x8;
typedef __attribute__((ext_vector_type(16))) float f32x16;
typedef __attribute__((ext_vector_type(8))) float f32x8;
typedef __attribute__((ext_vector_type(4))) float f32x4;
typedef __attribute__((ext_vector_type(4))) unsigned u32x4;
typedef __attribute__((ext_vector_type(2))) unsigned u32x2;
#define LAS __attribute__((address_space(3)))
#define GAS __attribute__((address_space(1)))

constexpr int S_ = 2048, D_ = 640, F_ = 1536;
constexpr int QUAD = 10752;
constexpr size_t WS_XN = 0;
constexpr size_t WS_QM = WS_XN + (size_t)S_ * D_ * 2;
constexpr size_t WS_QR = WS_QM + 32 * 64 * 1024;
constexpr size_t WS_KV = WS_QR + 32 * 2048 * 8;
constexpr size_t WS_HP = WS_KV + (size_t)32 * 16 * QUAD;
constexpr size_t WS_OM = WS_HP + (size_t)192 * 2048 * 16;
constexpr size_t WS_OR = WS_OM + (size_t)64 * 2048 * 16;

__device__ __forceinline__ unsigned pk2(float lo, float hi) {
  typedef __attribute__((ext_vector_type(2))) __bf16 bf16x2;
  typedef __attribute__((ext_vector_type(2))) float f32x2;
  f32x2 v = {lo, hi};
  return __builtin_bit_cast(unsigned, __builtin_convertvector(v, bf16x2));
}
__device__ __forceinline__ float blo(unsigned u) { return __uint_as_float(u << 16); }
__device__ __forceinline__ float bhi(unsigned u) { return __uint_as_float(u & 0xffff0000u); }
__device__ __forceinline__ void glds16(const void* g, void* l) {
  __builtin_amdgcn_global_load_lds((const GAS void*)g, (LAS void*)l, 16, 0, 0);
}
__device__ __forceinline__ s16x4 trread(const char* p) {
  return __builtin_amdgcn_ds_read_tr16_b64_v4i16((LAS s16x4*)p);
}

constexpr size_t WS_ST = 64u << 20;
__device__ __forceinline__ void stamp(char* ws, int k, int which) {
  if (threadIdx.x == 0) { unsigned long long* p = (unsigned long long*)(ws + WS_ST) + ((k * 2 + which) * 256 + blockIdx.x) * 2; p[0] = __builtin_amdgcn_s_memrealtime(); p[1] = __builtin_amdgcn_s_memtime(); }
}
__global__ void __launch_bounds__(256) k_report(char* ws) {
  const unsigned long long* p = (const unsigned long long*)(ws + WS_ST);
  __shared__ unsigned long long mn[4], mx[4];
  if (threadIdx.x < 4) { mn[threadIdx.x] = ~0ull; mx[threadIdx.x] = 0; }
  __syncthreads();
  int b = threadIdx.x;
  for (int k = 0; k < 4; ++k) { atomicMin(&mn[k], p[((k*2)*256+b)*2]); atomicMax(&mx[k], p[((k*2+1)*256+b)*2]); }
  __syncthreads();
  long long q = 0;
  if (QSEL == 1) q = mx[0] - mn[0];
  if (QSEL == 2) q = mn[1] - mx[0];
  if (QSEL == 3) q = mx[1] - mn[1];
  if (QSEL == 4) q = mx[2] - mn[2];
  if (QSEL == 5) q = mx[3] - mn[3];
  if (QSEL == 6) q = (p[(5*256+7)*2+1] - p[(4*256+7)*2+1]) * 10 / (p[(5*256+7)*2] - p[(4*256+7)*2]);
  if (QSEL == 7) q = mn[2] - mx[1];
  if (QSEL == 8) q = mn[3] - mx[2];
  if (QSEL == 9) q = mx[3] - mn[0];
  if (QSEL == 10) q = (p[(3*256+7)*2+1] - p[(2*256+7)*2+1]) * 10 / (p[(3*256+7)*2] - p[(2*256+7)*2]);
  if (q > 20000) q = 20000;
  unsigned long long t0 = mn[0];
  int it = 0;
  while ((long long)(__builtin_amdgcn_s_memrealtime() - t0) < 10000 + q * QMUL && ++it < 10000000) __builtin_amdgcn_s_sleep(1);
}
__global__ void __launch_bounds__(512) k_ln(const char* x, char* ws) {
  stamp(ws, 0, 0);
  int w = threadIdx.x >> 6, l = threadIdx.x & 63, row = blockIdx.x * 8 + w;
  u32x4 a = {0, 0, 0, 0}, b = {0, 0, 0, 0};
  if (l < 40) { a = *(const u32x4*)(x + (size_t)row * 1280 + l * 32); b = *(const u32x4*)(x + (size_t)row * 1280 + l * 32 + 16); }
  float v[16];
  for (int i = 0; i < 4; ++i) { v[2*i] = blo(a[i]); v[2*i+1] = bhi(a[i]); v[8+2*i] = blo(b[i]); v[9+2*i] = bhi(b[i]); }
  float s = 0; for (int i = 0; i < 16; ++i) s += v[i];
  for (int o = 32; o; o >>= 1) s += __shfl_xor(s, o);
  float m = s * (1.f / 640);
  float q = 0; if (l < 40) for (int i = 0; i < 16; ++i) q += (v[i] - m) * (v[i] - m);
  for (int o = 32; o; o >>= 1) q += __shfl_xor(q, o);
  float r = rsqrtf(q * (1.f / 640) + 1e-5f);
  if (l < 40) {
    for (int i = 0; i < 4; ++i) { a[i] = pk2((v[2*i] - m) * r, (v[2*i+1] - m) * r); b[i] = pk2((v[8+2*i] - m) * r, (v[9+2*i] - m) * r); }
    *(u32x4*)(ws + WS_XN + (size_t)row * 1280 + l * 32) = a; *(u32x4*)(ws + WS_XN + (size_t)row * 1280 + l * 32 + 16) = b;
  }
  __syncthreads(); stamp(ws, 0, 1);
}

template <bool ISV>
__device__ __forceinline__ void k1_loop(const char* X, const char* W, int wpitch, int ntiles, char* lds, f32x16 (&acc)[4], int w, int l) {
  const int rt = w & 3, ch = w >> 2, r = l & 31, h = l >> 5;
  const int nmine = ch == 0 ? 4 : ntiles - 4;
  auto stage = [&](int c, char* buf) {
    for (int j = 0; j < 2; ++j) {
      int p = w * 2 + j, row = p * 8 + (l >> 3);
      glds16(X + (size_t)row * 1280 + c * 128 + (((l & 7) ^ ((row >> 1) & 7)) << 4), buf + p * 1024);
    }
    if (w < ntiles)
      for (int j = 0; j < 4; ++j)
        glds16(W + (size_t)(c * 64 + j * 16 + (l >> 2)) * wpitch + w * 64 + (l & 3) * 16, buf + 16384 + w * 4096 + j * 1024);
  };
  stage(0, lds);
  for (int c = 0; c < 10; ++c) {
    __syncthreads();
    char* buf = lds + (c & 1) * 45056;
    if (c + 1 < 10) stage(c + 1, lds + ((c + 1) & 1) * 45056);
#pragma unroll
    for (int s = 0; s < 4; ++s) {
      int row = rt * 32 + r;
      bf16x8 xa = *(const bf16x8*)(buf + row * 128 + (((2 * s + h) ^ ((row >> 1) & 7)) << 4));
#pragma unroll
      for (int t = 0; t < 4; ++t) {
        if (t < nmine) {
          const char* wp = buf + 16384 + (ch * 4 + t) * 4096 + (s * 16 + 8 * h + ((l & 15) >> 2)) * 64 + ((l >> 4) & 1) * 32 + (l & 3) * 8;
          s16x4 lo = trread(wp), hi = trread(wp + 256);
          s16x8 wv = {lo[0], lo[1], lo[2], lo[3], hi[0], hi[1], hi[2], hi[3]};
          bf16x8 wb = __builtin_bit_cast(bf16x8, wv);
          if (ISV) acc[t] = __builtin_amdgcn_mfma_f32_32x32x16_bf16(xa, wb, acc[t], 0, 0, 0);
          else     acc[t] = __builtin_amdgcn_mfma_f32_32x32x16_bf16(wb, xa, acc[t], 0, 0, 0);
        }
      }
    }
  }
}

__global__ void __launch_bounds__(512) k_gemm1(const char* wqkv, const char* w1, char* ws) {
  stamp(ws, 1, 0);
  extern __shared__ __attribute__((aligned(16))) char lds[];
  int b = blockIdx.x, xcd = b & 7, i = b >> 3;
  int cb = (xcd & 3) * 4 + (i & 3), rb = (xcd >> 2) * 8 + (i >> 2);
  int w = __builtin_amdgcn_readfirstlane(threadIdx.x >> 6), l = threadIdx.x & 63;
  int kind, t0, nt;
  if (cb < 9) { kind = cb / 3; int j = cb % 3; t0 = j * 7; nt = j == 2 ? 6 : 7; }
  else { kind = 3; int j = cb - 9; t0 = j * 7; nt = j == 6 ? 6 : 7; }
  const char* W = kind < 3 ? wqkv + (kind * 20 + t0) * 64 : w1 + t0 * 64;
  int wpitch = kind < 3 ? 3840 : 3072;
  const char* X = ws + WS_XN + (size_t)rb * 128 * 1280;
  f32x16 acc[4];
  for (int t = 0; t < 4; ++t) for (int j = 0; j < 16; ++j) acc[t][j] = 0.f;
  if (kind == 2) k1_loop<true>(X, W, wpitch, nt, lds, acc, w, l);
  else k1_loop<false>(X, W, wpitch, nt, lds, acc, w, l);
  const int rt = w & 3, ch = w >> 2, r = l & 31, h = l >> 5;
  const int nmine = ch == 0 ? 4 : nt - 4;
  const int T = rb * 4 + rt;
  const float qs = 0.22360679774997896f * 1.4426950408889634f;
#pragma unroll
  for (int t = 0; t < 4; ++t) {
    if (t >= nmine) continue;
    int ct = t0 + ch * 4 + t;
#pragma unroll
    for (int gi = 0; gi < 4; ++gi) {
      float a0 = acc[t][4*gi], a1 = acc[t][4*gi+1], a2 = acc[t][4*gi+2], a3 = acc[t][4*gi+3];
      char* dst;
      if (kind == 0) {
        a0 *= qs; a1 *= qs; a2 *= qs; a3 *= qs;
        int G = ct * 8 + 2 * gi + h, head = G / 5, sub = G % 5;
        dst = sub < 4 ? ws + WS_QM + ((size_t)(head * 64 + T) * 64 + (sub >> 1) * 32 + r) * 16 + (sub & 1) * 8
                      : ws + WS_QR + ((size_t)head * 2048 + T * 32 + r) * 8;
      } else if (kind == 1) {
        int G = ct * 8 + 2 * gi + h, head = G / 5, sub = G % 5;
        char* base = ws + WS_KV + (size_t)(head * 16 + (T >> 2)) * QUAD;
        dst = sub < 4 ? base + ((T & 3) * 64 + (sub >> 1) * 32 + r) * 16 + (sub & 1) * 8
                      : base + 4096 + ((T & 1) * 32 + r) * 16 + ((T >> 1) & 1) * 8;
      } else if (kind == 2) {
        int n = ct * 32 + r, head = n / 20, d = n % 20;
        char* base = ws + WS_KV + (size_t)(head * 16 + (T >> 2)) * QUAD + 5120 + ((T & 3) * 4 + gi) * 352;
        dst = base + d * 16 + h * 8;
        if (d == 0) { unsigned c = h == 0 ? 0x3f803f80u : 0u; *(u32x4*)(base + 320 + h * 16) = (u32x4){c, c, c, c}; }
      } else {
        a0 = fmaxf(a0, 0.f); a1 = fmaxf(a1, 0.f); a2 = fmaxf(a2, 0.f); a3 = fmaxf(a3, 0.f);
        dst = ws + WS_HP + ((size_t)(ct * 4 + gi) * 2048 + T * 32 + r) * 16 + h * 8;
      }
      *(u32x2*)dst = (u32x2){pk2(a0, a1), pk2(a2, a3)};
    }
  }
  __syncthreads(); stamp(ws, 1, 1);
}

__device__ __forceinline__ f32x16 qk(bf16x8 km, s16x4 kr, bf16x8 qm, s16x4 qr) {
  f32x16 z; for (int i = 0; i < 16; ++i) z[i] = 0.f;
  f32x16 s = __builtin_amdgcn_mfma_f32_32x32x16_bf16(km, qm, z, 0, 0, 0);
  return __builtin_amdgcn_mfma_f32_32x32x8bf16_1k(kr, qr, s, 0, 0, 0);
}
__device__ __forceinline__ f32x16 pv(bf16x8 v0, bf16x8 v1, f32x16 p, f32x16 o) {
  f32x8 lo = __builtin_shufflevector(p, p, 0, 1, 2, 3, 4, 5, 6, 7), hi = __builtin_shufflevector(p, p, 8, 9, 10, 11, 12, 13, 14, 15);
  o = __builtin_amdgcn_smfmac_f32_32x32x32_bf16(v0, __builtin_bit_cast(bf16x16, lo), o, (int)0xDDDDDDDD, 0, 0);
  return __builtin_amdgcn_smfmac_f32_32x32x32_bf16(v1, __builtin_bit_cast(bf16x16, hi), o, (int)0xDDDDDDDD, 0, 0);
}

__device__ __forceinline__ void attn_safe(const char* kv, bf16x8 qm, s16x4 qa, s16x4 qb, int l, f32x16& o) {
  const int r = l & 31, h = l >> 5, e = r < 21 ? r : 21;
  float m = -INFINITY;
  for (int i = 0; i < 16; ++i) o[i] = 0.f;
  for (int t = 0; t < 64; ++t) {
    const char* base = kv + (t >> 2) * QUAD; int tq = t & 3;
    bf16x8 km = *(const bf16x8*)(base + tq * 1024 + l * 16);
    s16x4 kr = *(const s16x4*)(base + 4096 + l * 16 + (tq >> 1) * 8);
    bf16x8 v0 = *(const bf16x8*)(base + 5120 + (tq * 4 + h) * 352 + e * 16), v1 = *(const bf16x8*)(base + 5120 + (tq * 4 + 2 + h) * 352 + e * 16);
    f32x16 s = qk(km, kr, qm, (tq & 1) ? qb : qa);
    float mx = s[0]; for (int i = 1; i < 16; ++i) mx = fmaxf(mx, s[i]);
    mx = fmaxf(mx, __shfl_xor(mx, 32));
    float mn = fmaxf(m, mx), al = __builtin_amdgcn_exp2f(m - mn);
    m = mn;
    for (int i = 0; i < 16; ++i) { s[i] = __builtin_amdgcn_exp2f(s[i] - mn); o[i] *= al; }
    o = pv(v0, v1, s, o);
  }
}

__global__ void __launch_bounds__(512) k_attn(char* ws) {
  stamp(ws, 2, 0);
  extern __shared__ __attribute__((aligned(16))) char lds[];
  int b = blockIdx.x, xcd = b & 7, i = b >> 3;
  int head = xcd * 4 + (i & 3), qb = i >> 2;
  int w = __builtin_amdgcn_readfirstlane(threadIdx.x >> 6), l = threadIdx.x & 63;
  const int g = w & 3, par = w >> 2, r = l & 31, h = l >> 5, e = r < 21 ? r : 21;
  const char* kv = ws + WS_KV + (size_t)head * 16 * QUAD;
  auto stage = [&](int s, char* buf) {
    if (w < 7) for (int j = 0; j < 3; ++j) glds16(kv + (size_t)s * 2 * QUAD + (w * 3 + j) * 1024 + l * 16, buf + (w * 3 + j) * 1024);
  };
  stage(0, lds);
  bf16x8 qm[2]; s16x4 qa[2], qbb[2];
  for (int t = 0; t < 2; ++t) {
    int T = qb * 8 + g * 2 + t;
    qm[t] = *(const bf16x8*)(ws + WS_QM + ((size_t)(head * 64 + T) * 64 + l) * 16);
    s16x4 q = *(const s16x4*)(ws + WS_QR + ((size_t)head * 2048 + T * 32 + r) * 8);
    s16x4 z = {0, 0, 0, 0};
    qa[t] = h == 0 ? q : z; qbb[t] = h == 1 ? q : z;
  }
  f32x16 o[2];
  for (int t = 0; t < 2; ++t) for (int j = 0; j < 16; ++j) o[t][j] = 0.f;
  for (int s = 0; s < 8; ++s) {
    __syncthreads();
    const char* base = lds + (s & 1) * 2 * QUAD + par * QUAD;
    if (s + 1 < 8) stage(s + 1, lds + ((s + 1) & 1) * 2 * QUAD);
    s16x8 kr4 = *(const s16x8*)(base + 4096 + l * 16);
#pragma unroll
    for (int tq = 0; tq < 4; ++tq) {
      bf16x8 km = *(const bf16x8*)(base + tq * 1024 + l * 16);
      s16x4 kr = tq < 2 ? __builtin_shufflevector(kr4, kr4, 0, 1, 2, 3) : __builtin_shufflevector(kr4, kr4, 4, 5, 6, 7);
      bf16x8 v0 = *(const bf16x8*)(base + 5120 + (tq * 4 + h) * 352 + e * 16), v1 = *(const bf16x8*)(base + 5120 + (tq * 4 + 2 + h) * 352 + e * 16);
#pragma unroll
      for (int t = 0; t < 2; ++t) {
        f32x16 p = qk(km, kr, qm[t], (tq & 1) ? qbb[t] : qa[t]);
        for (int j = 0; j < 16; ++j) p[j] = __builtin_amdgcn_exp2f(p[j]);
        o[t] = pv(v0, v1, p, o[t]);
      }
    }
  }
  __syncthreads();
  f32x16 give = par ? o[0] : o[1], keep = par ? o[1] : o[0];
  f32x4* xb = (f32x4*)lds;
  for (int j = 0; j < 3; ++j) xb[(w * 3 + j) * 64 + l] = (f32x4){give[4*j], give[4*j+1], give[4*j+2], give[4*j+3]};
  __syncthreads();
  for (int j = 0; j < 3; ++j) { f32x4 v = xb[((w ^ 4) * 3 + j) * 64 + l]; for (int k = 0; k < 4; ++k) keep[4*j+k] += v[k]; }
  float lsum = __shfl(keep[8], r + 32);
  bool ok = lsum >= 0x1p-30f && lsum <= 0x1p60f;
  for (int j = 0; j < 12; ++j) ok = ok && (fabsf(keep[j]) <= 0x1p120f);
  if (__any(!ok)) {
    attn_safe(kv, par ? qm[1] : qm[0], par ? qa[1] : qa[0], par ? qbb[1] : qbb[0], l, keep);
    lsum = __shfl(keep[8], r + 32);
  }
  float inv = 1.f / lsum;
  int tok = (qb * 8 + g * 2 + par) * 32 + r;
  for (int j = 0; j < 2; ++j)
    *(u32x2*)(ws + WS_OM + ((size_t)(head * 2 + j) * 2048 + tok) * 16 + h * 8) = (u32x2){pk2(keep[4*j] * inv, keep[4*j+1] * inv), pk2(keep[4*j+2] * inv, keep[4*j+3] * inv)};
  if (h == 0) *(u32x2*)(ws + WS_OR + ((size_t)head * 2048 + tok) * 8) = (u32x2){pk2(keep[8] * inv, keep[9] * inv), pk2(keep[10] * inv, keep[11] * inv)};
  __syncthreads(); stamp(ws, 2, 1);
}

__global__ void __launch_bounds__(256) k_out(const char* x, const char* wo, const char* w2, char* ws, char* out) {
  stamp(ws, 3, 0);
  extern __shared__ __attribute__((aligned(16))) char lds[];
  int b = blockIdx.x, xcd = b & 7, i = b >> 3;
  int cb = i & 7, rb = xcd * 4 + (i >> 3);
  int w = __builtin_amdgcn_readfirstlane(threadIdx.x >> 6), l = threadIdx.x & 63;
  const int c = l & 15, g = l >> 4;
  const int tok = rb * 64 + w * 16 + c;
  auto stage = [&](int ch, char* buf) {
    int st = ch * 4 + w;
    for (int j = 0; j < 5; ++j) {
      int en = j * 64 + l, rho = en / 10, cc = en % 10;
      int gg = (rho & 15) >> 2, jj = (rho & 3) + 4 * (rho >> 4);
      const char* src;
      if (st < 16) { int p = st * 4 + gg; src = wo + (size_t)(20 * (p >> 1) + 8 * (p & 1) + jj) * 1280; }
      else if (st < 20) { src = wo + (size_t)(20 * (8 * (st - 16) + 2 * gg + (jj >> 2)) + 16 + (jj & 3)) * 1280; }
      else src = w2 + (size_t)((st - 20) * 32 + 8 * gg + jj) * 1280;
      glds16(src + cb * 160 + cc * 16, buf + w * 5120 + j * 1024);
    }
  };
  f32x4 acc[5], sv[5];
  for (int t = 0; t < 5; ++t) acc[t] = (f32x4){0, 0, 0, 0};
  stage(0, lds);
  for (int ch = 0; ch < 17; ++ch) {
    bf16x8 a[4];
#pragma unroll
    for (int s = 0; s < 4; ++s) {
      int st = ch * 4 + s;
      if (ch < 4) a[s] = *(const bf16x8*)(ws + WS_OM + ((size_t)(st * 4 + g) * 2048 + tok) * 16);
      else if (ch == 4) {
        int hd = 8 * s + 2 * g;
        u32x2 p = *(const u32x2*)(ws + WS_OR + ((size_t)hd * 2048 + tok) * 8), q = *(const u32x2*)(ws + WS_OR + ((size_t)(hd + 1) * 2048 + tok) * 8);
        a[s] = __builtin_bit_cast(bf16x8, (u32x4){p[0], p[1], q[0], q[1]});
      } else a[s] = *(const bf16x8*)(ws + WS_HP + ((size_t)((st - 20) * 4 + g) * 2048 + tok) * 16);
    }
    __syncthreads();
    const char* buf = lds + (ch & 1) * 20480;
    if (ch + 1 < 17) stage(ch + 1, lds + ((ch + 1) & 1) * 20480);
#pragma unroll
    for (int s = 0; s < 4; ++s)
#pragma unroll
      for (int t = 0; t < 5; ++t) {
        const char* wp = buf + s * 5120 + (4 * g + (c >> 2)) * 160 + t * 32 + (c & 3) * 8;
        s16x4 lo = trread(wp), hi = trread(wp + 2560);
        s16x8 wv = {lo[0], lo[1], lo[2], lo[3], hi[0], hi[1], hi[2], hi[3]};
        acc[t] = __builtin_amdgcn_mfma_f32_16x16x32_bf16(__builtin_bit_cast(bf16x8, wv), a[s], acc[t], 0, 0, 0);
      }
    if (ch == 4) for (int t = 0; t < 5; ++t) { sv[t] = acc[t]; acc[t] = (f32x4){0, 0, 0, 0}; }
  }
  for (int t = 0; t < 5; ++t) {
    size_t off = (size_t)tok * 1280 + cb * 160 + t * 32 + g * 8;
    u32x2 xv = *(const u32x2*)(x + off);
    float o[4];
    for (int j = 0; j < 4; ++j) {
      float gv = acc[t][j];
      float sg = gv / (1.f + __expf(-gv));
      float xx = (j & 1) ? bhi(xv[j >> 1]) : blo(xv[j >> 1]);
      o[j] = xx + sv[t][j] * sg;
    }
    *(u32x2*)(out + off) = (u32x2){pk2(o[0], o[1]), pk2(o[2], o[3])};
  }
  __syncthreads(); stamp(ws, 3, 1);
}

extern "C" void kernel_launch(void* const* d_in, const int* in_sizes, int n_in, void* d_out, int out_size,
                              void* d_ws, size_t ws_size, hipStream_t stream) {
  const char* x = (const char*)d_in[0];
  const char* wqkv = (const char*)d_in[1];
  const char* wo = (const char*)d_in[2];
  const char* w1 = (const char*)d_in[4];
  const char* w2 = (const char*)d_in[5];
  char* ws = (char*)d_ws;
  (void)hipFuncSetAttribute((const void*)k_gemm1, hipFuncAttributeMaxDynamicSharedMemorySize, 2 * 45056);
  k_ln<<<256, 512, 0, stream>>>(x, ws);
  k_gemm1<<<256, 512, 2 * 45056, stream>>>(wqkv, w1, ws);
  k_attn<<<256, 512, 4 * QUAD, stream>>>(ws);
  k_out<<<256, 256, 2 * 20480, stream>>>(x, wo, w2, ws, (char*)d_out);
  k_report<<<1, 256, 0, stream>>>(ws);
}
```

```cpp
#define WINDOW 20000
#include <hip/hip_runtime.h>
#include <hip/hip_bf16.h>

typedef __attribute__((ext_vector_type(8))) __bf16 bf16x8;
typedef __attribute__((ext_vector_type(16))) __bf16 bf16x16;
typedef __attribute__((ext_vector_type(4))) short s16x4;
typedef __attribute__((ext_vector_type(8))) short s16x8;
typedef __attribute__((ext_vector_type(16))) float f32x16;
typedef __attribute__((ext_vector_type(8))) float f32x8;
typedef __attribute__((ext_vector_type(4))) float f32x4;
typedef __attribute__((ext_vector_type(4))) unsigned u32x4;
typedef __attribute__((ext_vector_type(2))) unsigned u32x2;
#define LAS __attribute__((address_space(3)))
#define GAS __attribute__((address_space(1)))

constexpr int S_ = 2048, D_ = 640, F_ = 1536;
constexpr int QUAD = 10752;
constexpr size_t WS_XN = 0;
constexpr size_t WS_QM = WS_XN + (size_t)S_ * D_ * 2;
constexpr size_t WS_QR = WS_QM + 32 * 64 * 1024;
constexpr size_t WS_KV = WS_QR + 32 * 2048 * 8;
constexpr size_t WS_HP = WS_KV + (size_t)32 * 16 * QUAD;
constexpr size_t WS_OM = WS_HP + (size_t)192 * 2048 * 16;
constexpr size_t WS_OR = WS_OM + (size_t)64 * 2048 * 16;

__device__ __forceinline__ unsigned pk2(float lo, float hi) {
  typedef __attribute__((ext_vector_type(2))) __bf16 bf16x2;
  typedef __attribute__((ext_vector_type(2))) float f32x2;
  f32x2 v = {lo, hi};
  return __builtin_bit_cast(unsigned, __builtin_convertvector(v, bf16x2));
}
__device__ __forceinline__ float blo(unsigned u) { return __uint_as_float(u << 16); }
__device__ __forceinline__ float bhi(unsigned u) { return __uint_as_float(u & 0xffff0000u); }
__device__ __forceinline__ void glds16(const void* g, void* l) {
  __builtin_amdgcn_global_load_lds((const GAS void*)g, (LAS void*)l, 16, 0, 0);
}
__device__ __forceinline__ s16x4 trread(const char* p) {
  return __builtin_amdgcn_ds_read_tr16_b64_v4i16((LAS s16x4*)p);
}

__device__ __forceinline__ void k_ln(const char* x, char* ws) {
  int w = threadIdx.x >> 6, l = threadIdx.x & 63, row = blockIdx.x * 8 + w;
  u32x4 a = {0, 0, 0, 0}, b = {0, 0, 0, 0};
  if (l < 40) { a = *(const u32x4*)(x + (size_t)row * 1280 + l * 32); b = *(const u32x4*)(x + (size_t)row * 1280 + l * 32 + 16); }
  float v[16];
  for (int i = 0; i < 4; ++i) { v[2*i] = blo(a[i]); v[2*i+1] = bhi(a[i]); v[8+2*i] = blo(b[i]); v[9+2*i] = bhi(b[i]); }
  float s = 0; for (int i = 0; i < 16; ++i) s += v[i];
  for (int o = 32; o; o >>= 1) s += __shfl_xor(s, o);
  float m = s * (1.f / 640);
  float q = 0; if (l < 40) for (int i = 0; i < 16; ++i) q += (v[i] - m) * (v[i] - m);
  for (int o = 32; o; o >>= 1) q += __shfl_xor(q, o);
  float r = rsqrtf(q * (1.f / 640) + 1e-5f);
  if (l < 40) {
    for (int i = 0; i < 4; ++i) { a[i] = pk2((v[2*i] - m) * r, (v[2*i+1] - m) * r); b[i] = pk2((v[8+2*i] - m) * r, (v[9+2*i] - m) * r); }
    *(u32x4*)(ws + WS_XN + (size_t)row * 1280 + l * 32) = a; *(u32x4*)(ws + WS_XN + (size_t)row * 1280 + l * 32 + 16) = b;
  }
}

template <bool ISV>
__device__ __forceinline__ void k1_loop(const char* X, const char* W, int wpitch, int ntiles, char* lds, f32x16 (&acc)[4], int w, int l) {
  const int rt = w & 3, ch = w >> 2, r = l & 31, h = l >> 5;
  const int nmine = ch == 0 ? 4 : ntiles - 4;
  auto stage = [&](int c, char* buf) {
    for (int j = 0; j < 2; ++j) {
      int p = w * 2 + j, row = p * 8 + (l >> 3);
      glds16(X + (size_t)row * 1280 + c * 128 + (((l & 7) ^ ((row >> 1) & 7)) << 4), buf + p * 1024);
    }
    if (w < ntiles)
      for (int j = 0; j < 4; ++j)
        glds16(W + (size_t)(c * 64 + j * 16 + (l >> 2)) * wpitch + w * 64 + (l & 3) * 16, buf + 16384 + w * 4096 + j * 1024);
  };
  stage(0, lds);
  for (int c = 0; c < 10; ++c) {
    asm volatile("s_waitcnt vmcnt(0)" ::: "memory"); __syncthreads();
    char* buf = lds + (c & 1) * 45056;
    if (c + 1 < 10) stage(c + 1, lds + ((c + 1) & 1) * 45056);
#pragma unroll
    for (int s = 0; s < 4; ++s) {
      int row = rt * 32 + r;
      bf16x8 xa = *(const bf16x8*)(buf + row * 128 + (((2 * s + h) ^ ((row >> 1) & 7)) << 4));
#pragma unroll
      for (int t = 0; t < 4; ++t) {
        if (t < nmine) {
          const char* wp = buf + 16384 + (ch * 4 + t) * 4096 + (s * 16 + 8 * h + ((l & 15) >> 2)) * 64 + ((l >> 4) & 1) * 32 + (l & 3) * 8;
          s16x4 lo = trread(wp), hi = trread(wp + 256);
          s16x8 wv = {lo[0], lo[1], lo[2], lo[3], hi[0], hi[1], hi[2], hi[3]};
          bf16x8 wb = __builtin_bit_cast(bf16x8, wv);
          if (ISV) acc[t] = __builtin_amdgcn_mfma_f32_32x32x16_bf16(xa, wb, acc[t], 0, 0, 0);
          else     acc[t] = __builtin_amdgcn_mfma_f32_32x32x16_bf16(wb, xa, acc[t], 0, 0, 0);
        }
      }
    }
  }
}

__device__ __forceinline__ void k_gemm1(const char* wqkv, const char* w1, char* ws) {
  extern __shared__ __attribute__((aligned(16))) char lds[];
  int b = blockIdx.x, xcd = b & 7, i = b >> 3;
  int cb = (xcd & 3) * 4 + (i & 3), rb = (xcd >> 2) * 8 + (i >> 2);
  int w = __builtin_amdgcn_readfirstlane(threadIdx.x >> 6), l = threadIdx.x & 63;
  int kind, t0, nt;
  if (cb < 9) { kind = cb / 3; int j = cb % 3; t0 = j * 7; nt = j == 2 ? 6 : 7; }
  else { kind = 3; int j = cb - 9; t0 = j * 7; nt = j == 6 ? 6 : 7; }
  const char* W = kind < 3 ? wqkv + (kind * 20 + t0) * 64 : w1 + t0 * 64;
  int wpitch = kind < 3 ? 3840 : 3072;
  const char* X = ws + WS_XN + (size_t)rb * 128 * 1280;
  f32x16 acc[4];
  for (int t = 0; t < 4; ++t) for (int j = 0; j < 16; ++j) acc[t][j] = 0.f;
  if (kind == 2) k1_loop<true>(X, W, wpitch, nt, lds, acc, w, l);
  else k1_loop<false>(X, W, wpitch, nt, lds, acc, w, l);
  const int rt = w & 3, ch = w >> 2, r = l & 31, h = l >> 5;
  const int nmine = ch == 0 ? 4 : nt - 4;
  const int T = rb * 4 + rt;
  const float qs = 0.22360679774997896f * 1.4426950408889634f;
#pragma unroll
  for (int t = 0; t < 4; ++t) {
    if (t >= nmine) continue;
    int ct = t0 + ch * 4 + t;
#pragma unroll
    for (int gi = 0; gi < 4; ++gi) {
      float a0 = acc[t][4*gi], a1 = acc[t][4*gi+1], a2 = acc[t][4*gi+2], a3 = acc[t][4*gi+3];
      char* dst;
      if (kind == 0) {
        a0 *= qs; a1 *= qs; a2 *= qs; a3 *= qs;
        int G = ct * 8 + 2 * gi + h, head = G / 5, sub = G % 5;
        dst = sub < 4 ? ws + WS_QM + ((size_t)(head * 64 + T) * 64 + (sub >> 1) * 32 + r) * 16 + (sub & 1) * 8
                      : ws + WS_QR + ((size_t)head * 2048 + T * 32 + r) * 8;
      } else if (kind == 1) {
        int G = ct * 8 + 2 * gi + h, head = G / 5, sub = G % 5;
        char* base = ws + WS_KV + (size_t)(head * 16 + (T >> 2)) * QUAD;
        dst = sub < 4 ? base + ((T & 3) * 64 + (sub >> 1) * 32 + r) * 16 + (sub & 1) * 8
                      : base + 4096 + ((T & 1) * 32 + r) * 16 + ((T >> 1) & 1) * 8;
      } else if (kind == 2) {
        int n = ct * 32 + r, head = n / 20, d = n % 20;
        char* base = ws + WS_KV + (size_t)(head * 16 + (T >> 2)) * QUAD + 5120 + ((T & 3) * 4 + gi) * 352;
        dst = base + d * 16 + h * 8;
        if (d == 0) { unsigned c = h == 0 ? 0x3f803f80u : 0u; *(u32x4*)(base + 320 + h * 16) = (u32x4){c, c, c, c}; }
      } else {
        a0 = fmaxf(a0, 0.f); a1 = fmaxf(a1, 0.f); a2 = fmaxf(a2, 0.f); a3 = fmaxf(a3, 0.f);
        dst = ws + WS_HP + ((size_t)(ct * 4 + gi) * 2048 + T * 32 + r) * 16 + h * 8;
      }
      *(u32x2*)dst = (u32x2){pk2(a0, a1), pk2(a2, a3)};
    }
  }
}

__device__ __forceinline__ f32x16 qk(bf16x8 km, s16x4 kr, bf16x8 qm, s16x4 qr) {
  f32x16 z; for (int i = 0; i < 16; ++i) z[i] = 0.f;
  f32x16 s = __builtin_amdgcn_mfma_f32_32x32x16_bf16(km, qm, z, 0, 0, 0);
  return __builtin_amdgcn_mfma_f32_32x32x8bf16_1k(kr, qr, s, 0, 0, 0);
}
__device__ __forceinline__ f32x16 pv(bf16x8 v0, bf16x8 v1, f32x16 p, f32x16 o) {
  f32x8 lo = __builtin_shufflevector(p, p, 0, 1, 2, 3, 4, 5, 6, 7), hi = __builtin_shufflevector(p, p, 8, 9, 10, 11, 12, 13, 14, 15);
  o = __builtin_amdgcn_smfmac_f32_32x32x32_bf16(v0, __builtin_bit_cast(bf16x16, lo), o, (int)0xDDDDDDDD, 0, 0);
  return __builtin_amdgcn_smfmac_f32_32x32x32_bf16(v1, __builtin_bit_cast(bf16x16, hi), o, (int)0xDDDDDDDD, 0, 0);
}

__device__ __forceinline__ void attn_safe(const char* kv, bf16x8 qm, s16x4 qa, s16x4 qb, int l, f32x16& o) {
  const int r = l & 31, h = l >> 5, e = r < 21 ? r : 21;
  float m = -INFINITY;
  for (int i = 0; i < 16; ++i) o[i] = 0.f;
  for (int t = 0; t < 64; ++t) {
    const char* base = kv + (t >> 2) * QUAD; int tq = t & 3;
    bf16x8 km = *(const bf16x8*)(base + tq * 1024 + l * 16);
    s16x4 kr = *(const s16x4*)(base + 4096 + l * 16 + (tq >> 1) * 8);
    bf16x8 v0 = *(const bf16x8*)(base + 5120 + (tq * 4 + h) * 352 + e * 16), v1 = *(const bf16x8*)(base + 5120 + (tq * 4 + 2 + h) * 352 + e * 16);
    f32x16 s = qk(km, kr, qm, (tq & 1) ? qb : qa);
    float mx = s[0]; for (int i = 1; i < 16; ++i) mx = fmaxf(mx, s[i]);
    mx = fmaxf(mx, __shfl_xor(mx, 32));
    float mn = fmaxf(m, mx), al = __builtin_amdgcn_exp2f(m - mn);
    m = mn;
    for (int i = 0; i < 16; ++i) { s[i] = __builtin_amdgcn_exp2f(s[i] - mn); o[i] *= al; }
    o = pv(v0, v1, s, o);
  }
}

__device__ __forceinline__ void k_attn(char* ws) {
  extern __shared__ __attribute__((aligned(16))) char lds[];
  int b = blockIdx.x, xcd = b & 7, i = b >> 3;
  int head = xcd * 4 + (i & 3), qb = i >> 2;
  int w = __builtin_amdgcn_readfirstlane(threadIdx.x >> 6), l = threadIdx.x & 63;
  const int g = w & 3, par = w >> 2, r = l & 31, h = l >> 5, e = r < 21 ? r : 21;
  const char* kv = ws + WS_KV + (size_t)head * 16 * QUAD;
  auto stage = [&](int s, char* buf) {
    if (w < 7) for (int j = 0; j < 3; ++j) glds16(kv + (size_t)s * 2 * QUAD + (w * 3 + j) * 1024 + l * 16, buf + (w * 3 + j) * 1024);
  };
  stage(0, lds);
  bf16x8 qm[2]; s16x4 qa[2], qbb[2];
  for (int t = 0; t < 2; ++t) {
    int T = qb * 8 + g * 2 + t;
    qm[t] = *(const bf16x8*)(ws + WS_QM + ((size_t)(head * 64 + T) * 64 + l) * 16);
    s16x4 q = *(const s16x4*)(ws + WS_QR + ((size_t)head * 2048 + T * 32 + r) * 8);
    s16x4 z = {0, 0, 0, 0};
    qa[t] = h == 0 ? q : z; qbb[t] = h == 1 ? q : z;
  }
  f32x16 o[2];
  for (int t = 0; t < 2; ++t) for (int j = 0; j < 16; ++j) o[t][j] = 0.f;
  for (int s = 0; s < 8; ++s) {
    asm volatile("s_waitcnt vmcnt(0)" ::: "memory"); __syncthreads();
    const char* base = lds + (s & 1) * 2 * QUAD + par * QUAD;
    if (s + 1 < 8) stage(s + 1, lds + ((s + 1) & 1) * 2 * QUAD);
    s16x8 kr4 = *(const s16x8*)(base + 4096 + l * 16);
#pragma unroll
    for (int tq = 0; tq < 4; ++tq) {
      bf16x8 km = *(const bf16x8*)(base + tq * 1024 + l * 16);
      s16x4 kr = tq < 2 ? __builtin_shufflevector(kr4, kr4, 0, 1, 2, 3) : __builtin_shufflevector(kr4, kr4, 4, 5, 6, 7);
      bf16x8 v0 = *(const bf16x8*)(base + 5120 + (tq * 4 + h) * 352 + e * 16), v1 = *(const bf16x8*)(base + 5120 + (tq * 4 + 2 + h) * 352 + e * 16);
#pragma unroll
      for (int t = 0; t < 2; ++t) {
        f32x16 p = qk(km, kr, qm[t], (tq & 1) ? qbb[t] : qa[t]);
        for (int j = 0; j < 16; ++j) p[j] = __builtin_amdgcn_exp2f(p[j]);
        o[t] = pv(v0, v1, p, o[t]);
      }
    }
  }
  __syncthreads();
  f32x16 give = par ? o[0] : o[1], keep = par ? o[1] : o[0];
  f32x4* xb = (f32x4*)lds;
  for (int j = 0; j < 3; ++j) xb[(w * 3 + j) * 64 + l] = (f32x4){give[4*j], give[4*j+1], give[4*j+2], give[4*j+3]};
  __syncthreads();
  for (int j = 0; j < 3; ++j) { f32x4 v = xb[((w ^ 4) * 3 + j) * 64 + l]; for (int k = 0; k < 4; ++k) keep[4*j+k] += v[k]; }
  float lsum = __shfl(keep[8], r + 32);
  bool ok = lsum >= 0x1p-30f && lsum <= 0x1p60f;
  for (int j = 0; j < 12; ++j) ok = ok && (fabsf(keep[j]) <= 0x1p120f);
  if (__any(!ok)) {
    attn_safe(kv, par ? qm[1] : qm[0], par ? qa[1] : qa[0], par ? qbb[1] : qbb[0], l, keep);
    lsum = __shfl(keep[8], r + 32);
  }
  float inv = 1.f / lsum;
  int tok = (qb * 8 + g * 2 + par) * 32 + r;
  for (int j = 0; j < 2; ++j)
    *(u32x2*)(ws + WS_OM + ((size_t)(head * 2 + j) * 2048 + tok) * 16 + h * 8) = (u32x2){pk2(keep[4*j] * inv, keep[4*j+1] * inv), pk2(keep[4*j+2] * inv, keep[4*j+3] * inv)};
  if (h == 0) *(u32x2*)(ws + WS_OR + ((size_t)head * 2048 + tok) * 8) = (u32x2){pk2(keep[8] * inv, keep[9] * inv), pk2(keep[10] * inv, keep[11] * inv)};
}

__device__ __forceinline__ void k_out(const char* x, const char* wo, const char* w2, char* ws, char* out) {
  extern __shared__ __attribute__((aligned(16))) char lds[];
  int b = blockIdx.x, xcd = b & 7, i = b >> 3;
  int cb = i & 7, rb = xcd * 4 + (i >> 3);
  int w = __builtin_amdgcn_readfirstlane(threadIdx.x >> 6), l = threadIdx.x & 63;
  if (w >= 4) { for (int ch = 0; ch < 17; ++ch) __syncthreads(); return; }
  const int c = l & 15, g = l >> 4;
  const int tok = rb * 64 + w * 16 + c;
  auto stage = [&](int ch, char* buf) {
    int st = ch * 4 + w;
    for (int j = 0; j < 5; ++j) {
      int en = j * 64 + l, rho = en / 10, cc = en % 10;
      int gg = (rho & 15) >> 2, jj = (rho & 3) + 4 * (rho >> 4);
      const char* src;
      if (st < 16) { int p = st * 4 + gg; src = wo + (size_t)(20 * (p >> 1) + 8 * (p & 1) + jj) * 1280; }
      else if (st < 20) { src = wo + (size_t)(20 * (8 * (st - 16) + 2 * gg + (jj >> 2)) + 16 + (jj & 3)) * 1280; }
      else src = w2 + (size_t)((st - 20) * 32 + 8 * gg + jj) * 1280;
      glds16(src + cb * 160 + cc * 16, buf + w * 5120 + j * 1024);
    }
  };
  f32x4 acc[5], sv[5];
  for (int t = 0; t < 5; ++t) acc[t] = (f32x4){0, 0, 0, 0};
  stage(0, lds);
  for (int ch = 0; ch < 17; ++ch) {
    bf16x8 a[4];
#pragma unroll
    for (int s = 0; s < 4; ++s) {
      int st = ch * 4 + s;
      if (ch < 4) a[s] = *(const bf16x8*)(ws + WS_OM + ((size_t)(st * 4 + g) * 2048 + tok) * 16);
      else if (ch == 4) {
        int hd = 8 * s + 2 * g;
        u32x2 p = *(const u32x2*)(ws + WS_OR + ((size_t)hd * 2048 + tok) * 8), q = *(const u32x2*)(ws + WS_OR + ((size_t)(hd + 1) * 2048 + tok) * 8);
        a[s] = __builtin_bit_cast(bf16x8, (u32x4){p[0], p[1], q[0], q[1]});
      } else a[s] = *(const bf16x8*)(ws + WS_HP + ((size_t)((st - 20) * 4 + g) * 2048 + tok) * 16);
    }
    asm volatile("s_waitcnt vmcnt(0)" ::: "memory"); __syncthreads();
    const char* buf = lds + (ch & 1) * 20480;
    if (ch + 1 < 17) stage(ch + 1, lds + ((ch + 1) & 1) * 20480);
#pragma unroll
    for (int s = 0; s < 4; ++s)
#pragma unroll
      for (int t = 0; t < 5; ++t) {
        const char* wp = buf + s * 5120 + (4 * g + (c >> 2)) * 160 + t * 32 + (c & 3) * 8;
        s16x4 lo = trread(wp), hi = trread(wp + 2560);
        s16x8 wv = {lo[0], lo[1], lo[2], lo[3], hi[0], hi[1], hi[2], hi[3]};
        acc[t] = __builtin_amdgcn_mfma_f32_16x16x32_bf16(__builtin_bit_cast(bf16x8, wv), a[s], acc[t], 0, 0, 0);
      }
    if (ch == 4) for (int t = 0; t < 5; ++t) { sv[t] = acc[t]; acc[t] = (f32x4){0, 0, 0, 0}; }
  }
  for (int t = 0; t < 5; ++t) {
    size_t off = (size_t)tok * 1280 + cb * 160 + t * 32 + g * 8;
    u32x2 xv = *(const u32x2*)(x + off);
    float o[4];
    for (int j = 0; j < 4; ++j) {
      float gv = acc[t][j];
      float sg = gv / (1.f + __expf(-gv));
      float xx = (j & 1) ? bhi(xv[j >> 1]) : blo(xv[j >> 1]);
      o[j] = xx + sv[t][j] * sg;
    }
    *(u32x2*)(out + off) = (u32x2){pk2(o[0], o[1]), pk2(o[2], o[3])};
  }
}

constexpr size_t WS_FL = 64u << 20;
__device__ __forceinline__ void gbar(char* ws, int st, unsigned long long tag) {
  unsigned long long* fl = (unsigned long long*)(ws + WS_FL + 4096) + st * 256;
  __threadfence(); asm volatile("s_waitcnt vmcnt(0)" ::: "memory"); __syncthreads();
  if (threadIdx.x == 0) __hip_atomic_store(fl + blockIdx.x, tag, __ATOMIC_RELAXED, __HIP_MEMORY_SCOPE_AGENT);
  if (threadIdx.x < 256) { int it = 0; while (__hip_atomic_load(fl + threadIdx.x, __ATOMIC_RELAXED, __HIP_MEMORY_SCOPE_AGENT) != tag && ++it < 2000000) __builtin_amdgcn_s_sleep(2); }
  __threadfence(); asm volatile("s_waitcnt vmcnt(0)" ::: "memory"); __syncthreads();
}
__global__ void __launch_bounds__(512) mega(const char* x, const char* wqkv, const char* wo, const char* w1, const char* w2, char* ws, char* out) {
  unsigned long long t0 = __builtin_amdgcn_s_memrealtime();
  unsigned long long* ep = (unsigned long long*)(ws + WS_FL);
  unsigned long long tag = __hip_atomic_load(ep, __ATOMIC_RELAXED, __HIP_MEMORY_SCOPE_AGENT) * 6364136223846793005ull + 1442695040888963407ull;
  k_ln(x, ws);
  gbar(ws, 0, tag);
  k_gemm1(wqkv, w1, ws);
  gbar(ws, 1, tag);
  k_attn(ws);
  gbar(ws, 2, tag);
  if (blockIdx.x == 0 && threadIdx.x == 0) __hip_atomic_store(ep, tag, __ATOMIC_RELAXED, __HIP_MEMORY_SCOPE_AGENT);
  k_out(x, wo, w2, ws, out);
  if (blockIdx.x == 0) { int it = 0; while ((long long)(__builtin_amdgcn_s_memrealtime() - t0) < WINDOW && ++it < 10000000) __builtin_amdgcn_s_sleep(1); }
}
extern "C" void kernel_launch(void* const* d_in, const int* in_sizes, int n_in, void* d_out, int out_size,
                              void* d_ws, size_t ws_size, hipStream_t stream) {
  (void)hipFuncSetAttribute((const void*)mega, hipFuncAttributeMaxDynamicSharedMemorySize, 2 * 45056);
  mega<<<256, 512, 2 * 45056, stream>>>((const char*)d_in[0], (const char*)d_in[1], (const char*)d_in[2], (const char*)d_in[4], (const char*)d_in[5], (char*)d_ws, (char*)d_out);
}
```

```cpp
#include <hip/hip_runtime.h>
#include <hip/hip_bf16.h>

struct Tables { unsigned s[2048][32]; unsigned v[64][6][64][4]; };
constexpr Tables make_tables() {
  Tables t{};
  for (unsigned b = 0; b < 256; ++b) for (unsigned w = 0; w < 8; ++w) {
    const unsigned cb = (b & 3) * 4 + ((b >> 3) & 3),
      rb = ((b >> 2) & 1) * 8 + (b >> 5),
      ph = (rb + ((cb + 1) >> 4)) % 5,
      cf = cb == 15 ? rb : cb == rb ? 15 : cb,
      rt = w & 3,
      ch = w >> 2,
      T = rb * 4 + rt,
      hd = (b & 7) * 4 + ((b >> 3) & 3),
      qb = b >> 5,
      odd = hd & 1,
      rp = (hd >> 1) * 5 + 2,
      mp = rp + odd * 3 - 2,
      tb = qb * 8 + rt * 2 + ch,
      r64 = ((b >> 1) & 3) * 8 + (b >> 5),
      c8 = ((b & 1) * 4 + ((b >> 3) & 3)) * 160,
      row = r64 * 64 + rt * 16,
      bb = b < 170 ? b : 0,
      cg = bb / 17,
      sg = (bb % 17) * 4 + (w >> 1),
      kh = sg < 48 ? sg / 24 : (sg - 48) / 10,
      pp = sg < 48 ? sg % 24 : 24 + (sg - 48) % 10;
    unsigned* p = t.s[b * 8 + w];
    p[0] = ch ? 4279238655u : 4294967295u;
    p[1] = ch ? 4294905855u : 4294967295u;
    p[2] = ch ? 268435215u : 4294967295u;
    p[3] = ch ? 4279238655u : 4294967295u;
    p[4] = ch ? 4294905855u : 4294967295u;
    p[5] = ch ? 268435215u : 4294967295u;
    p[6] = ch ? 327680 : 4096;
    p[7] = cb >= 10 ? (cb * 4 + ch * 2 + ((rt ^ 0) & 1)) * 512 + T * 40960 + 5222400 : (cb * 4 + ch * 2 + ((rt ^ 0) & 1)) * 131072 + T * 512 + 0;
    p[8] = cb >= 10 ? (cb * 4 + ch * 2 + ((rt ^ 1) & 1)) * 512 + T * 40960 + 5222400 : (cb * 4 + ch * 2 + ((rt ^ 1) & 1)) * 131072 + T * 512 + 0;
    p[9] = (cf * 3 + (ch ? 2 : (rt ^ 0) & 1)) * 131072 + T * 512 + 7864320;
    p[10] = (cf * 3 + (ch ? 2 : (rt ^ 1) & 1)) * 131072 + T * 512 + 7864320;
    p[11] = rt * 1024 + 2621440;
    p[12] = qb * 4096 + rt * 1024 + 0;
    p[13] = rt * 81920 + 5242880;
    p[14] = ch * 3145728 + row * 16 + 7864320;
    p[15] = hd * 65536 + tb * 512 + 14155776;
    p[16] = hd * 16384 + tb * 256 + 16252928;
    p[17] = ch * 1048576 + row * 16 + 14155776;
    p[18] = (cb * 16 + rb) * 8 + 16925696;
    p[19] = (rb * 16 + cb) * 8 + 16927744;
    p[20] = (tb * 32 + hd) * 8 + 16929792;
    p[21] = (r64 >> 1) * 128 + 16927744;
    p[22] = r64 * 512 + 16929792;
    p[23] = row * 16 + 16777224;
    p[24] = (c8 / 160) * 352256 + w * 1024 + 16948224;
    p[25] = ((cg * 4 + (w & 1) * 2 + 0) / 5) * 352256 + ((pp * 2 + kh) * 5 + (kh ? 4 - (cg * 4 + (w & 1) * 2 + 0) % 5 : (cg * 4 + (w & 1) * 2 + 0) % 5)) * 1024 + 16948224;
    p[26] = ((cg * 4 + (w & 1) * 2 + 1) / 5) * 352256 + ((pp * 2 + kh) * 5 + (kh ? 4 - (cg * 4 + (w & 1) * 2 + 1) % 5 : (cg * 4 + (w & 1) * 2 + 1) % 5)) * 1024 + 16948224;
    p[27] = cb < 5 ? 1051011932u : 1065353216u;
    p[28] = (ph * 8 + w) * 49152 + cf * 192;
    p[29] = (sg < 48 ? sg * 32 : pp < 32 ? kh * 320 + (pp - 24) * 40 : kh * 320 + (pp - 32) * 160 + 16) * 1280 + cg * 128 + (w & 1) * 64 + (sg < 48 ? 4 : pp < 32 ? 5 : 20);
    p[30] = (c8 / 128) * 136 + 16946176;
    p[31] = row * 1280 + c8;
  }
  for (unsigned hd = 0; hd < 32; ++hd) for (unsigned ch = 0; ch < 2; ++ch) for (unsigned l = 0; l < 64; ++l) {
    const unsigned odd = hd & 1,
      rp = (hd >> 1) * 5 + 2,
      mp = rp + odd * 3 - 2,
      r = l & 31,
      h = l >> 5,
      c0 = (hd * 20) >> 7,
      c1 = (hd * 20 + 19) >> 7,
      o0 = (64 * 0 + l) / 24,
      d0 = (64 * 0 + l) % 24,
      o1 = (64 * 1 + l) / 24,
      d1 = (64 * 1 + l) % 24,
      o2 = (64 * 2 + l) / 24,
      d2 = (64 * 2 + l) % 24;
    auto& q = t.v[hd * 2 + ch];
    q[0][l][0] = (l >> 2) * 1280 + (l & 3) * 16;
    q[0][l][1] = ((l >> 4) * 8 + ((l >> 2) & 3)) * 64 + (l & 3) * 8;
    q[0][l][2] = l * 16;
    q[0][l][3] = l * 16 + ch * 5120;
    q[1][l][0] = (l & 15) * 1280 + (l >> 5) * 16 + (ch ? 4 - ((l >> 4) & 1) : (l >> 4) & 1) * 32;
    q[1][l][1] = ch ? (d0 < 19 ? d0 : 19) * 16 + o0 * 10240 + hd * 320 - 0 : l * 16 + mp * 32768;
    q[1][l][2] = ch ? (d1 < 19 ? d1 : 19) * 16 + o1 * 10240 + hd * 320 - 1024 : l * 16 + mp * 32768 + 31744;
    q[1][l][3] = ch ? (d2 < 19 ? d2 : 19) * 16 + o2 * 10240 + hd * 320 - 2048 : l * 16 + rp * 32768 - 2048;
    q[2][l][0] = h * 1024 + r * 16 + ch * 6144 + 114688;
    q[2][l][1] = ((r > 27 ? 0 : (r < 21 ? r : 21) + 3) + h * 24) * 16 + ch * 6144 + 126928;
    q[2][l][2] = l * 16 + ch * 6144 + odd * 8 + 116736;
    q[2][l][3] = (l >> 4) * 32768 + (l & 15) * 16;
    q[3][l][0] = (l & 15) * 1280 + (l >> 4) * 8 + (ch ? 4 - 0 : 0) * 32;
    q[3][l][1] = (l & 15) * 1280 + (l >> 4) * 8 + (ch ? 4 - 1 : 1) * 32;
    q[3][l][2] = (l & 15) * 1280 + (l >> 4) * 8 + (ch ? 4 - 2 : 2) * 32;
    q[3][l][3] = (l & 15) * 8;
    q[4][l][0] = l * 16 + mp * 32768;
    q[4][l][1] = l * 16 + mp * 32768 + 31744;
    q[4][l][2] = l * 16 + rp * 32768 - 2048;
    q[4][l][3] = ((((l >> 4) & 1) ? c1 : c0) * 16 + (l & 15)) * 8 + 16925696;
    q[5][l][0] = ((((l >> 4) & 1) ? c1 : c0) * 16 + (l & 15)) * 8 + 16925696 + (h + 1) * 640;
    q[5][l][1] = h * 1024 + r * 16 + ch * 512 + 98304;
    q[5][l][2] = r * 16 + ch * 512 + odd * 8 + 98304;
    q[5][l][3] = (l >> 1) * 384 + (l & 1) * 16 + 127296;
  }
  return t;
}
static __device__ const __attribute__((aligned(4096))) Tables k_tables = make_tables();

__global__ void __launch_bounds__(512, 2)
fused_block(const void* x, const void* wqkv, const void* wo, const void* w1, const void* w2, void* ws, void* out) {
  __shared__ __attribute__((aligned(16))) char lds[163840];
  const int w = __builtin_amdgcn_readfirstlane(threadIdx.x >> 6);
  asm volatile(
"s_load_dwordx8 s[8:15], %0, 0x0\n"
"s_load_dwordx4 s[16:19], %0, 0x20\n"
"s_load_dwordx2 s[20:21], %0, 0x30\n"
"s_mov_b32 s22, %1\n"
"s_mov_b32 s23, %2\n"
"v_mbcnt_lo_u32_b32 v0, -1, 0\n"
"v_mbcnt_hi_u32_b32 v0, -1, v0\n"
"s_and_b32 s40, s22, 3\n"
"s_lshl_b32 s40, s40, 2\n"
"s_lshr_b32 s41, s22, 3\n"
"s_and_b32 s41, s41, 3\n"
"s_add_u32 s26, s40, s41\n"
"s_lshr_b32 s41, s22, 2\n"
"s_and_b32 s41, s41, 1\n"
"s_lshl_b32 s41, s41, 3\n"
"s_lshr_b32 s40, s22, 5\n"
"s_add_u32 s27, s41, s40\n"
"s_add_u32 s40, s26, 1\n"
"s_lshr_b32 s40, s40, 4\n"
"s_add_u32 s45, s27, s40\n"
"s_mul_i32 s40, s45, 13108\n"
"s_lshr_b32 s40, s40, 16\n"
"s_mul_i32 s40, s40, 5\n"
"s_sub_u32 s28, s45, s40\n"
"s_lshl_b32 s30, s23, 12\n"
"v_lshrrev_b32 v3, 3, v0\n"
"s_lshl_b32 s40, s23, 4\n"
"v_add_u32 v3, s40, v3\n"
"v_mul_u32_u24 v6, 1280, v3\n"
"v_and_b32 v3, 7, v0\n"
"v_lshrrev_b32 v4, 4, v0\n"
"v_xor_b32 v7, v3, v4\n"
"v_lshl_add_u32 v28, v7, 4, v6\n"
"v_xor_b32 v4, 4, v7\n"
"v_lshl_add_u32 v4, v4, 4, v6\n"
"v_add_u32 v29, 9216, v4\n"
"v_and_b32 v4, 15, v0\n"
"v_lshrrev_b32 v3, 4, v0\n"
"v_lshlrev_b32 v3, 2, v3\n"
"v_xor_b32 v3, v4, v3\n"
"v_lshrrev_b32 v4, 4, v0\n"
"s_lshl_b32 s40, s23, 3\n"
"v_add_u32 v4, s40, v4\n"
"v_mul_u32_u24 v4, 3840, v4\n"
"v_lshl_add_u32 v6, v3, 4, v4\n"
"v_add_u32 v30, 2048, v6\n"
"v_add_u32 v31, 16384, v6\n"
"s_mul_i32 s40, s27, 163840\n"
"s_lshl_b32 s41, s28, 8\n"
"s_add_u32 s44, s40, s41\n"
"s_mul_i32 s41, s28, 491520\n"
"s_lshl_b32 s40, s26, 8\n"
"s_add_u32 s45, s41, s40\n"
"s_lshl_b32 s29, s28, 1\n"
"s_waitcnt lgkmcnt(0)\n"
"s_add_u32 s34, s8, s44\n"
"s_addc_u32 s35, s9, 0\n"
"s_cmp_eq_u32 s26, 15\n"
"s_cbranch_scc1 s0_1_%=\n"
"s_add_u32 s36, s10, s45\n"
"s_addc_u32 s37, s11, 0\n"
"s_sub_u32 s36, s36, 4096\n"
"s_subb_u32 s37, s37, 0\n"
"s_add_u32 m0, s30, 0\n"
"s_add_u32 s29, s29, 1\n"
"global_load_lds_dwordx4 v28, s[34:35] offset:0\n"
"global_load_lds_dwordx4 v29, s[34:35] offset:1024\n"
"global_load_lds_dwordx4 v30, s[36:37] offset:2048\n"
"global_load_lds_dwordx4 v31, s[36:37] offset:3072\n"
"s_add_u32 s34, s34, 128\n"
"s_addc_u32 s35, s35, 0\n"
"s_add_u32 s36, s36, 245760\n"
"s_addc_u32 s37, s37, 0\n"
"s_add_u32 m0, s30, 32768\n"
"s_add_u32 s29, s29, 1\n"
"global_load_lds_dwordx4 v28, s[34:35] offset:0\n"
"s_branch s1_2_%=\n"
"s0_1_%=:\n"
"s_sub_u32 s36, s34, 1920\n"
"s_subb_u32 s37, s35, 0\n"
"s_mov_b32 s29, s28\n"
"s_add_u32 m0, s30, 0\n"
"s_add_u32 s29, s29, 1\n"
"global_load_lds_dwordx4 v28, s[34:35] offset:0\n"
"global_load_lds_dwordx4 v29, s[34:35] offset:1024\n"
"global_load_lds_dwordx4 v28, s[36:37] offset:2048\n"
"global_load_lds_dwordx4 v29, s[36:37] offset:3072\n"
"s_mov_b32 s40, 256\n"
"s_mov_b32 s41, 256\n"
"s_cmp_eq_u32 s29, 5\n"
"s_cmov_b32 s40, -1024\n"
"s_cmov_b32 s41, -1024\n"
"s_cselect_b32 s42, -1, 0\n"
"s_add_u32 s34, s34, s40\n"
"s_addc_u32 s35, s35, s42\n"
"s_add_u32 s36, s36, s41\n"
"s_addc_u32 s37, s37, s42\n"
"s_add_u32 m0, s30, 32768\n"
"s_add_u32 s29, s29, 1\n"
"global_load_lds_dwordx4 v28, s[34:35] offset:0\n"
"s1_2_%=:\n"
"s_getpc_b64 s[38:39]\n"
"pc_10_%=:\n"
"s_add_u32 s44, s18, 16924672\n"
"s_addc_u32 s45, s19, 0\n"
"s_load_dwordx2 s[24:25], s[44:45], 0x0\n"
"s_mov_b64 s[98:99], %4\n"
"s_lshl_b32 s40, s22, 3\n"
"s_add_u32 s40, s40, s23\n"
"s_lshl_b32 s45, s40, 7\n"
"s_add_u32 s40, s98, s45\n"
"s_addc_u32 s41, s99, 0\n"
"s_load_dwordx16 s[48:63], s[40:41], 0x0\n"
"s_load_dwordx16 s[64:79], s[40:41], 0x40\n"
"s_and_b32 s40, s22, 7\n"
"s_lshl_b32 s40, s40, 2\n"
"s_lshr_b32 s41, s22, 3\n"
"s_and_b32 s41, s41, 3\n"
"s_add_u32 s41, s40, s41\n"
"s_mul_i32 s41, s41, 12288\n"
"s_add_u32 s45, s41, 262144\n"
"s_add_u32 s98, s98, s45\n"
"s_addc_u32 s99, s99, 0\n"
"s_mul_i32 s33, s23, 3072\n"
"s_and_b32 s46, s23, 3\n"
"s_lshr_b32 s47, s23, 2\n"
"v_and_b32 v1, 31, v0\n"
"v_lshrrev_b32 v2, 5, v0\n"
"v_lshlrev_b32 v24, 4, v0\n"
"v_lshrrev_b32 v4, 4, v1\n"
"v_and_b32 v3, 15, v0\n"
"v_lshlrev_b32 v3, 7, v3\n"
"v_lshl_add_u32 v3, v4, 12, v3\n"
"s_lshl_b32 s41, s46, 13\n"
"v_add_u32 v6, s41, v3\n"
"v_and_b32 v3, 15, v0\n"
"v_lshrrev_b32 v3, 1, v3\n"
"v_xor_b32 v3, v3, v2\n"
"s_and_b32 s41, s46, 2\n"
"v_xor_b32 v7, s41, v3\n"
"v_lshl_add_u32 v36, v7, 4, v6\n"
"v_xor_b32 v3, 2, v7\n"
"v_lshl_add_u32 v37, v3, 4, v6\n"
"v_xor_b32 v3, 4, v7\n"
"v_lshl_add_u32 v38, v3, 4, v6\n"
"v_xor_b32 v3, 6, v7\n"
"v_lshl_add_u32 v39, v3, 4, v6\n"
"v_lshrrev_b32 v3, 2, v0\n"
"v_and_b32 v6, 3, v3\n"
"v_lshrrev_b32 v3, 4, v0\n"
"v_and_b32 v3, 1, v3\n"
"v_and_b32 v4, 3, v0\n"
"v_lshlrev_b32 v4, 3, v4\n"
"v_lshl_add_u32 v7, v3, 5, v4\n"
"s_and_b32 s41, s46, 1\n"
"s_lshl_b32 s40, s47, 1\n"
"s_add_u32 s40, s41, s40\n"
"v_xor_b32 v4, s40, v6\n"
"v_lshlrev_b32 v3, 8, v6\n"
"v_lshl_add_u32 v3, v2, 12, v3\n"
"v_add_u32 v3, v3, v7\n"
"v_lshl_add_u32 v3, v4, 6, v3\n"
"s_lshr_b32 s40, s46, 1\n"
"s_lshl_b32 s40, s40, 13\n"
"s_add_u32 s40, s40, 2048\n"
"v_add_u32 v32, s40, v3\n"
"v_xor_b32 v33, 8192, v32\n"
"v_xor_b32 v34, 64, v32\n"
"v_xor_b32 v35, 64, v33\n"
"v_mul_u32_u24 v3, 192, v6\n"
"v_mul_u32_u24 v4, 1536, v2\n"
"v_add_u32 v4, v3, v4\n"
"v_add_u32 v52, v4, v7\n"
"s_and_b32 s45, s46, 1\n"
"s_xor_b32 s44, s45, 1\n"
"s_cmp_eq_u32 s47, 1\n"
"s_cmov_b32 s45, 2\n"
"s_lshr_b32 s40, s46, 1\n"
"s_mul_i32 s40, s40, 3072\n"
"s_add_u32 s40, s40, 114688\n"
"v_add_u32 v53, s40, v52\n"
"s_lshl_b32 s40, s45, 6\n"
"v_add_u32 v6, s40, v53\n"
"s_lshl_b32 s40, s44, 6\n"
"v_add_u32 v8, s40, v53\n"
"s_lshr_b32 s40, s46, 1\n"
"s_xor_b32 s40, s40, 1\n"
"s_mul_i32 s40, s40, 3072\n"
"s_add_u32 s40, s40, 114688\n"
"v_add_u32 v53, s40, v52\n"
"s_lshl_b32 s40, s45, 6\n"
"v_add_u32 v7, s40, v53\n"
"s_lshl_b32 s40, s44, 6\n"
"v_add_u32 v9, s40, v53\n"
"s_and_b32 s40, s23, 5\n"
"s_lshl_b32 s40, s40, 8\n"
"s_add_u32 s40, s40, 110592\n"
"v_lshl_add_u32 v4, v0, 2, s40\n"
"v_lshlrev_b32 v3, 8, v2\n"
"v_xor_b32 v11, v4, v3\n"
"s_lshl_b32 s40, s23, 8\n"
"s_add_u32 s40, s40, 110592\n"
"v_lshl_add_u32 v10, v0, 2, s40\n"
"v_lshlrev_b32 v52, 4, v1\n"
"s_lshl_b32 s40, s27, 2\n"
"s_add_u32 s40, s40, s46\n"
"s_lshl_b32 s40, s40, 9\n"
"s_add_u32 s40, s40, 16777216\n"
"v_add_u32 v13, s40, v52\n"
"s_cmp_eq_u32 s26, s27\n"
"s_cselect_b32 s44, 15, s26\n"
"s_cmp_eq_u32 s26, 15\n"
"s_cmov_b32 s44, s27\n"
"v_xor_b32 v3, s45, v2\n"
"v_min_u32 v3, 2, v3\n"
"s_mul_i32 s40, s44, 3\n"
"v_add_u32 v3, s40, v3\n"
"v_lshl_add_u32 v3, v3, 9, v52\n"
"v_add_u32 v12, 16809984, v3\n"
"v_lshl_add_u32 v17, v2, 15, v52\n"
"v_mul_u32_u24 v3, 10240, v2\n"
"v_add_u32 v16, v3, v52\n"
"v_lshrrev_b32 v3, 2, v0\n"
"v_lshrrev_b32 v4, 4, v0\n"
"v_xor_b32 v4, v3, v4\n"
"v_and_b32 v4, 1, v4\n"
"v_and_b32 v3, 3, v0\n"
"v_or_b32 v52, v4, v3\n"
"v_cmp_eq_u32 vcc, 0, v52\n"
"v_mov_b32 v52, 0x3f803f80\n"
"s_nop 0\n"
"v_cndmask_b32 v40, 0, v52, vcc\n"
"v_cndmask_b32 v41, 0, v52, vcc\n"
"v_cndmask_b32 v42, 0, v52, vcc\n"
"v_cndmask_b32 v43, 0, v52, vcc\n"
"v_mul_u32_u24 v3, 5462, v0\n"
"v_lshrrev_b32 v53, 16, v3\n"
"v_mul_u32_u24 v3, 2880, v53\n"
"v_add_u32 v25, v3, v24\n"
"v_add_u32 v52, 64, v0\n"
"v_mul_u32_u24 v3, 5462, v52\n"
"v_lshrrev_b32 v53, 16, v3\n"
"v_mul_u32_u24 v3, 2880, v53\n"
"v_add_u32 v26, v3, v24\n"
"v_add_u32 v52, 128, v0\n"
"v_mul_u32_u24 v3, 5462, v52\n"
"v_lshrrev_b32 v53, 16, v3\n"
"v_mul_u32_u24 v3, 2880, v53\n"
"v_add_u32 v27, v3, v24\n"
"v_and_b32 v3, 1, v0\n"
"s_lshr_b32 s40, s22, 3\n"
"s_lshl_b32 s40, s40, 3\n"
"s_add_u32 s40, s40, s23\n"
"s_lshl_b32 s40, s40, 8\n"
"v_lshl_add_u32 v3, v3, 7, s40\n"
"v_min_u32 v3, exact_9_%=-pc_10_%=, v3\n"
"global_load_dword v15, v3, s[38:39]\n"
"v_min_u32 v3, 1, v0\n"
"s_lshr_b32 s40, s22, 5\n"
"s_lshl_b32 s40, s40, 3\n"
"s_add_u32 s40, s40, s23\n"
"s_lshl_b32 s40, s40, 1\n"
"v_add_u32 v3, s40, v3\n"
"v_min_u32 v3, 95, v3\n"
"v_lshlrev_b32 v3, 7, v3\n"
"global_load_dword v15, v3, s[98:99]\n"
"s_cmp_eq_u32 s26, 15\n"
"s_cbranch_scc1 stat_3_%=\n"
"s_cmp_ge_u32 s26, 10\n"
"s_cbranch_scc1 p1v_4_%=\n"
"global_load_lds_dwordx4 v29, s[34:35] offset:1024\n"
"global_load_lds_dwordx4 v30, s[36:37] offset:2048\n"
"global_load_lds_dwordx4 v31, s[36:37] offset:3072\n"
"s_mov_b32 s40, 128\n"
"s_mov_b32 s41, 245760\n"
"s_cmp_eq_u32 s29, 10\n"
"s_cmov_b32 s40, -1152\n"
"s_cmov_b32 s41, -2211840\n"
"s_cselect_b32 s42, -1, 0\n"
"s_add_u32 s34, s34, s40\n"
"s_addc_u32 s35, s35, s42\n"
"s_add_u32 s36, s36, s41\n"
"s_addc_u32 s37, s37, s42\n"
"s_waitcnt lgkmcnt(0)\n"
"s_mul_i32 s24, s24, 1664525\n"
"s_add_u32 s24, s24, 1013904223\n"
"s_mul_i32 s25, s25, 22695477\n"
"s_add_u32 s25, s25, 1\n"
"s_mov_b32 s31, 100000\n"
"s_add_u32 s38, s14, s76\n"
"s_addc_u32 s39, s15, 0\n"
"s_add_u32 s8, s8, s79\n"
"s_addc_u32 s9, s9, 0\n"
"s_add_u32 s20, s20, s79\n"
"s_addc_u32 s21, s21, 0\n"
"s_cmp_eq_u32 s46, 0\n"
"s_cselect_b32 s76, 2, 1\n"
"s_cmp_lt_u32 s27, 5\n"
"s_cselect_b32 s76, s76, 0\n"
"s_waitcnt vmcnt(6)\n"
"s_barrier\n"
"ds_read_b64_tr_b16 v[48:49], v32 offset:0\n"
"ds_read_b64_tr_b16 v[50:51], v32 offset:1024\n"
"ds_read_b128 v[252:255], v36 offset:0\n"
"ds_read_b64_tr_b16 v[52:53], v34 offset:0\n"
"ds_read_b64_tr_b16 v[54:55], v34 offset:1024\n"
"s_setprio 2\n"
"ds_read_b64_tr_b16 v[56:57], v33 offset:0\n"
"ds_read_b64_tr_b16 v[58:59], v33 offset:1024\n"
"ds_read_b128 v[248:251], v37 offset:0\n"
"s_waitcnt lgkmcnt(5)\n"
"v_mfma_f32_32x32x16_bf16 v[64:79], v[48:51], v[252:255], 0\n"
"s_cmp_lt_u32 s76, 1\n"
"s_cbranch_scc1 nocq_11_%=\n"
"v_mfma_f32_16x16x32_bf16 v[44:47], v[40:43], v[48:51], 0\n"
"nocq_11_%=:\n"
"ds_read_b64_tr_b16 v[60:61], v35 offset:0\n"
"ds_read_b64_tr_b16 v[62:63], v35 offset:1024\n"
"s_waitcnt lgkmcnt(5)\n"
"v_mfma_f32_32x32x16_bf16 v[80:95], v[52:55], v[252:255], 0\n"
"s_add_u32 m0, s30, 65536\n"
"s_add_u32 s29, s29, 1\n"
"global_load_lds_dwordx4 v28, s[34:35] offset:0\n"
"global_load_lds_dwordx4 v29, s[34:35] offset:1024\n"
"s_setprio 1\n"
"ds_read_b64_tr_b16 v[48:49], v32 offset:16384\n"
"ds_read_b64_tr_b16 v[50:51], v32 offset:17408\n"
"ds_read_b128 v[244:247], v38 offset:0\n"
"s_waitcnt lgkmcnt(5)\n"
"v_mfma_f32_32x32x16_bf16 v[64:79], v[56:59], v[248:251], v[64:79]\n"
"ds_read_b64_tr_b16 v[52:53], v34 offset:16384\n"
"ds_read_b64_tr_b16 v[54:55], v34 offset:17408\n"
"s_waitcnt lgkmcnt(5)\n"
"v_mfma_f32_32x32x16_bf16 v[80:95], v[60:63], v[248:251], v[80:95]\n"
"global_load_lds_dwordx4 v30, s[36:37] offset:2048\n"
"s_setprio 0\n"
"ds_read_b64_tr_b16 v[56:57], v33 offset:16384\n"
"ds_read_b64_tr_b16 v[58:59], v33 offset:17408\n"
"ds_read_b128 v[240:243], v39 offset:0\n"
"s_waitcnt lgkmcnt(5)\n"
"v_mfma_f32_32x32x16_bf16 v[64:79], v[48:51], v[244:247], v[64:79]\n"
"s_cmp_lt_u32 s76, 1\n"
"s_cbranch_scc1 nocq_12_%=\n"
"v_mfma_f32_16x16x32_bf16 v[44:47], v[40:43], v[48:51], v[44:47]\n"
"nocq_12_%=:\n"
"ds_read_b64_tr_b16 v[60:61], v35 offset:16384\n"
"ds_read_b64_tr_b16 v[62:63], v35 offset:17408\n"
"s_waitcnt lgkmcnt(5)\n"
"v_mfma_f32_32x32x16_bf16 v[80:95], v[52:55], v[244:247], v[80:95]\n"
"global_load_lds_dwordx4 v31, s[36:37] offset:3072\n"
"s_add_u32 s34, s34, 128\n"
"s_addc_u32 s35, s35, 0\n"
"s_add_u32 s36, s36, 245760\n"
"s_addc_u32 s37, s37, 0\n"
"s_waitcnt vmcnt(4)\n"
"s_waitcnt lgkmcnt(0)\n"
"s_barrier\n"
"s_setprio 3\n"
"ds_read_b64_tr_b16 v[48:49], v32 offset:32768\n"
"ds_read_b64_tr_b16 v[50:51], v32 offset:33792\n"
"ds_read_b128 v[236:239], v36 offset:32768\n"
"v_mfma_f32_32x32x16_bf16 v[64:79], v[56:59], v[240:243], v[64:79]\n"
"ds_read_b64_tr_b16 v[52:53], v34 offset:32768\n"
"ds_read_b64_tr_b16 v[54:55], v34 offset:33792\n"
"v_mfma_f32_32x32x16_bf16 v[80:95], v[60:63], v[240:243], v[80:95]\n"
"s_add_u32 m0, s30, 0\n"
"s_add_u32 s29, s29, 1\n"
"global_load_lds_dwordx4 v28, s[34:35] offset:0\n"
"s_setprio 2\n"
"ds_read_b64_tr_b16 v[56:57], v33 offset:32768\n"
"ds_read_b64_tr_b16 v[58:59], v33 offset:33792\n"
"ds_read_b128 v[232:235], v37 offset:32768\n"
"s_waitcnt lgkmcnt(5)\n"
"v_mfma_f32_32x32x16_bf16 v[64:79], v[48:51], v[236:239], v[64:79]\n"
"s_cmp_lt_u32 s76, 1\n"
"s_cbranch_scc1 nocq_13_%=\n"
"v_mfma_f32_16x16x32_bf16 v[44:47], v[40:43], v[48:51], v[44:47]\n"
"nocq_13_%=:\n"
"ds_read_b64_tr_b16 v[60:61], v35 offset:32768\n"
"ds_read_b64_tr_b16 v[62:63], v35 offset:33792\n"
"s_waitcnt lgkmcnt(5)\n"
"v_mfma_f32_32x32x16_bf16 v[80:95], v[52:55], v[236:239], v[80:95]\n"
"global_load_lds_dwordx4 v29, s[34:35] offset:1024\n"
"s_setprio 1\n"
"ds_read_b64_tr_b16 v[48:49], v32 offset:49152\n"
"ds_read_b64_tr_b16 v[50:51], v32 offset:50176\n"
"ds_read_b128 v[228:231], v38 offset:32768\n"
"s_waitcnt lgkmcnt(5)\n"
"v_mfma_f32_32x32x16_bf16 v[64:79], v[56:59], v[232:235], v[64:79]\n"
"ds_read_b64_tr_b16 v[52:53], v34 offset:49152\n"
"ds_read_b64_tr_b16 v[54:55], v34 offset:50176\n"
"s_waitcnt lgkmcnt(5)\n"
"v_mfma_f32_32x32x16_bf16 v[80:95], v[60:63], v[232:235], v[80:95]\n"
"global_load_lds_dwordx4 v30, s[36:37] offset:2048\n"
"s_setprio 0\n"
"ds_read_b64_tr_b16 v[56:57], v33 offset:49152\n"
"ds_read_b64_tr_b16 v[58:59], v33 offset:50176\n"
"ds_read_b128 v[224:227], v39 offset:32768\n"
"s_waitcnt lgkmcnt(5)\n"
"v_mfma_f32_32x32x16_bf16 v[64:79], v[48:51], v[228:231], v[64:79]\n"
"s_cmp_lt_u32 s76, 1\n"
"s_cbranch_scc1 nocq_14_%=\n"
"v_mfma_f32_16x16x32_bf16 v[44:47], v[40:43], v[48:51], v[44:47]\n"
"nocq_14_%=:\n"
"ds_read_b64_tr_b16 v[60:61], v35 offset:49152\n"
"ds_read_b64_tr_b16 v[62:63], v35 offset:50176\n"
"s_waitcnt lgkmcnt(5)\n"
"v_mfma_f32_32x32x16_bf16 v[80:95], v[52:55], v[228:231], v[80:95]\n"
"global_load_lds_dwordx4 v31, s[36:37] offset:3072\n"
"s_mov_b32 s40, 128\n"
"s_mov_b32 s41, 245760\n"
"s_cmp_eq_u32 s29, 10\n"
"s_cmov_b32 s40, -1152\n"
"s_cmov_b32 s41, -2211840\n"
"s_cselect_b32 s42, -1, 0\n"
"s_add_u32 s34, s34, s40\n"
"s_addc_u32 s35, s35, s42\n"
"s_add_u32 s36, s36, s41\n"
"s_addc_u32 s37, s37, s42\n"
"s_waitcnt vmcnt(4)\n"
"s_waitcnt lgkmcnt(0)\n"
"s_barrier\n"
"s_setprio 3\n"
"v_add_u32 v32, 65536, v32\n"
"v_add_u32 v33, 65536, v33\n"
"ds_read_b64_tr_b16 v[48:49], v32 offset:0\n"
"ds_read_b64_tr_b16 v[50:51], v32 offset:1024\n"
"v_add_u32 v36, 65536, v36\n"
"v_add_u32 v37, 65536, v37\n"
"v_add_u32 v38, 65536, v38\n"
"v_add_u32 v39, 65536, v39\n"
"ds_read_b128 v[220:223], v36 offset:0\n"
"v_mfma_f32_32x32x16_bf16 v[64:79], v[56:59], v[224:227], v[64:79]\n"
"v_add_u32 v34, 65536, v34\n"
"v_add_u32 v35, 65536, v35\n"
"ds_read_b64_tr_b16 v[52:53], v34 offset:0\n"
"ds_read_b64_tr_b16 v[54:55], v34 offset:1024\n"
"v_mfma_f32_32x32x16_bf16 v[80:95], v[60:63], v[224:227], v[80:95]\n"
"s_add_u32 m0, s30, 32768\n"
"s_add_u32 s29, s29, 1\n"
"global_load_lds_dwordx4 v28, s[34:35] offset:0\n"
"s_setprio 2\n"
"s_cmp_lt_u32 s76, 1\n"
"s_cbranch_scc1 nocq_15_%=\n"
"ds_write_b32 v10, v44\n"
"nocq_15_%=:\n"
"ds_read_b64_tr_b16 v[56:57], v33 offset:0\n"
"ds_read_b64_tr_b16 v[58:59], v33 offset:1024\n"
"ds_read_b128 v[216:219], v37 offset:0\n"
"s_waitcnt lgkmcnt(5)\n"
"v_mfma_f32_32x32x16_bf16 v[64:79], v[48:51], v[220:223], v[64:79]\n"
"s_mov_b32 s44, m0\n"
"s_add_u32 m0, s33, 114688\n"
"s_nop 0\n"
"global_load_lds_dwordx4 v25, s[38:39] offset:0\n"
"s_mov_b32 m0, s44\n"
"ds_read_b64_tr_b16 v[60:61], v35 offset:0\n"
"ds_read_b64_tr_b16 v[62:63], v35 offset:1024\n"
"s_waitcnt lgkmcnt(5)\n"
"v_mfma_f32_32x32x16_bf16 v[80:95], v[52:55], v[220:223], v[80:95]\n"
"global_load_lds_dwordx4 v29, s[34:35] offset:1024\n"
"s_setprio 1\n"
"ds_read_b64_tr_b16 v[48:49], v32 offset:16384\n"
"ds_read_b64_tr_b16 v[50:51], v32 offset:17408\n"
"ds_read_b128 v[212:215], v38 offset:0\n"
"s_waitcnt lgkmcnt(5)\n"
"v_mfma_f32_32x32x16_bf16 v[64:79], v[56:59], v[216:219], v[64:79]\n"
"s_mov_b32 s44, m0\n"
"s_add_u32 m0, s33, 114688\n"
"s_nop 0\n"
"global_load_lds_dwordx4 v26, s[38:39] offset:1024\n"
"s_mov_b32 m0, s44\n"
"ds_read_b64_tr_b16 v[52:53], v34 offset:16384\n"
"ds_read_b64_tr_b16 v[54:55], v34 offset:17408\n"
"s_waitcnt lgkmcnt(5)\n"
"v_mfma_f32_32x32x16_bf16 v[80:95], v[60:63], v[216:219], v[80:95]\n"
"global_load_lds_dwordx4 v30, s[36:37] offset:2048\n"
"s_setprio 0\n"
"ds_read_b64_tr_b16 v[56:57], v33 offset:16384\n"
"ds_read_b64_tr_b16 v[58:59], v33 offset:17408\n"
"ds_read_b128 v[208:211], v39 offset:0\n"
"s_waitcnt lgkmcnt(5)\n"
"v_mfma_f32_32x32x16_bf16 v[64:79], v[48:51], v[212:215], v[64:79]\n"
"s_mov_b32 s44, m0\n"
"s_add_u32 m0, s33, 114688\n"
"s_nop 0\n"
"global_load_lds_dwordx4 v27, s[38:39] offset:2048\n"
"s_cmp_eq_u32 s28, 4\n"
"s_cselect_b32 s45, 1966080, 0\n"
"s_add_u32 s38, s38, 393216\n"
"s_addc_u32 s39, s39, 0\n"
"s_sub_u32 s38, s38, s45\n"
"s_subb_u32 s39, s39, 0\n"
"s_mov_b32 m0, s44\n"
"ds_read_b64_tr_b16 v[60:61], v35 offset:16384\n"
"ds_read_b64_tr_b16 v[62:63], v35 offset:17408\n"
"s_waitcnt lgkmcnt(5)\n"
"v_mfma_f32_32x32x16_bf16 v[80:95], v[52:55], v[212:215], v[80:95]\n"
"global_load_lds_dwordx4 v31, s[36:37] offset:3072\n"
"s_add_u32 s34, s34, 128\n"
"s_addc_u32 s35, s35, 0\n"
"s_add_u32 s36, s36, 245760\n"
"s_addc_u32 s37, s37, 0\n"
"s_waitcnt vmcnt(7)\n"
"s_waitcnt lgkmcnt(0)\n"
"s_barrier\n"
"s_cmp_lt_u32 s76, 2\n"
"s_cbranch_scc1 nocq_16_%=\n"
"ds_read2st64_b32 v[40:41], v11 offset0:0 offset1:2\n"
"nocq_16_%=:\n"
"s_setprio 3\n"
"v_subrev_u32 v32, 65536, v32\n"
"v_subrev_u32 v33, 65536, v33\n"
"ds_read_b64_tr_b16 v[48:49], v32 offset:0\n"
"ds_read_b64_tr_b16 v[50:51], v32 offset:1024\n"
"v_subrev_u32 v36, 65536, v36\n"
"v_subrev_u32 v37, 65536, v37\n"
"v_subrev_u32 v38, 65536, v38\n"
"v_subrev_u32 v39, 65536, v39\n"
"ds_read_b128 v[204:207], v36 offset:0\n"
"v_mfma_f32_32x32x16_bf16 v[64:79], v[56:59], v[208:211], v[64:79]\n"
"v_subrev_u32 v34, 65536, v34\n"
"v_subrev_u32 v35, 65536, v35\n"
"ds_read_b64_tr_b16 v[52:53], v34 offset:0\n"
"ds_read_b64_tr_b16 v[54:55], v34 offset:1024\n"
"v_mfma_f32_32x32x16_bf16 v[80:95], v[60:63], v[208:211], v[80:95]\n"
"s_add_u32 m0, s30, 65536\n"
"s_add_u32 s29, s29, 1\n"
"global_load_lds_dwordx4 v28, s[34:35] offset:0\n"
"s_setprio 2\n"
"ds_read_b64_tr_b16 v[56:57], v33 offset:0\n"
"ds_read_b64_tr_b16 v[58:59], v33 offset:1024\n"
"ds_read_b128 v[200:203], v37 offset:0\n"
"s_waitcnt lgkmcnt(5)\n"
"v_mfma_f32_32x32x16_bf16 v[64:79], v[48:51], v[204:207], v[64:79]\n"
"ds_read_b64_tr_b16 v[60:61], v35 offset:0\n"
"ds_read_b64_tr_b16 v[62:63], v35 offset:1024\n"
"s_waitcnt lgkmcnt(5)\n"
"v_mfma_f32_32x32x16_bf16 v[80:95], v[52:55], v[204:207], v[80:95]\n"
"global_load_lds_dwordx4 v29, s[34:35] offset:1024\n"
"s_setprio 1\n"
"ds_read_b64_tr_b16 v[48:49], v32 offset:16384\n"
"ds_read_b64_tr_b16 v[50:51], v32 offset:17408\n"
"ds_read_b128 v[196:199], v38 offset:0\n"
"s_waitcnt lgkmcnt(5)\n"
"v_mfma_f32_32x32x16_bf16 v[64:79], v[56:59], v[200:203], v[64:79]\n"
"ds_read_b64_tr_b16 v[52:53], v34 offset:16384\n"
"ds_read_b64_tr_b16 v[54:55], v34 offset:17408\n"
"s_waitcnt lgkmcnt(5)\n"
"v_mfma_f32_32x32x16_bf16 v[80:95], v[60:63], v[200:203], v[80:95]\n"
"global_load_lds_dwordx4 v30, s[36:37] offset:2048\n"
"s_setprio 0\n"
"ds_read_b64_tr_b16 v[56:57], v33 offset:16384\n"
"ds_read_b64_tr_b16 v[58:59], v33 offset:17408\n"
"ds_read_b128 v[192:195], v39 offset:0\n"
"s_waitcnt lgkmcnt(5)\n"
"v_mfma_f32_32x32x16_bf16 v[64:79], v[48:51], v[196:199], v[64:79]\n"
"ds_read_b64_tr_b16 v[60:61], v35 offset:16384\n"
"ds_read_b64_tr_b16 v[62:63], v35 offset:17408\n"
"s_waitcnt lgkmcnt(5)\n"
"v_mfma_f32_32x32x16_bf16 v[80:95], v[52:55], v[196:199], v[80:95]\n"
"global_load_lds_dwordx4 v31, s[36:37] offset:3072\n"
"s_mov_b32 s40, 128\n"
"s_mov_b32 s41, 245760\n"
"s_cmp_eq_u32 s29, 10\n"
"s_cmov_b32 s40, -1152\n"
"s_cmov_b32 s41, -2211840\n"
"s_cselect_b32 s42, -1, 0\n"
"s_add_u32 s34, s34, s40\n"
"s_addc_u32 s35, s35, s42\n"
"s_add_u32 s36, s36, s41\n"
"s_addc_u32 s37, s37, s42\n"
"s_waitcnt vmcnt(4)\n"
"s_waitcnt lgkmcnt(0)\n"
"s_barrier\n"
"s_cmp_lt_u32 s76, 2\n"
"s_cbranch_scc1 nocq_17_%=\n"
"v_add_f32 v40, v40, v41\n"
"v_mov_b32 v41, s24\n"
"v_mov_b32 v42, v40\n"
"v_mov_b32 v43, s25\n"
"s_mul_i32 s40, s26, 5\n"
"s_add_u32 s40, s40, s28\n"
"s_lshl_b32 s40, s40, 1\n"
"s_add_u32 s40, s40, s47\n"
"s_lshl_b32 s40, s40, 9\n"
"s_add_u32 s45, s40, 16842752\n"
"s_add_u32 s40, s18, s45\n"
"s_addc_u32 s41, s19, 0\n"
"v_permlane32_swap_b32 v40, v42\n"
"s_mov_b32 exec_hi, 0\n"
"global_store_dwordx4 v17, v[40:43], s[40:41] sc1\n"
"s_mov_b32 exec_hi, -1\n"
"nocq_17_%=:\n"
"s_setprio 3\n"
"ds_read_b64_tr_b16 v[48:49], v32 offset:32768\n"
"ds_read_b64_tr_b16 v[50:51], v32 offset:33792\n"
"ds_read_b128 v[188:191], v36 offset:32768\n"
"v_mfma_f32_32x32x16_bf16 v[64:79], v[56:59], v[192:195], v[64:79]\n"
"ds_read_b64_tr_b16 v[52:53], v34 offset:32768\n"
"ds_read_b64_tr_b16 v[54:55], v34 offset:33792\n"
"v_mfma_f32_32x32x16_bf16 v[80:95], v[60:63], v[192:195], v[80:95]\n"
"s_add_u32 m0, s30, 0\n"
"s_add_u32 s29, s29, 1\n"
"global_load_lds_dwordx4 v28, s[34:35] offset:0\n"
"s_setprio 2\n"
"ds_read_b64_tr_b16 v[56:57], v33 offset:32768\n"
"ds_read_b64_tr_b16 v[58:59], v33 offset:33792\n"
"ds_read_b128 v[184:187], v37 offset:32768\n"
"s_waitcnt lgkmcnt(5)\n"
"v_mfma_f32_32x32x16_bf16 v[64:79], v[48:51], v[188:191], v[64:79]\n"
"ds_read_b64_tr_b16 v[60:61], v35 offset:32768\n"
"ds_read_b64_tr_b16 v[62:63], v35 offset:33792\n"
"s_waitcnt lgkmcnt(5)\n"
"v_mfma_f32_32x32x16_bf16 v[80:95], v[52:55], v[188:191], v[80:95]\n"
"global_load_lds_dwordx4 v29, s[34:35] offset:1024\n"
"s_setprio 1\n"
"ds_read_b64_tr_b16 v[48:49], v32 offset:49152\n"
"ds_read_b64_tr_b16 v[50:51], v32 offset:50176\n"
"ds_read_b128 v[180:183], v38 offset:32768\n"
"s_waitcnt lgkmcnt(5)\n"
"v_mfma_f32_32x32x16_bf16 v[64:79], v[56:59], v[184:187], v[64:79]\n"
"ds_read_b64_tr_b16 v[52:53], v34 offset:49152\n"
"ds_read_b64_tr_b16 v[54:55], v34 offset:50176\n"
"s_waitcnt lgkmcnt(5)\n"
"v_mfma_f32_32x32x16_bf16 v[80:95], v[60:63], v[184:187], v[80:95]\n"
"global_load_lds_dwordx4 v30, s[36:37] offset:2048\n"
"s_setprio 0\n"
"ds_read_b64_tr_b16 v[56:57], v33 offset:49152\n"
"ds_read_b64_tr_b16 v[58:59], v33 offset:50176\n"
"ds_read_b128 v[176:179], v39 offset:32768\n"
"s_waitcnt lgkmcnt(5)\n"
"v_mfma_f32_32x32x16_bf16 v[64:79], v[48:51], v[180:183], v[64:79]\n"
"ds_read_b64_tr_b16 v[60:61], v35 offset:49152\n"
"ds_read_b64_tr_b16 v[62:63], v35 offset:50176\n"
"s_waitcnt lgkmcnt(5)\n"
"v_mfma_f32_32x32x16_bf16 v[80:95], v[52:55], v[180:183], v[80:95]\n"
"global_load_lds_dwordx4 v31, s[36:37] offset:3072\n"
"s_add_u32 s34, s34, 128\n"
"s_addc_u32 s35, s35, 0\n"
"s_add_u32 s36, s36, 245760\n"
"s_addc_u32 s37, s37, 0\n"
"s_waitcnt vmcnt(5)\n"
"s_cmp_eq_u32 s76, 2\n"
"s_cbranch_scc1 cqw_18_%=\n"
"s_waitcnt vmcnt(4)\n"
"cqw_18_%=:\n"
"s_waitcnt lgkmcnt(0)\n"
"s_barrier\n"
"s_setprio 3\n"
"v_add_u32 v32, 65536, v32\n"
"v_add_u32 v33, 65536, v33\n"
"ds_read_b64_tr_b16 v[48:49], v32 offset:0\n"
"ds_read_b64_tr_b16 v[50:51], v32 offset:1024\n"
"v_add_u32 v36, 65536, v36\n"
"v_add_u32 v37, 65536, v37\n"
"v_add_u32 v38, 65536, v38\n"
"v_add_u32 v39, 65536, v39\n"
"ds_read_b128 v[172:175], v36 offset:0\n"
"v_mfma_f32_32x32x16_bf16 v[64:79], v[56:59], v[176:179], v[64:79]\n"
"v_add_u32 v34, 65536, v34\n"
"v_add_u32 v35, 65536, v35\n"
"ds_read_b64_tr_b16 v[52:53], v34 offset:0\n"
"ds_read_b64_tr_b16 v[54:55], v34 offset:1024\n"
"v_mfma_f32_32x32x16_bf16 v[80:95], v[60:63], v[176:179], v[80:95]\n"
"s_add_u32 m0, s30, 32768\n"
"s_add_u32 s29, s29, 1\n"
"global_load_lds_dwordx4 v28, s[34:35] offset:0\n"
"s_setprio 2\n"
"ds_read_b64_tr_b16 v[56:57], v33 offset:0\n"
"ds_read_b64_tr_b16 v[58:59], v33 offset:1024\n"
"ds_read_b128 v[168:171], v37 offset:0\n"
"s_waitcnt lgkmcnt(5)\n"
"v_mfma_f32_32x32x16_bf16 v[64:79], v[48:51], v[172:175], v[64:79]\n"
"s_mov_b32 s44, m0\n"
"s_add_u32 m0, s33, 139264\n"
"s_nop 0\n"
"global_load_lds_dwordx4 v25, s[38:39] offset:0\n"
"s_mov_b32 m0, s44\n"
"ds_read_b64_tr_b16 v[60:61], v35 offset:0\n"
"ds_read_b64_tr_b16 v[62:63], v35 offset:1024\n"
"s_waitcnt lgkmcnt(5)\n"
"v_mfma_f32_32x32x16_bf16 v[80:95], v[52:55], v[172:175], v[80:95]\n"
"global_load_lds_dwordx4 v29, s[34:35] offset:1024\n"
"s_setprio 1\n"
"ds_read_b64_tr_b16 v[48:49], v32 offset:16384\n"
"ds_read_b64_tr_b16 v[50:51], v32 offset:17408\n"
"ds_read_b128 v[164:167], v38 offset:0\n"
"s_waitcnt lgkmcnt(5)\n"
"v_mfma_f32_32x32x16_bf16 v[64:79], v[56:59], v[168:171], v[64:79]\n"
"ds_read_b64_tr_b16 v[52:53], v34 offset:16384\n"
"ds_read_b64_tr_b16 v[54:55], v34 offset:17408\n"
"s_waitcnt lgkmcnt(5)\n"
"v_mfma_f32_32x32x16_bf16 v[80:95], v[60:63], v[168:171], v[80:95]\n"
"global_load_lds_dwordx4 v30, s[36:37] offset:2048\n"
"s_setprio 0\n"
"ds_read_b64_tr_b16 v[56:57], v33 offset:16384\n"
"ds_read_b64_tr_b16 v[58:59], v33 offset:17408\n"
"ds_read_b128 v[160:163], v39 offset:0\n"
"s_waitcnt lgkmcnt(5)\n"
"v_mfma_f32_32x32x16_bf16 v[64:79], v[48:51], v[164:167], v[64:79]\n"
"ds_read_b64_tr_b16 v[60:61], v35 offset:16384\n"
"ds_read_b64_tr_b16 v[62:63], v35 offset:17408\n"
"s_waitcnt lgkmcnt(5)\n"
"v_mfma_f32_32x32x16_bf16 v[80:95], v[52:55], v[164:167], v[80:95]\n"
"global_load_lds_dwordx4 v31, s[36:37] offset:3072\n"
"s_mov_b32 s40, 128\n"
"s_mov_b32 s41, 245760\n"
"s_cmp_eq_u32 s29, 10\n"
"s_cmov_b32 s40, -1152\n"
"s_cmov_b32 s41, -2211840\n"
"s_cselect_b32 s42, -1, 0\n"
"s_add_u32 s34, s34, s40\n"
"s_addc_u32 s35, s35, s42\n"
"s_add_u32 s36, s36, s41\n"
"s_addc_u32 s37, s37, s42\n"
"s_waitcnt vmcnt(5)\n"
"s_waitcnt lgkmcnt(0)\n"
"s_barrier\n"
"s_setprio 3\n"
"v_subrev_u32 v32, 65536, v32\n"
"v_subrev_u32 v33, 65536, v33\n"
"ds_read_b64_tr_b16 v[48:49], v32 offset:0\n"
"ds_read_b64_tr_b16 v[50:51], v32 offset:1024\n"
"v_subrev_u32 v36, 65536, v36\n"
"v_subrev_u32 v37, 65536, v37\n"
"v_subrev_u32 v38, 65536, v38\n"
"v_subrev_u32 v39, 65536, v39\n"
"ds_read_b128 v[156:159], v36 offset:0\n"
"v_mfma_f32_32x32x16_bf16 v[64:79], v[56:59], v[160:163], v[64:79]\n"
"v_subrev_u32 v34, 65536, v34\n"
"v_subrev_u32 v35, 65536, v35\n"
"ds_read_b64_tr_b16 v[52:53], v34 offset:0\n"
"ds_read_b64_tr_b16 v[54:55], v34 offset:1024\n"
"v_mfma_f32_32x32x16_bf16 v[80:95], v[60:63], v[160:163], v[80:95]\n"
"s_add_u32 m0, s30, 65536\n"
"s_add_u32 s29, s29, 1\n"
"global_load_lds_dwordx4 v28, s[34:35] offset:0\n"
"s_setprio 2\n"
"ds_read_b64_tr_b16 v[56:57], v33 offset:0\n"
"ds_read_b64_tr_b16 v[58:59], v33 offset:1024\n"
"ds_read_b128 v[152:155], v37 offset:0\n"
"s_waitcnt lgkmcnt(5)\n"
"v_mfma_f32_32x32x16_bf16 v[64:79], v[48:51], v[156:159], v[64:79]\n"
"ds_read_b64_tr_b16 v[60:61], v35 offset:0\n"
"ds_read_b64_tr_b16 v[62:63], v35 offset:1024\n"
"s_waitcnt lgkmcnt(5)\n"
"v_mfma_f32_32x32x16_bf16 v[80:95], v[52:55], v[156:159], v[80:95]\n"
"global_load_lds_dwordx4 v29, s[34:35] offset:1024\n"
"s_setprio 1\n"
"ds_read_b64_tr_b16 v[48:49], v32 offset:16384\n"
"ds_read_b64_tr_b16 v[50:51], v32 offset:17408\n"
"ds_read_b128 v[148:151], v38 offset:0\n"
"s_waitcnt lgkmcnt(5)\n"
"v_mfma_f32_32x32x16_bf16 v[64:79], v[56:59], v[152:155], v[64:79]\n"
"ds_read_b64_tr_b16 v[52:53], v34 offset:16384\n"
"ds_read_b64_tr_b16 v[54:55], v34 offset:17408\n"
"s_waitcnt lgkmcnt(5)\n"
"v_mfma_f32_32x32x16_bf16 v[80:95], v[60:63], v[152:155], v[80:95]\n"
"global_load_lds_dwordx4 v30, s[36:37] offset:2048\n"
"s_setprio 0\n"
"s_sub_u32 s40, 4, s46\n"
"s_lshl_b32 s40, s40, 10\n"
"v_mul_u32_u24 v3, s40, v2\n"
"s_lshl_b32 s40, s46, 10\n"
"s_mul_i32 s41, s26, 10\n"
"s_add_u32 s41, s41, s47\n"
"s_lshl_b32 s41, s41, 9\n"
"s_add_u32 s41, s40, s41\n"
"s_add_u32 s41, s41, 16842752\n"
"v_lshl_add_u32 v4, v1, 4, s41\n"
"v_add_u32 v41, v3, v4\n"
"ds_read_b64_tr_b16 v[56:57], v33 offset:16384\n"
"ds_read_b64_tr_b16 v[58:59], v33 offset:17408\n"
"ds_read_b128 v[144:147], v39 offset:0\n"
"s_waitcnt lgkmcnt(5)\n"
"v_mfma_f32_32x32x16_bf16 v[64:79], v[48:51], v[148:151], v[64:79]\n"
"ds_read_b64_tr_b16 v[60:61], v35 offset:16384\n"
"ds_read_b64_tr_b16 v[62:63], v35 offset:17408\n"
"s_waitcnt lgkmcnt(5)\n"
"v_mfma_f32_32x32x16_bf16 v[80:95], v[52:55], v[148:151], v[80:95]\n"
"global_load_lds_dwordx4 v31, s[36:37] offset:3072\n"
"s_add_u32 s34, s34, 128\n"
"s_addc_u32 s35, s35, 0\n"
"s_add_u32 s36, s36, 245760\n"
"s_addc_u32 s37, s37, 0\n"
"global_load_dwordx4 v[44:47], v41, s[18:19] sc1\n"
"s_waitcnt vmcnt(5)\n"
"s_waitcnt lgkmcnt(0)\n"
"s_barrier\n"
"s_setprio 3\n"
"ds_read_b64_tr_b16 v[48:49], v32 offset:32768\n"
"ds_read_b64_tr_b16 v[50:51], v32 offset:33792\n"
"ds_read_b128 v[140:143], v36 offset:32768\n"
"v_mfma_f32_32x32x16_bf16 v[64:79], v[56:59], v[144:147], v[64:79]\n"
"ds_read_b64_tr_b16 v[52:53], v34 offset:32768\n"
"ds_read_b64_tr_b16 v[54:55], v34 offset:33792\n"
"v_mfma_f32_32x32x16_bf16 v[80:95], v[60:63], v[144:147], v[80:95]\n"
"s_add_u32 m0, s30, 0\n"
"s_add_u32 s29, s29, 1\n"
"global_load_lds_dwordx4 v28, s[34:35] offset:0\n"
"s_setprio 2\n"
"ds_read_b64_tr_b16 v[56:57], v33 offset:32768\n"
"ds_read_b64_tr_b16 v[58:59], v33 offset:33792\n"
"ds_read_b128 v[136:139], v37 offset:32768\n"
"s_waitcnt lgkmcnt(5)\n"
"v_mfma_f32_32x32x16_bf16 v[64:79], v[48:51], v[140:143], v[64:79]\n"
"ds_read_b64_tr_b16 v[60:61], v35 offset:32768\n"
"ds_read_b64_tr_b16 v[62:63], v35 offset:33792\n"
"s_waitcnt lgkmcnt(5)\n"
"v_mfma_f32_32x32x16_bf16 v[80:95], v[52:55], v[140:143], v[80:95]\n"
"global_load_lds_dwordx4 v29, s[34:35] offset:1024\n"
"s_setprio 1\n"
"ds_read_b64_tr_b16 v[48:49], v32 offset:49152\n"
"ds_read_b64_tr_b16 v[50:51], v32 offset:50176\n"
"ds_read_b128 v[132:135], v38 offset:32768\n"
"s_waitcnt lgkmcnt(5)\n"
"v_mfma_f32_32x32x16_bf16 v[64:79], v[56:59], v[136:139], v[64:79]\n"
"ds_read_b64_tr_b16 v[52:53], v34 offset:49152\n"
"ds_read_b64_tr_b16 v[54:55], v34 offset:50176\n"
"s_waitcnt lgkmcnt(5)\n"
"v_mfma_f32_32x32x16_bf16 v[80:95], v[60:63], v[136:139], v[80:95]\n"
"global_load_lds_dwordx4 v30, s[36:37] offset:2048\n"
"s_setprio 0\n"
"s_waitcnt vmcnt(3)\n"
"v_cmp_ne_u32 vcc, s24, v45\n"
"v_cmp_ne_u32 s[42:43], s25, v47\n"
"s_nop 0\n"
"s_or_b64 vcc, vcc, s[42:43]\n"
"s_cmp_eq_u64 vcc, 0\n"
"s_cbranch_scc0 retry_19_%=\n"
"ok_20_%=:\n"
"ds_write2st64_b32 v10, v44, v46 offset0:0 offset1:8\n"
"v_xor_b32 v4, s46, v2\n"
"v_and_b32 v4, 1, v4\n"
"s_lshl_b32 s41, s47, 10\n"
"s_add_u32 s41, s41, 110592\n"
"v_lshl_add_u32 v3, v1, 2, s41\n"
"v_lshl_add_u32 v45, v4, 11, v3\n"
"ds_read_b64_tr_b16 v[56:57], v33 offset:49152\n"
"ds_read_b64_tr_b16 v[58:59], v33 offset:50176\n"
"ds_read_b128 v[128:131], v39 offset:32768\n"
"s_waitcnt lgkmcnt(6)\n"
"v_mfma_f32_32x32x16_bf16 v[64:79], v[48:51], v[132:135], v[64:79]\n"
"ds_read_b64_tr_b16 v[60:61], v35 offset:49152\n"
"ds_read_b64_tr_b16 v[62:63], v35 offset:50176\n"
"s_waitcnt lgkmcnt(6)\n"
"v_mfma_f32_32x32x16_bf16 v[80:95], v[52:55], v[132:135], v[80:95]\n"
"global_load_lds_dwordx4 v31, s[36:37] offset:3072\n"
"global_load_dwordx4 v[20:23], v13, s[18:19] sc1\n"
"s_waitcnt vmcnt(6)\n"
"s_waitcnt lgkmcnt(0)\n"
"s_barrier\n"
"ds_read2st64_b32 v[40:41], v45 offset0:0 offset1:1\n"
"ds_read2st64_b32 v[42:43], v45 offset0:2 offset1:3\n"
"ds_read_b32 v44, v45 offset:128\n"
"s_setprio 3\n"
"v_add_u32 v32, 65536, v32\n"
"v_add_u32 v33, 65536, v33\n"
"ds_read_b64_tr_b16 v[48:49], v32 offset:0\n"
"ds_read_b64_tr_b16 v[50:51], v32 offset:1024\n"
"v_add_u32 v36, 65536, v36\n"
"v_add_u32 v37, 65536, v37\n"
"v_add_u32 v38, 65536, v38\n"
"v_add_u32 v39, 65536, v39\n"
"ds_read_b128 v[124:127], v36 offset:0\n"
"v_mfma_f32_32x32x16_bf16 v[64:79], v[56:59], v[128:131], v[64:79]\n"
"s_add_u32 s34, s18, s55\n"
"s_addc_u32 s35, s19, 0\n"
"s_add_u32 s36, s18, s56\n"
"s_addc_u32 s37, s19, 0\n"
"ds_read_b64_tr_b16 v[52:53], v33 offset:0\n"
"ds_read_b64_tr_b16 v[54:55], v33 offset:1024\n"
"ds_read_b128 v[120:123], v37 offset:0\n"
"v_mfma_f32_32x32x16_bf16 v[80:95], v[60:63], v[128:131], v[80:95]\n"
"ds_read_b64_tr_b16 v[56:57], v32 offset:16384\n"
"ds_read_b64_tr_b16 v[58:59], v32 offset:17408\n"
"ds_read_b128 v[116:119], v38 offset:0\n"
"s_add_u32 m0, s33, 139264\n"
"s_nop 0\n"
"global_load_lds_dwordx4 v26, s[38:39] offset:1024\n"
"v_mov_b32 v4, 0\n"
"ds_write_b32 v4, v4 offset:61440\n"
"s_setprio 2\n"
"s_waitcnt lgkmcnt(7)\n"
"v_mfma_f32_32x32x16_bf16 v[64:79], v[48:51], v[124:127], v[64:79]\n"
"ds_read_b64_tr_b16 v[60:61], v33 offset:16384\n"
"ds_read_b64_tr_b16 v[62:63], v33 offset:17408\n"
"ds_read_b128 v[112:115], v39 offset:0\n"
"s_add_u32 m0, s33, 139264\n"
"s_nop 0\n"
"global_load_lds_dwordx4 v27, s[38:39] offset:2048\n"
"s_cmp_eq_u32 s28, 3\n"
"s_cselect_b32 s45, 1966080, 0\n"
"s_add_u32 s38, s38, 393216\n"
"s_addc_u32 s39, s39, 0\n"
"s_sub_u32 s38, s38, s45\n"
"s_subb_u32 s39, s39, 0\n"
"s_waitcnt vmcnt(3)\n"
"s_barrier\n"
"s_waitcnt lgkmcnt(7)\n"
"v_mfma_f32_32x32x16_bf16 v[64:79], v[52:55], v[120:123], v[64:79]\n"
"v_subrev_u32 v32, 65536, v32\n"
"v_subrev_u32 v33, 65536, v33\n"
"ds_read_b64_tr_b16 v[48:49], v32 offset:0\n"
"ds_read_b64_tr_b16 v[50:51], v32 offset:1024\n"
"v_subrev_u32 v36, 65536, v36\n"
"v_subrev_u32 v37, 65536, v37\n"
"v_subrev_u32 v38, 65536, v38\n"
"v_subrev_u32 v39, 65536, v39\n"
"ds_read_b128 v[108:111], v36 offset:0\n"
"s_add_u32 m0, s33, 32768\n"
"s_nop 0\n"
"global_load_lds_dwordx4 v25, s[38:39] offset:0\n"
"v_add_f32 v40, v40, v41\n"
"v_add_f32 v42, v42, v43\n"
"v_add_f32 v40, v40, v44\n"
"v_add_f32 v40, v40, v42\n"
"v_and_b32 v41, 0xffff0000, v40\n"
"v_sub_f32 v40, v40, v41\n"
"v_cvt_pk_bf16_f32 v10, v41, v41\n"
"v_cvt_pk_bf16_f32 v11, v40, v40\n"
"s_waitcnt lgkmcnt(7)\n"
"v_mfma_f32_32x32x16_bf16 v[64:79], v[56:59], v[116:119], v[64:79]\n"
"ds_read_b64_tr_b16 v[52:53], v33 offset:0\n"
"ds_read_b64_tr_b16 v[54:55], v33 offset:1024\n"
"ds_read_b128 v[104:107], v37 offset:0\n"
"s_add_u32 m0, s33, 32768\n"
"s_nop 0\n"
"global_load_lds_dwordx4 v26, s[38:39] offset:1024\n"
"s_setprio 1\n"
"s_waitcnt lgkmcnt(6)\n"
"v_mfma_f32_32x32x16_bf16 v[64:79], v[60:63], v[112:115], v[64:79]\n"
"ds_read_b64_tr_b16 v[56:57], v32 offset:16384\n"
"ds_read_b64_tr_b16 v[58:59], v32 offset:17408\n"
"ds_read_b128 v[100:103], v38 offset:0\n"
"s_add_u32 m0, s33, 32768\n"
"s_nop 0\n"
"global_load_lds_dwordx4 v27, s[38:39] offset:2048\n"
"s_cmp_eq_u32 s28, 2\n"
"s_cselect_b32 s45, 1966080, 0\n"
"s_add_u32 s38, s38, 393216\n"
"s_addc_u32 s39, s39, 0\n"
"s_sub_u32 s38, s38, s45\n"
"s_subb_u32 s39, s39, 0\n"
"s_waitcnt lgkmcnt(6)\n"
"v_mfma_f32_32x32x16_bf16 v[64:79], v[48:51], v[108:111], v[64:79]\n"
"ds_read_b64_tr_b16 v[60:61], v33 offset:16384\n"
"ds_read_b64_tr_b16 v[62:63], v33 offset:17408\n"
"ds_read_b128 v[96:99], v39 offset:0\n"
"s_waitcnt lgkmcnt(6)\n"
"v_mfma_f32_32x32x16_bf16 v[64:79], v[52:55], v[104:107], v[64:79]\n"
"v_add_u32 v34, 65536, v34\n"
"v_add_u32 v35, 65536, v35\n"
"ds_read_b64_tr_b16 v[48:49], v34 offset:0\n"
"ds_read_b64_tr_b16 v[50:51], v34 offset:1024\n"
"s_lshl_b32 s45, s23, 10\n"
"s_add_u32 s40, s98, s45\n"
"s_addc_u32 s41, s99, 0\n"
"s_add_u32 m0, s45, 98304\n"
"s_nop 0\n"
"global_load_lds_dwordx4 v24, s[40:41]\n"
"novt_21_%=:\n"
"s_waitcnt vmcnt(6)\n"
"v_cmp_ne_u32 vcc, s24, v21\n"
"v_cmp_ne_u32 s[42:43], s25, v23\n"
"s_nop 0\n"
"s_or_b64 vcc, vcc, s[42:43]\n"
"s_cmp_eq_u64 vcc, 0\n"
"s_cbranch_scc0 retry_22_%=\n"
"ok_23_%=:\n"
"v_xor_b32 v3, 0x80000000, v20\n"
"v_and_b32 v4, 0xffff0000, v3\n"
"v_sub_f32 v5, v3, v4\n"
"v_cmp_eq_u32 vcc, 0, v2\n"
"v_cvt_pk_bf16_f32 v5, v4, v5\n"
"s_nop 0\n"
"v_cndmask_b32 v18, 0, v5, vcc\n"
"v_cndmask_b32 v19, 0, v5, vcc\n"
"v_cndmask_b32 v14, v5, 0, vcc\n"
"v_cndmask_b32 v15, v5, 0, vcc\n"
"v_mul_f32 v3, s75, v22\n"
"s_setprio 0\n"
"s_waitcnt lgkmcnt(5)\n"
"v_mfma_f32_32x32x16_bf16 v[64:79], v[56:59], v[100:103], v[64:79]\n"
"ds_read_b64_tr_b16 v[52:53], v35 offset:0\n"
"ds_read_b64_tr_b16 v[54:55], v35 offset:1024\n"
"v_mfma_f32_32x32x8_bf16 v[64:79], v[10:11], v[18:19], v[64:79]\n"
"s_waitcnt lgkmcnt(4)\n"
"v_mfma_f32_32x32x16_bf16 v[64:79], v[60:63], v[96:99], v[64:79]\n"
"ds_read_b64_tr_b16 v[56:57], v34 offset:16384\n"
"ds_read_b64_tr_b16 v[58:59], v34 offset:17408\n"
"s_cmp_eq_u32 s47, 0\n"
"s_cbranch_scc1 novt_24_%=\n"
"s_lshl_b32 s41, s23, 10\n"
"s_add_u32 s45, s41, 4096\n"
"s_add_u32 s40, s98, s45\n"
"s_addc_u32 s41, s99, 0\n"
"s_add_u32 m0, s45, 98304\n"
"s_nop 0\n"
"global_load_lds_dwordx4 v24, s[40:41]\n"
"novt_24_%=:\n"
"s_waitcnt lgkmcnt(4)\n"
"v_mfma_f32_32x32x16_bf16 v[80:95], v[48:51], v[124:127], v[80:95]\n"
"ds_read_b64_tr_b16 v[60:61], v35 offset:16384\n"
"ds_read_b64_tr_b16 v[62:63], v35 offset:17408\n"
"v_mfma_f32_32x32x8_bf16 v[80:95], v[10:11], v[14:15], v[80:95]\n"
"s_waitcnt lgkmcnt(4)\n"
"v_mfma_f32_32x32x16_bf16 v[80:95], v[52:55], v[120:123], v[80:95]\n"
"v_subrev_u32 v34, 65536, v34\n"
"v_subrev_u32 v35, 65536, v35\n"
"ds_read_b64_tr_b16 v[48:49], v34 offset:0\n"
"ds_read_b64_tr_b16 v[50:51], v34 offset:1024\n"
"s_waitcnt lgkmcnt(4)\n"
"v_mfma_f32_32x32x16_bf16 v[80:95], v[56:59], v[116:119], v[80:95]\n"
"ds_read_b64_tr_b16 v[52:53], v35 offset:0\n"
"ds_read_b64_tr_b16 v[54:55], v35 offset:1024\n"
"v_mul_f32 v64, v64, v3\n"
"v_mul_f32 v65, v65, v3\n"
"v_mul_f32 v66, v66, v3\n"
"v_mul_f32 v67, v67, v3\n"
"v_mul_f32 v68, v68, v3\n"
"v_mul_f32 v69, v69, v3\n"
"s_waitcnt lgkmcnt(4)\n"
"v_mfma_f32_32x32x16_bf16 v[80:95], v[60:63], v[112:115], v[80:95]\n"
"ds_read_b64_tr_b16 v[56:57], v34 offset:16384\n"
"ds_read_b64_tr_b16 v[58:59], v34 offset:17408\n"
"v_mul_f32 v70, v70, v3\n"
"v_mul_f32 v71, v71, v3\n"
"v_mul_f32 v72, v72, v3\n"
"v_mul_f32 v73, v73, v3\n"
"v_mul_f32 v74, v74, v3\n"
"v_mul_f32 v75, v75, v3\n"
"s_waitcnt lgkmcnt(4)\n"
"v_mfma_f32_32x32x16_bf16 v[80:95], v[48:51], v[108:111], v[80:95]\n"
"ds_read_b64_tr_b16 v[60:61], v35 offset:16384\n"
"ds_read_b64_tr_b16 v[62:63], v35 offset:17408\n"
"v_mul_f32 v76, v76, v3\n"
"v_mul_f32 v77, v77, v3\n"
"v_mul_f32 v78, v78, v3\n"
"v_mul_f32 v79, v79, v3\n"
"v_cvt_pk_bf16_f32 v64, v64, v65\n"
"v_cvt_pk_bf16_f32 v65, v66, v67\n"
"s_waitcnt lgkmcnt(4)\n"
"v_mfma_f32_32x32x16_bf16 v[80:95], v[52:55], v[104:107], v[80:95]\n"
"v_cvt_pk_bf16_f32 v66, v68, v69\n"
"v_cvt_pk_bf16_f32 v67, v70, v71\n"
"v_cvt_pk_bf16_f32 v68, v72, v73\n"
"v_cvt_pk_bf16_f32 v69, v74, v75\n"
"v_cvt_pk_bf16_f32 v70, v76, v77\n"
"v_cvt_pk_bf16_f32 v71, v78, v79\n"
"s_waitcnt lgkmcnt(2)\n"
"v_mfma_f32_32x32x16_bf16 v[80:95], v[56:59], v[100:103], v[80:95]\n"
"v_permlane32_swap_b32 v64, v66\n"
"v_permlane32_swap_b32 v65, v67\n"
"v_permlane32_swap_b32 v68, v70\n"
"v_permlane32_swap_b32 v69, v71\n"
"global_store_dwordx4 v17, v[64:67], s[34:35] sc1\n"
"s_add_u32 s34, s34, 65536\n"
"s_addc_u32 s35, s35, 0\n"
"global_store_dwordx4 v17, v[68:71], s[34:35] sc1\n"
"s_waitcnt lgkmcnt(0)\n"
"v_mfma_f32_32x32x16_bf16 v[80:95], v[60:63], v[96:99], v[80:95]\n"
"s_nop 11\n"
"v_mul_f32 v80, v80, v3\n"
"v_mul_f32 v81, v81, v3\n"
"v_mul_f32 v82, v82, v3\n"
"v_mul_f32 v83, v83, v3\n"
"v_mul_f32 v84, v84, v3\n"
"v_mul_f32 v85, v85, v3\n"
"v_mul_f32 v86, v86, v3\n"
"v_mul_f32 v87, v87, v3\n"
"v_mul_f32 v88, v88, v3\n"
"v_mul_f32 v89, v89, v3\n"
"v_mul_f32 v90, v90, v3\n"
"v_mul_f32 v91, v91, v3\n"
"v_mul_f32 v92, v92, v3\n"
"v_mul_f32 v93, v93, v3\n"
"v_mul_f32 v94, v94, v3\n"
"v_mul_f32 v95, v95, v3\n"
"v_cvt_pk_bf16_f32 v80, v80, v81\n"
"v_cvt_pk_bf16_f32 v81, v82, v83\n"
"v_cvt_pk_bf16_f32 v82, v84, v85\n"
"v_cvt_pk_bf16_f32 v83, v86, v87\n"
"v_cvt_pk_bf16_f32 v84, v88, v89\n"
"v_cvt_pk_bf16_f32 v85, v90, v91\n"
"v_cvt_pk_bf16_f32 v86, v92, v93\n"
"v_cvt_pk_bf16_f32 v87, v94, v95\n"
"v_permlane32_swap_b32 v80, v82\n"
"v_permlane32_swap_b32 v81, v83\n"
"v_permlane32_swap_b32 v84, v86\n"
"v_permlane32_swap_b32 v85, v87\n"
"global_store_dwordx4 v17, v[80:83], s[36:37] sc1\n"
"s_add_u32 s36, s36, 65536\n"
"s_addc_u32 s37, s37, 0\n"
"global_store_dwordx4 v17, v[84:87], s[36:37] sc1\n"
"s_branch p2_5_%=\n"
"stat_3_%=:\n"
"v_mov_b32 v64, 0x3f803f80\n"
"v_mov_b32 v65, 0x3f803f80\n"
"v_mov_b32 v48, 0\n"
"v_mov_b32 v49, 0\n"
"v_mov_b32 v50, 0\n"
"v_mov_b32 v51, 0\n"
"v_mov_b32 v52, 0\n"
"v_mov_b32 v53, 0\n"
"v_mov_b32 v54, 0\n"
"v_mov_b32 v55, 0\n"
"v_mov_b32 v56, 0\n"
"v_mov_b32 v57, 0\n"
"v_mov_b32 v58, 0\n"
"v_mov_b32 v59, 0\n"
"v_mov_b32 v60, 0\n"
"v_mov_b32 v61, 0\n"
"v_mov_b32 v62, 0\n"
"v_mov_b32 v63, 0\n"
"global_load_lds_dwordx4 v29, s[34:35] offset:1024\n"
"global_load_lds_dwordx4 v28, s[36:37] offset:2048\n"
"global_load_lds_dwordx4 v29, s[36:37] offset:3072\n"
"s_mov_b32 s40, 256\n"
"s_mov_b32 s41, 256\n"
"s_cmp_eq_u32 s29, 5\n"
"s_cmov_b32 s40, -1024\n"
"s_cmov_b32 s41, -1024\n"
"s_cselect_b32 s42, -1, 0\n"
"s_add_u32 s34, s34, s40\n"
"s_addc_u32 s35, s35, s42\n"
"s_add_u32 s36, s36, s41\n"
"s_addc_u32 s37, s37, s42\n"
"s_waitcnt lgkmcnt(0)\n"
"s_mul_i32 s24, s24, 1664525\n"
"s_add_u32 s24, s24, 1013904223\n"
"s_mul_i32 s25, s25, 22695477\n"
"s_add_u32 s25, s25, 1\n"
"s_mov_b32 s31, 100000\n"
"s_add_u32 s38, s14, s76\n"
"s_addc_u32 s39, s15, 0\n"
"s_add_u32 s8, s8, s79\n"
"s_addc_u32 s9, s9, 0\n"
"s_add_u32 s20, s20, s79\n"
"s_addc_u32 s21, s21, 0\n"
"s_waitcnt vmcnt(6)\n"
"s_barrier\n"
"ds_read_b128 v[252:255], v36 offset:0\n"
"ds_read_b128 v[248:251], v37 offset:0\n"
"ds_read_b128 v[244:247], v38 offset:0\n"
"ds_read_b128 v[240:243], v39 offset:0\n"
"ds_read_b128 v[236:239], v36 offset:2048\n"
"ds_read_b128 v[232:235], v37 offset:2048\n"
"ds_read_b128 v[228:231], v38 offset:2048\n"
"ds_read_b128 v[224:227], v39 offset:2048\n"
"s_add_u32 m0, s30, 65536\n"
"s_add_u32 s29, s29, 1\n"
"global_load_lds_dwordx4 v28, s[34:35] offset:0\n"
"global_load_lds_dwordx4 v29, s[34:35] offset:1024\n"
"global_load_lds_dwordx4 v28, s[36:37] offset:2048\n"
"global_load_lds_dwordx4 v29, s[36:37] offset:3072\n"
"s_mov_b32 s40, 256\n"
"s_mov_b32 s41, 256\n"
"s_cmp_eq_u32 s29, 5\n"
"s_cmov_b32 s40, -1024\n"
"s_cmov_b32 s41, -1024\n"
"s_cselect_b32 s42, -1, 0\n"
"s_add_u32 s34, s34, s40\n"
"s_addc_u32 s35, s35, s42\n"
"s_add_u32 s36, s36, s41\n"
"s_addc_u32 s37, s37, s42\n"
"s_waitcnt lgkmcnt(0)\n"
"s_cmp_eq_u32 s47, 1\n"
"s_cbranch_scc1 nost_25_%=\n"
"v_mfma_f32_4x4x4_16b_bf16 v[48:51], v[64:65], v[252:253], v[48:51]\n"
"v_mfma_f32_4x4x4_16b_bf16 v[52:55], v[64:65], v[254:255], v[52:55]\n"
"v_mfma_f32_4x4x4_16b_bf16 v[56:59], v[252:253], v[252:253], v[56:59]\n"
"v_mfma_f32_4x4x4_16b_bf16 v[60:63], v[254:255], v[254:255], v[60:63]\n"
"v_mfma_f32_4x4x4_16b_bf16 v[48:51], v[64:65], v[248:249], v[48:51]\n"
"v_mfma_f32_4x4x4_16b_bf16 v[52:55], v[64:65], v[250:251], v[52:55]\n"
"v_mfma_f32_4x4x4_16b_bf16 v[56:59], v[248:249], v[248:249], v[56:59]\n"
"v_mfma_f32_4x4x4_16b_bf16 v[60:63], v[250:251], v[250:251], v[60:63]\n"
"v_mfma_f32_4x4x4_16b_bf16 v[48:51], v[64:65], v[244:245], v[48:51]\n"
"v_mfma_f32_4x4x4_16b_bf16 v[52:55], v[64:65], v[246:247], v[52:55]\n"
"v_mfma_f32_4x4x4_16b_bf16 v[56:59], v[244:245], v[244:245], v[56:59]\n"
"v_mfma_f32_4x4x4_16b_bf16 v[60:63], v[246:247], v[246:247], v[60:63]\n"
"v_mfma_f32_4x4x4_16b_bf16 v[48:51], v[64:65], v[240:241], v[48:51]\n"
"v_mfma_f32_4x4x4_16b_bf16 v[52:55], v[64:65], v[242:243], v[52:55]\n"
"v_mfma_f32_4x4x4_16b_bf16 v[56:59], v[240:241], v[240:241], v[56:59]\n"
"v_mfma_f32_4x4x4_16b_bf16 v[60:63], v[242:243], v[242:243], v[60:63]\n"
"v_mfma_f32_4x4x4_16b_bf16 v[48:51], v[64:65], v[236:237], v[48:51]\n"
"v_mfma_f32_4x4x4_16b_bf16 v[52:55], v[64:65], v[238:239], v[52:55]\n"
"v_mfma_f32_4x4x4_16b_bf16 v[56:59], v[236:237], v[236:237], v[56:59]\n"
"v_mfma_f32_4x4x4_16b_bf16 v[60:63], v[238:239], v[238:239], v[60:63]\n"
"v_mfma_f32_4x4x4_16b_bf16 v[48:51], v[64:65], v[232:233], v[48:51]\n"
"v_mfma_f32_4x4x4_16b_bf16 v[52:55], v[64:65], v[234:235], v[52:55]\n"
"v_mfma_f32_4x4x4_16b_bf16 v[56:59], v[232:233], v[232:233], v[56:59]\n"
"v_mfma_f32_4x4x4_16b_bf16 v[60:63], v[234:235], v[234:235], v[60:63]\n"
"v_mfma_f32_4x4x4_16b_bf16 v[48:51], v[64:65], v[228:229], v[48:51]\n"
"v_mfma_f32_4x4x4_16b_bf16 v[52:55], v[64:65], v[230:231], v[52:55]\n"
"v_mfma_f32_4x4x4_16b_bf16 v[56:59], v[228:229], v[228:229], v[56:59]\n"
"v_mfma_f32_4x4x4_16b_bf16 v[60:63], v[230:231], v[230:231], v[60:63]\n"
"v_mfma_f32_4x4x4_16b_bf16 v[48:51], v[64:65], v[224:225], v[48:51]\n"
"v_mfma_f32_4x4x4_16b_bf16 v[52:55], v[64:65], v[226:227], v[52:55]\n"
"v_mfma_f32_4x4x4_16b_bf16 v[56:59], v[224:225], v[224:225], v[56:59]\n"
"v_mfma_f32_4x4x4_16b_bf16 v[60:63], v[226:227], v[226:227], v[60:63]\n"
"nost_25_%=:\n"
"s_waitcnt vmcnt(4)\n"
"s_barrier\n"
"ds_read_b128 v[220:223], v36 offset:32768\n"
"ds_read_b128 v[216:219], v37 offset:32768\n"
"ds_read_b128 v[212:215], v38 offset:32768\n"
"ds_read_b128 v[208:211], v39 offset:32768\n"
"ds_read_b128 v[204:207], v36 offset:34816\n"
"ds_read_b128 v[200:203], v37 offset:34816\n"
"ds_read_b128 v[196:199], v38 offset:34816\n"
"ds_read_b128 v[192:195], v39 offset:34816\n"
"s_add_u32 m0, s30, 0\n"
"s_add_u32 s29, s29, 1\n"
"global_load_lds_dwordx4 v28, s[34:35] offset:0\n"
"global_load_lds_dwordx4 v29, s[34:35] offset:1024\n"
"global_load_lds_dwordx4 v28, s[36:37] offset:2048\n"
"global_load_lds_dwordx4 v29, s[36:37] offset:3072\n"
"s_mov_b32 s40, 256\n"
"s_mov_b32 s41, 256\n"
"s_cmp_eq_u32 s29, 5\n"
"s_cmov_b32 s40, -1024\n"
"s_cmov_b32 s41, -1024\n"
"s_cselect_b32 s42, -1, 0\n"
"s_add_u32 s34, s34, s40\n"
"s_addc_u32 s35, s35, s42\n"
"s_add_u32 s36, s36, s41\n"
"s_addc_u32 s37, s37, s42\n"
"s_waitcnt lgkmcnt(0)\n"
"s_cmp_eq_u32 s47, 1\n"
"s_cbranch_scc1 nost_26_%=\n"
"v_mfma_f32_4x4x4_16b_bf16 v[48:51], v[64:65], v[220:221], v[48:51]\n"
"v_mfma_f32_4x4x4_16b_bf16 v[52:55], v[64:65], v[222:223], v[52:55]\n"
"v_mfma_f32_4x4x4_16b_bf16 v[56:59], v[220:221], v[220:221], v[56:59]\n"
"v_mfma_f32_4x4x4_16b_bf16 v[60:63], v[222:223], v[222:223], v[60:63]\n"
"v_mfma_f32_4x4x4_16b_bf16 v[48:51], v[64:65], v[216:217], v[48:51]\n"
"v_mfma_f32_4x4x4_16b_bf16 v[52:55], v[64:65], v[218:219], v[52:55]\n"
"v_mfma_f32_4x4x4_16b_bf16 v[56:59], v[216:217], v[216:217], v[56:59]\n"
"v_mfma_f32_4x4x4_16b_bf16 v[60:63], v[218:219], v[218:219], v[60:63]\n"
"v_mfma_f32_4x4x4_16b_bf16 v[48:51], v[64:65], v[212:213], v[48:51]\n"
"v_mfma_f32_4x4x4_16b_bf16 v[52:55], v[64:65], v[214:215], v[52:55]\n"
"v_mfma_f32_4x4x4_16b_bf16 v[56:59], v[212:213], v[212:213], v[56:59]\n"
"v_mfma_f32_4x4x4_16b_bf16 v[60:63], v[214:215], v[214:215], v[60:63]\n"
"v_mfma_f32_4x4x4_16b_bf16 v[48:51], v[64:65], v[208:209], v[48:51]\n"
"v_mfma_f32_4x4x4_16b_bf16 v[52:55], v[64:65], v[210:211], v[52:55]\n"
"v_mfma_f32_4x4x4_16b_bf16 v[56:59], v[208:209], v[208:209], v[56:59]\n"
"v_mfma_f32_4x4x4_16b_bf16 v[60:63], v[210:211], v[210:211], v[60:63]\n"
"v_mfma_f32_4x4x4_16b_bf16 v[48:51], v[64:65], v[204:205], v[48:51]\n"
"v_mfma_f32_4x4x4_16b_bf16 v[52:55], v[64:65], v[206:207], v[52:55]\n"
"v_mfma_f32_4x4x4_16b_bf16 v[56:59], v[204:205], v[204:205], v[56:59]\n"
"v_mfma_f32_4x4x4_16b_bf16 v[60:63], v[206:207], v[206:207], v[60:63]\n"
"v_mfma_f32_4x4x4_16b_bf16 v[48:51], v[64:65], v[200:201], v[48:51]\n"
"v_mfma_f32_4x4x4_16b_bf16 v[52:55], v[64:65], v[202:203], v[52:55]\n"
"v_mfma_f32_4x4x4_16b_bf16 v[56:59], v[200:201], v[200:201], v[56:59]\n"
"v_mfma_f32_4x4x4_16b_bf16 v[60:63], v[202:203], v[202:203], v[60:63]\n"
"v_mfma_f32_4x4x4_16b_bf16 v[48:51], v[64:65], v[196:197], v[48:51]\n"
"v_mfma_f32_4x4x4_16b_bf16 v[52:55], v[64:65], v[198:199], v[52:55]\n"
"v_mfma_f32_4x4x4_16b_bf16 v[56:59], v[196:197], v[196:197], v[56:59]\n"
"v_mfma_f32_4x4x4_16b_bf16 v[60:63], v[198:199], v[198:199], v[60:63]\n"
"v_mfma_f32_4x4x4_16b_bf16 v[48:51], v[64:65], v[192:193], v[48:51]\n"
"v_mfma_f32_4x4x4_16b_bf16 v[52:55], v[64:65], v[194:195], v[52:55]\n"
"v_mfma_f32_4x4x4_16b_bf16 v[56:59], v[192:193], v[192:193], v[56:59]\n"
"v_mfma_f32_4x4x4_16b_bf16 v[60:63], v[194:195], v[194:195], v[60:63]\n"
"nost_26_%=:\n"
"s_waitcnt vmcnt(4)\n"
"s_barrier\n"
"v_add_u32 v36, 65536, v36\n"
"v_add_u32 v37, 65536, v37\n"
"v_add_u32 v38, 65536, v38\n"
"v_add_u32 v39, 65536, v39\n"
"ds_read_b128 v[188:191], v36 offset:0\n"
"ds_read_b128 v[184:187], v37 offset:0\n"
"ds_read_b128 v[180:183], v38 offset:0\n"
"ds_read_b128 v[176:179], v39 offset:0\n"
"ds_read_b128 v[172:175], v36 offset:2048\n"
"ds_read_b128 v[168:171], v37 offset:2048\n"
"ds_read_b128 v[164:167], v38 offset:2048\n"
"ds_read_b128 v[160:163], v39 offset:2048\n"
"s_add_u32 m0, s30, 32768\n"
"s_add_u32 s29, s29, 1\n"
"global_load_lds_dwordx4 v28, s[34:35] offset:0\n"
"global_load_lds_dwordx4 v29, s[34:35] offset:1024\n"
"global_load_lds_dwordx4 v28, s[36:37] offset:2048\n"
"global_load_lds_dwordx4 v29, s[36:37] offset:3072\n"
"s_waitcnt lgkmcnt(0)\n"
"s_cmp_eq_u32 s47, 1\n"
"s_cbranch_scc1 nost_27_%=\n"
"v_mfma_f32_4x4x4_16b_bf16 v[48:51], v[64:65], v[188:189], v[48:51]\n"
"v_mfma_f32_4x4x4_16b_bf16 v[52:55], v[64:65], v[190:191], v[52:55]\n"
"v_mfma_f32_4x4x4_16b_bf16 v[56:59], v[188:189], v[188:189], v[56:59]\n"
"v_mfma_f32_4x4x4_16b_bf16 v[60:63], v[190:191], v[190:191], v[60:63]\n"
"v_mfma_f32_4x4x4_16b_bf16 v[48:51], v[64:65], v[184:185], v[48:51]\n"
"v_mfma_f32_4x4x4_16b_bf16 v[52:55], v[64:65], v[186:187], v[52:55]\n"
"v_mfma_f32_4x4x4_16b_bf16 v[56:59], v[184:185], v[184:185], v[56:59]\n"
"v_mfma_f32_4x4x4_16b_bf16 v[60:63], v[186:187], v[186:187], v[60:63]\n"
"v_mfma_f32_4x4x4_16b_bf16 v[48:51], v[64:65], v[180:181], v[48:51]\n"
"v_mfma_f32_4x4x4_16b_bf16 v[52:55], v[64:65], v[182:183], v[52:55]\n"
"v_mfma_f32_4x4x4_16b_bf16 v[56:59], v[180:181], v[180:181], v[56:59]\n"
"v_mfma_f32_4x4x4_16b_bf16 v[60:63], v[182:183], v[182:183], v[60:63]\n"
"v_mfma_f32_4x4x4_16b_bf16 v[48:51], v[64:65], v[176:177], v[48:51]\n"
"v_mfma_f32_4x4x4_16b_bf16 v[52:55], v[64:65], v[178:179], v[52:55]\n"
"v_mfma_f32_4x4x4_16b_bf16 v[56:59], v[176:177], v[176:177], v[56:59]\n"
"v_mfma_f32_4x4x4_16b_bf16 v[60:63], v[178:179], v[178:179], v[60:63]\n"
"v_mfma_f32_4x4x4_16b_bf16 v[48:51], v[64:65], v[172:173], v[48:51]\n"
"v_mfma_f32_4x4x4_16b_bf16 v[52:55], v[64:65], v[174:175], v[52:55]\n"
"v_mfma_f32_4x4x4_16b_bf16 v[56:59], v[172:173], v[172:173], v[56:59]\n"
"v_mfma_f32_4x4x4_16b_bf16 v[60:63], v[174:175], v[174:175], v[60:63]\n"
"v_mfma_f32_4x4x4_16b_bf16 v[48:51], v[64:65], v[168:169], v[48:51]\n"
"v_mfma_f32_4x4x4_16b_bf16 v[52:55], v[64:65], v[170:171], v[52:55]\n"
"v_mfma_f32_4x4x4_16b_bf16 v[56:59], v[168:169], v[168:169], v[56:59]\n"
"v_mfma_f32_4x4x4_16b_bf16 v[60:63], v[170:171], v[170:171], v[60:63]\n"
"v_mfma_f32_4x4x4_16b_bf16 v[48:51], v[64:65], v[164:165], v[48:51]\n"
"v_mfma_f32_4x4x4_16b_bf16 v[52:55], v[64:65], v[166:167], v[52:55]\n"
"v_mfma_f32_4x4x4_16b_bf16 v[56:59], v[164:165], v[164:165], v[56:59]\n"
"v_mfma_f32_4x4x4_16b_bf16 v[60:63], v[166:167], v[166:167], v[60:63]\n"
"v_mfma_f32_4x4x4_16b_bf16 v[48:51], v[64:65], v[160:161], v[48:51]\n"
"v_mfma_f32_4x4x4_16b_bf16 v[52:55], v[64:65], v[162:163], v[52:55]\n"
"v_mfma_f32_4x4x4_16b_bf16 v[56:59], v[160:161], v[160:161], v[56:59]\n"
"v_mfma_f32_4x4x4_16b_bf16 v[60:63], v[162:163], v[162:163], v[60:63]\n"
"nost_27_%=:\n"
"s_waitcnt vmcnt(4)\n"
"s_barrier\n"
"v_subrev_u32 v36, 65536, v36\n"
"v_subrev_u32 v37, 65536, v37\n"
"v_subrev_u32 v38, 65536, v38\n"
"v_subrev_u32 v39, 65536, v39\n"
"ds_read_b128 v[156:159], v36 offset:0\n"
"ds_read_b128 v[152:155], v37 offset:0\n"
"ds_read_b128 v[148:151], v38 offset:0\n"
"ds_read_b128 v[144:147], v39 offset:0\n"
"ds_read_b128 v[140:143], v36 offset:2048\n"
"ds_read_b128 v[136:139], v37 offset:2048\n"
"ds_read_b128 v[132:135], v38 offset:2048\n"
"ds_read_b128 v[128:131], v39 offset:2048\n"
"s_add_u32 m0, s33, 114688\n"
"s_nop 0\n"
"global_load_lds_dwordx4 v25, s[38:39] offset:0\n"
"global_load_lds_dwordx4 v26, s[38:39] offset:1024\n"
"global_load_lds_dwordx4 v27, s[38:39] offset:2048\n"
"s_cmp_eq_u32 s28, 4\n"
"s_cselect_b32 s45, 1966080, 0\n"
"s_add_u32 s38, s38, 393216\n"
"s_addc_u32 s39, s39, 0\n"
"s_sub_u32 s38, s38, s45\n"
"s_subb_u32 s39, s39, 0\n"
"s_waitcnt lgkmcnt(0)\n"
"s_cmp_eq_u32 s47, 1\n"
"s_cbranch_scc1 nost_28_%=\n"
"v_mfma_f32_4x4x4_16b_bf16 v[48:51], v[64:65], v[156:157], v[48:51]\n"
"v_mfma_f32_4x4x4_16b_bf16 v[52:55], v[64:65], v[158:159], v[52:55]\n"
"v_mfma_f32_4x4x4_16b_bf16 v[56:59], v[156:157], v[156:157], v[56:59]\n"
"v_mfma_f32_4x4x4_16b_bf16 v[60:63], v[158:159], v[158:159], v[60:63]\n"
"v_mfma_f32_4x4x4_16b_bf16 v[48:51], v[64:65], v[152:153], v[48:51]\n"
"v_mfma_f32_4x4x4_16b_bf16 v[52:55], v[64:65], v[154:155], v[52:55]\n"
"v_mfma_f32_4x4x4_16b_bf16 v[56:59], v[152:153], v[152:153], v[56:59]\n"
"v_mfma_f32_4x4x4_16b_bf16 v[60:63], v[154:155], v[154:155], v[60:63]\n"
"v_mfma_f32_4x4x4_16b_bf16 v[48:51], v[64:65], v[148:149], v[48:51]\n"
"v_mfma_f32_4x4x4_16b_bf16 v[52:55], v[64:65], v[150:151], v[52:55]\n"
"v_mfma_f32_4x4x4_16b_bf16 v[56:59], v[148:149], v[148:149], v[56:59]\n"
"v_mfma_f32_4x4x4_16b_bf16 v[60:63], v[150:151], v[150:151], v[60:63]\n"
"v_mfma_f32_4x4x4_16b_bf16 v[48:51], v[64:65], v[144:145], v[48:51]\n"
"v_mfma_f32_4x4x4_16b_bf16 v[52:55], v[64:65], v[146:147], v[52:55]\n"
"v_mfma_f32_4x4x4_16b_bf16 v[56:59], v[144:145], v[144:145], v[56:59]\n"
"v_mfma_f32_4x4x4_16b_bf16 v[60:63], v[146:147], v[146:147], v[60:63]\n"
"v_mfma_f32_4x4x4_16b_bf16 v[48:51], v[64:65], v[140:141], v[48:51]\n"
"v_mfma_f32_4x4x4_16b_bf16 v[52:55], v[64:65], v[142:143], v[52:55]\n"
"v_mfma_f32_4x4x4_16b_bf16 v[56:59], v[140:141], v[140:141], v[56:59]\n"
"v_mfma_f32_4x4x4_16b_bf16 v[60:63], v[142:143], v[142:143], v[60:63]\n"
"v_mfma_f32_4x4x4_16b_bf16 v[48:51], v[64:65], v[136:137], v[48:51]\n"
"v_mfma_f32_4x4x4_16b_bf16 v[52:55], v[64:65], v[138:139], v[52:55]\n"
"v_mfma_f32_4x4x4_16b_bf16 v[56:59], v[136:137], v[136:137], v[56:59]\n"
"v_mfma_f32_4x4x4_16b_bf16 v[60:63], v[138:139], v[138:139], v[60:63]\n"
"v_mfma_f32_4x4x4_16b_bf16 v[48:51], v[64:65], v[132:133], v[48:51]\n"
"v_mfma_f32_4x4x4_16b_bf16 v[52:55], v[64:65], v[134:135], v[52:55]\n"
"v_mfma_f32_4x4x4_16b_bf16 v[56:59], v[132:133], v[132:133], v[56:59]\n"
"v_mfma_f32_4x4x4_16b_bf16 v[60:63], v[134:135], v[134:135], v[60:63]\n"
"v_mfma_f32_4x4x4_16b_bf16 v[48:51], v[64:65], v[128:129], v[48:51]\n"
"v_mfma_f32_4x4x4_16b_bf16 v[52:55], v[64:65], v[130:131], v[52:55]\n"
"v_mfma_f32_4x4x4_16b_bf16 v[56:59], v[128:129], v[128:129], v[56:59]\n"
"v_mfma_f32_4x4x4_16b_bf16 v[60:63], v[130:131], v[130:131], v[60:63]\n"
"nost_28_%=:\n"
"s_waitcnt vmcnt(3)\n"
"s_barrier\n"
"ds_read_b128 v[124:127], v36 offset:32768\n"
"ds_read_b128 v[120:123], v37 offset:32768\n"
"ds_read_b128 v[116:119], v38 offset:32768\n"
"ds_read_b128 v[112:115], v39 offset:32768\n"
"ds_read_b128 v[108:111], v36 offset:34816\n"
"ds_read_b128 v[104:107], v37 offset:34816\n"
"ds_read_b128 v[100:103], v38 offset:34816\n"
"ds_read_b128 v[96:99], v39 offset:34816\n"
"s_add_u32 m0, s33, 139264\n"
"s_nop 0\n"
"global_load_lds_dwordx4 v25, s[38:39] offset:0\n"
"global_load_lds_dwordx4 v26, s[38:39] offset:1024\n"
"global_load_lds_dwordx4 v27, s[38:39] offset:2048\n"
"s_cmp_eq_u32 s28, 3\n"
"s_cselect_b32 s45, 1966080, 0\n"
"s_add_u32 s38, s38, 393216\n"
"s_addc_u32 s39, s39, 0\n"
"s_sub_u32 s38, s38, s45\n"
"s_subb_u32 s39, s39, 0\n"
"s_waitcnt lgkmcnt(0)\n"
"s_cmp_eq_u32 s47, 1\n"
"s_cbranch_scc1 nost_29_%=\n"
"v_mfma_f32_4x4x4_16b_bf16 v[48:51], v[64:65], v[124:125], v[48:51]\n"
"v_mfma_f32_4x4x4_16b_bf16 v[52:55], v[64:65], v[126:127], v[52:55]\n"
"v_mfma_f32_4x4x4_16b_bf16 v[56:59], v[124:125], v[124:125], v[56:59]\n"
"v_mfma_f32_4x4x4_16b_bf16 v[60:63], v[126:127], v[126:127], v[60:63]\n"
"v_mfma_f32_4x4x4_16b_bf16 v[48:51], v[64:65], v[120:121], v[48:51]\n"
"v_mfma_f32_4x4x4_16b_bf16 v[52:55], v[64:65], v[122:123], v[52:55]\n"
"v_mfma_f32_4x4x4_16b_bf16 v[56:59], v[120:121], v[120:121], v[56:59]\n"
"v_mfma_f32_4x4x4_16b_bf16 v[60:63], v[122:123], v[122:123], v[60:63]\n"
"v_mfma_f32_4x4x4_16b_bf16 v[48:51], v[64:65], v[116:117], v[48:51]\n"
"v_mfma_f32_4x4x4_16b_bf16 v[52:55], v[64:65], v[118:119], v[52:55]\n"
"v_mfma_f32_4x4x4_16b_bf16 v[56:59], v[116:117], v[116:117], v[56:59]\n"
"v_mfma_f32_4x4x4_16b_bf16 v[60:63], v[118:119], v[118:119], v[60:63]\n"
"v_mfma_f32_4x4x4_16b_bf16 v[48:51], v[64:65], v[112:113], v[48:51]\n"
"v_mfma_f32_4x4x4_16b_bf16 v[52:55], v[64:65], v[114:115], v[52:55]\n"
"v_mfma_f32_4x4x4_16b_bf16 v[56:59], v[112:113], v[112:113], v[56:59]\n"
"v_mfma_f32_4x4x4_16b_bf16 v[60:63], v[114:115], v[114:115], v[60:63]\n"
"v_mfma_f32_4x4x4_16b_bf16 v[48:51], v[64:65], v[108:109], v[48:51]\n"
"v_mfma_f32_4x4x4_16b_bf16 v[52:55], v[64:65], v[110:111], v[52:55]\n"
"v_mfma_f32_4x4x4_16b_bf16 v[56:59], v[108:109], v[108:109], v[56:59]\n"
"v_mfma_f32_4x4x4_16b_bf16 v[60:63], v[110:111], v[110:111], v[60:63]\n"
"v_mfma_f32_4x4x4_16b_bf16 v[48:51], v[64:65], v[104:105], v[48:51]\n"
"v_mfma_f32_4x4x4_16b_bf16 v[52:55], v[64:65], v[106:107], v[52:55]\n"
"v_mfma_f32_4x4x4_16b_bf16 v[56:59], v[104:105], v[104:105], v[56:59]\n"
"v_mfma_f32_4x4x4_16b_bf16 v[60:63], v[106:107], v[106:107], v[60:63]\n"
"v_mfma_f32_4x4x4_16b_bf16 v[48:51], v[64:65], v[100:101], v[48:51]\n"
"v_mfma_f32_4x4x4_16b_bf16 v[52:55], v[64:65], v[102:103], v[52:55]\n"
"v_mfma_f32_4x4x4_16b_bf16 v[56:59], v[100:101], v[100:101], v[56:59]\n"
"v_mfma_f32_4x4x4_16b_bf16 v[60:63], v[102:103], v[102:103], v[60:63]\n"
"v_mfma_f32_4x4x4_16b_bf16 v[48:51], v[64:65], v[96:97], v[48:51]\n"
"v_mfma_f32_4x4x4_16b_bf16 v[52:55], v[64:65], v[98:99], v[52:55]\n"
"v_mfma_f32_4x4x4_16b_bf16 v[56:59], v[96:97], v[96:97], v[56:59]\n"
"v_mfma_f32_4x4x4_16b_bf16 v[60:63], v[98:99], v[98:99], v[60:63]\n"
"nost_29_%=:\n"
"s_cmp_eq_u32 s47, 1\n"
"s_cbranch_scc1 nopub_30_%=\n"
"s_nop 4\n"
"v_add_f32 v56, v56, v60\n"
"v_add_f32 v57, v57, v61\n"
"v_add_f32 v58, v58, v62\n"
"v_add_f32 v59, v59, v63\n"
"v_add_f32 v3, v48, v52\n"
"v_and_b32 v5, 3, v0\n"
"v_mov_b32 v4, v56\n"
"v_cmp_eq_u32 vcc, 1, v5\n"
"s_nop 1\n"
"v_cndmask_b32 v4, v4, v57, vcc\n"
"v_cmp_eq_u32 vcc, 2, v5\n"
"s_nop 1\n"
"v_cndmask_b32 v4, v4, v58, vcc\n"
"v_cmp_eq_u32 vcc, 3, v5\n"
"s_nop 1\n"
"v_cndmask_b32 v4, v4, v59, vcc\n"
"v_mov_b32 v5, v3\n"
"v_mov_b32 v48, v4\n"
"s_nop 1\n"
"v_permlane32_swap_b32 v3, v5\n"
"v_permlane32_swap_b32 v4, v48\n"
"s_nop 1\n"
"v_add_f32 v3, v3, v5\n"
"v_add_f32 v4, v4, v48\n"
"v_mul_f32 v20, 0x3acccccd, v3\n"
"v_mul_f32 v4, 0x3acccccd, v4\n"
"v_fma_f32 v5, -v20, v20, v4\n"
"v_cmp_gt_f32 vcc, 0, v5\n"
"v_mov_b32 v21, s24\n"
"v_mov_b32 v23, s25\n"
"v_cndmask_b32 v5, v5, 0, vcc\n"
"v_add_f32 v5, 0x3727c5ac, v5\n"
"v_rsq_f32 v22, v5\n"
"s_mov_b32 exec_hi, 0\n"
"global_store_dwordx4 v13, v[20:23], s[18:19] sc1\n"
"s_mov_b32 exec_hi, -1\n"
"nopub_30_%=:\n"
"s_barrier\n"
"v_mov_b32 v4, 0\n"
"ds_write_b32 v4, v4 offset:61440\n"
"s_add_u32 m0, s33, 32768\n"
"s_nop 0\n"
"global_load_lds_dwordx4 v25, s[38:39] offset:0\n"
"global_load_lds_dwordx4 v26, s[38:39] offset:1024\n"
"global_load_lds_dwordx4 v27, s[38:39] offset:2048\n"
"s_cmp_eq_u32 s28, 2\n"
"s_cselect_b32 s45, 1966080, 0\n"
"s_add_u32 s38, s38, 393216\n"
"s_addc_u32 s39, s39, 0\n"
"s_sub_u32 s38, s38, s45\n"
"s_subb_u32 s39, s39, 0\n"
"s_mov_b64 s[36:37], s[38:39]\n"
"s_add_u32 m0, s33, 65536\n"
"s_nop 0\n"
"global_load_lds_dwordx4 v25, s[38:39] offset:0\n"
"global_load_lds_dwordx4 v26, s[38:39] offset:1024\n"
"global_load_lds_dwordx4 v27, s[38:39] offset:2048\n"
"s_cmp_eq_u32 s28, 1\n"
"s_cselect_b32 s45, 1966080, 0\n"
"s_add_u32 s38, s38, 393216\n"
"s_addc_u32 s39, s39, 0\n"
"s_sub_u32 s38, s38, s45\n"
"s_subb_u32 s39, s39, 0\n"
"s_add_u32 m0, s33, 0\n"
"s_nop 0\n"
"global_load_lds_dwordx4 v25, s[38:39] offset:0\n"
"global_load_lds_dwordx4 v26, s[38:39] offset:1024\n"
"global_load_lds_dwordx4 v27, s[38:39] offset:2048\n"
"s_mov_b64 s[38:39], s[36:37]\n"
"s_lshl_b32 s45, s23, 10\n"
"s_add_u32 s40, s98, s45\n"
"s_addc_u32 s41, s99, 0\n"
"s_add_u32 m0, s45, 98304\n"
"s_nop 0\n"
"global_load_lds_dwordx4 v24, s[40:41]\n"
"novt_31_%=:\n"
"s_cmp_eq_u32 s47, 0\n"
"s_cbranch_scc1 novt_32_%=\n"
"s_lshl_b32 s41, s23, 10\n"
"s_add_u32 s45, s41, 4096\n"
"s_add_u32 s40, s98, s45\n"
"s_addc_u32 s41, s99, 0\n"
"s_add_u32 m0, s45, 98304\n"
"s_nop 0\n"
"global_load_lds_dwordx4 v24, s[40:41]\n"
"novt_32_%=:\n"
"v_lshrrev_b32 v3, 2, v0\n"
"v_and_b32 v3, 3, v3\n"
"v_mul_u32_u24 v3, 192, v3\n"
"v_mul_u32_u24 v4, 1536, v2\n"
"v_add_u32 v4, v3, v4\n"
"v_add_u32 v28, s33, v4\n"
"v_lshrrev_b32 v4, 4, v0\n"
"v_and_b32 v4, 1, v4\n"
"v_lshl_add_u32 v28, v4, 5, v28\n"
"v_and_b32 v4, 3, v0\n"
"v_lshl_add_u32 v28, v4, 3, v28\n"
"s_waitcnt vmcnt(13)\n"
"v_add_u32 v28, 114688, v28\n"
"ds_read_b64_tr_b16 v[80:81], v28 offset:0\n"
"ds_read_b64_tr_b16 v[82:83], v28 offset:768\n"
"ds_read_b64_tr_b16 v[84:85], v28 offset:64\n"
"ds_read_b64_tr_b16 v[86:87], v28 offset:832\n"
"ds_read_b64_tr_b16 v[88:89], v28 offset:128\n"
"ds_read_b64_tr_b16 v[90:91], v28 offset:896\n"
"s_waitcnt lgkmcnt(0)\n"
"v_mfma_f32_16x16x32_bf16 v[68:71], v[40:43], v[80:83], 0\n"
"v_mfma_f32_16x16x32_bf16 v[72:75], v[40:43], v[84:87], 0\n"
"v_mfma_f32_16x16x32_bf16 v[76:79], v[40:43], v[88:91], 0\n"
"s_waitcnt vmcnt(10)\n"
"ds_read_b64_tr_b16 v[80:81], v28 offset:24576\n"
"ds_read_b64_tr_b16 v[82:83], v28 offset:25344\n"
"ds_read_b64_tr_b16 v[84:85], v28 offset:24640\n"
"ds_read_b64_tr_b16 v[86:87], v28 offset:25408\n"
"ds_read_b64_tr_b16 v[88:89], v28 offset:24704\n"
"ds_read_b64_tr_b16 v[90:91], v28 offset:25472\n"
"s_waitcnt lgkmcnt(0)\n"
"v_mfma_f32_16x16x32_bf16 v[68:71], v[40:43], v[80:83], v[68:71]\n"
"v_mfma_f32_16x16x32_bf16 v[72:75], v[40:43], v[84:87], v[72:75]\n"
"v_mfma_f32_16x16x32_bf16 v[76:79], v[40:43], v[88:91], v[76:79]\n"
"s_waitcnt vmcnt(7)\n"
"v_subrev_u32 v28, 81920, v28\n"
"ds_read_b64_tr_b16 v[80:81], v28 offset:0\n"
"ds_read_b64_tr_b16 v[82:83], v28 offset:768\n"
"ds_read_b64_tr_b16 v[84:85], v28 offset:64\n"
"ds_read_b64_tr_b16 v[86:87], v28 offset:832\n"
"ds_read_b64_tr_b16 v[88:89], v28 offset:128\n"
"ds_read_b64_tr_b16 v[90:91], v28 offset:896\n"
"s_waitcnt lgkmcnt(0)\n"
"v_mfma_f32_16x16x32_bf16 v[68:71], v[40:43], v[80:83], v[68:71]\n"
"v_mfma_f32_16x16x32_bf16 v[72:75], v[40:43], v[84:87], v[72:75]\n"
"v_mfma_f32_16x16x32_bf16 v[76:79], v[40:43], v[88:91], v[76:79]\n"
"s_waitcnt vmcnt(4)\n"
"ds_read_b64_tr_b16 v[80:81], v28 offset:32768\n"
"ds_read_b64_tr_b16 v[82:83], v28 offset:33536\n"
"ds_read_b64_tr_b16 v[84:85], v28 offset:32832\n"
"ds_read_b64_tr_b16 v[86:87], v28 offset:33600\n"
"ds_read_b64_tr_b16 v[88:89], v28 offset:32896\n"
"ds_read_b64_tr_b16 v[90:91], v28 offset:33664\n"
"s_waitcnt lgkmcnt(0)\n"
"v_mfma_f32_16x16x32_bf16 v[68:71], v[40:43], v[80:83], v[68:71]\n"
"v_mfma_f32_16x16x32_bf16 v[72:75], v[40:43], v[84:87], v[72:75]\n"
"v_mfma_f32_16x16x32_bf16 v[76:79], v[40:43], v[88:91], v[76:79]\n"
"s_waitcnt vmcnt(1)\n"
"v_subrev_u32 v28, 32768, v28\n"
"ds_read_b64_tr_b16 v[80:81], v28 offset:0\n"
"ds_read_b64_tr_b16 v[82:83], v28 offset:768\n"
"ds_read_b64_tr_b16 v[84:85], v28 offset:64\n"
"ds_read_b64_tr_b16 v[86:87], v28 offset:832\n"
"ds_read_b64_tr_b16 v[88:89], v28 offset:128\n"
"ds_read_b64_tr_b16 v[90:91], v28 offset:896\n"
"s_waitcnt lgkmcnt(0)\n"
"v_mfma_f32_16x16x32_bf16 v[68:71], v[40:43], v[80:83], v[68:71]\n"
"v_mfma_f32_16x16x32_bf16 v[72:75], v[40:43], v[84:87], v[72:75]\n"
"v_mfma_f32_16x16x32_bf16 v[76:79], v[40:43], v[88:91], v[76:79]\n"
"s_lshl_b32 s41, s23, 8\n"
"s_add_u32 s41, s41, 24576\n"
"v_lshl_add_u32 v28, v0, 2, s41\n"
"s_nop 2\n"
"ds_write_b32 v28, v68 offset:0\n"
"ds_write_b32 v28, v72 offset:2048\n"
"ds_write_b32 v28, v76 offset:4096\n"
"s_min_u32 s41, s23, 2\n"
"s_lshl_b32 s41, s41, 11\n"
"s_add_u32 s41, s41, 24576\n"
"v_lshl_add_u32 v28, v0, 2, s41\n"
"s_waitcnt lgkmcnt(0)\n"
"s_barrier\n"
"ds_read2st64_b32 v[80:81], v28 offset0:0 offset1:1\n"
"ds_read2st64_b32 v[82:83], v28 offset0:2 offset1:3\n"
"ds_read2st64_b32 v[84:85], v28 offset0:4 offset1:5\n"
"ds_read2st64_b32 v[86:87], v28 offset0:6 offset1:7\n"
"s_mul_i32 s41, s27, 3\n"
"s_min_u32 s40, s23, 2\n"
"s_add_u32 s40, s41, s40\n"
"s_lshl_b32 s40, s40, 9\n"
"s_add_u32 s40, s40, 16809984\n"
"v_lshl_add_u32 v29, v1, 4, s40\n"
"v_mov_b32 v69, s24\n"
"v_mov_b32 v71, s25\n"
"s_waitcnt lgkmcnt(0)\n"
"v_add_f32 v80, v80, v81\n"
"v_add_f32 v80, v80, v82\n"
"v_add_f32 v80, v80, v83\n"
"v_add_f32 v80, v80, v84\n"
"v_add_f32 v80, v80, v85\n"
"v_add_f32 v80, v80, v86\n"
"v_add_f32 v80, v80, v87\n"
"v_and_b32 v37, 0xffff0000, v80\n"
"v_sub_f32 v38, v80, v37\n"
"v_cvt_pk_bf16_f32 v68, v37, v37\n"
"v_cvt_pk_bf16_f32 v70, v38, v38\n"
"s_cmp_ge_u32 s23, 3\n"
"s_cbranch_scc1 nocs_33_%=\n"
"s_mov_b32 exec_hi, 0\n"
"global_store_dwordx4 v29, v[68:71], s[18:19] sc1\n"
"s_mov_b32 exec_hi, -1\n"
"nocs_33_%=:\n"
"global_load_dwordx4 v[20:23], v13, s[18:19] sc1\n"
"s_waitcnt vmcnt(0)\n"
"v_cmp_ne_u32 vcc, s24, v21\n"
"v_cmp_ne_u32 s[42:43], s25, v23\n"
"s_nop 0\n"
"s_or_b64 vcc, vcc, s[42:43]\n"
"s_cmp_eq_u64 vcc, 0\n"
"s_cbranch_scc0 retry_34_%=\n"
"ok_35_%=:\n"
"v_xor_b32 v3, 0x80000000, v20\n"
"v_and_b32 v4, 0xffff0000, v3\n"
"v_sub_f32 v5, v3, v4\n"
"v_cmp_eq_u32 vcc, 0, v2\n"
"v_cvt_pk_bf16_f32 v5, v4, v5\n"
"s_nop 0\n"
"v_cndmask_b32 v18, 0, v5, vcc\n"
"v_cndmask_b32 v19, 0, v5, vcc\n"
"v_cndmask_b32 v14, v5, 0, vcc\n"
"v_cndmask_b32 v15, v5, 0, vcc\n"
"s_barrier\n"
"s_branch p2_5_%=\n"
"p1v_4_%=:\n"
"global_load_lds_dwordx4 v29, s[34:35] offset:1024\n"
"global_load_lds_dwordx4 v30, s[36:37] offset:2048\n"
"global_load_lds_dwordx4 v31, s[36:37] offset:3072\n"
"s_mov_b32 s40, 128\n"
"s_mov_b32 s41, 245760\n"
"s_cmp_eq_u32 s29, 10\n"
"s_cmov_b32 s40, -1152\n"
"s_cmov_b32 s41, -2211840\n"
"s_cselect_b32 s42, -1, 0\n"
"s_add_u32 s34, s34, s40\n"
"s_addc_u32 s35, s35, s42\n"
"s_add_u32 s36, s36, s41\n"
"s_addc_u32 s37, s37, s42\n"
"s_waitcnt lgkmcnt(0)\n"
"s_mul_i32 s24, s24, 1664525\n"
"s_add_u32 s24, s24, 1013904223\n"
"s_mul_i32 s25, s25, 22695477\n"
"s_add_u32 s25, s25, 1\n"
"s_mov_b32 s31, 100000\n"
"s_add_u32 s38, s14, s76\n"
"s_addc_u32 s39, s15, 0\n"
"s_add_u32 s8, s8, s79\n"
"s_addc_u32 s9, s9, 0\n"
"s_add_u32 s20, s20, s79\n"
"s_addc_u32 s21, s21, 0\n"
"s_cmp_eq_u32 s46, 0\n"
"s_cselect_b32 s76, 2, 1\n"
"s_cmp_lt_u32 s27, 5\n"
"s_cselect_b32 s76, s76, 0\n"
"s_waitcnt vmcnt(6)\n"
"s_barrier\n"
"ds_read_b64_tr_b16 v[48:49], v32 offset:0\n"
"ds_read_b64_tr_b16 v[50:51], v32 offset:1024\n"
"ds_read_b128 v[252:255], v36 offset:0\n"
"ds_read_b64_tr_b16 v[52:53], v34 offset:0\n"
"ds_read_b64_tr_b16 v[54:55], v34 offset:1024\n"
"s_setprio 2\n"
"ds_read_b64_tr_b16 v[56:57], v33 offset:0\n"
"ds_read_b64_tr_b16 v[58:59], v33 offset:1024\n"
"ds_read_b128 v[248:251], v37 offset:0\n"
"s_waitcnt lgkmcnt(5)\n"
"v_mfma_f32_32x32x16_bf16 v[64:79], v[252:255], v[48:51], 0\n"
"s_cmp_lt_u32 s76, 1\n"
"s_cbranch_scc1 nocq_36_%=\n"
"v_mfma_f32_16x16x32_bf16 v[44:47], v[40:43], v[48:51], 0\n"
"nocq_36_%=:\n"
"ds_read_b64_tr_b16 v[60:61], v35 offset:0\n"
"ds_read_b64_tr_b16 v[62:63], v35 offset:1024\n"
"s_waitcnt lgkmcnt(5)\n"
"v_mfma_f32_32x32x16_bf16 v[80:95], v[252:255], v[52:55], 0\n"
"s_add_u32 m0, s30, 65536\n"
"s_add_u32 s29, s29, 1\n"
"global_load_lds_dwordx4 v28, s[34:35] offset:0\n"
"global_load_lds_dwordx4 v29, s[34:35] offset:1024\n"
"s_setprio 1\n"
"ds_read_b64_tr_b16 v[48:49], v32 offset:16384\n"
"ds_read_b64_tr_b16 v[50:51], v32 offset:17408\n"
"ds_read_b128 v[244:247], v38 offset:0\n"
"s_waitcnt lgkmcnt(5)\n"
"v_mfma_f32_32x32x16_bf16 v[64:79], v[248:251], v[56:59], v[64:79]\n"
"ds_read_b64_tr_b16 v[52:53], v34 offset:16384\n"
"ds_read_b64_tr_b16 v[54:55], v34 offset:17408\n"
"s_waitcnt lgkmcnt(5)\n"
"v_mfma_f32_32x32x16_bf16 v[80:95], v[248:251], v[60:63], v[80:95]\n"
"global_load_lds_dwordx4 v30, s[36:37] offset:2048\n"
"s_setprio 0\n"
"ds_read_b64_tr_b16 v[56:57], v33 offset:16384\n"
"ds_read_b64_tr_b16 v[58:59], v33 offset:17408\n"
"ds_read_b128 v[240:243], v39 offset:0\n"
"s_waitcnt lgkmcnt(5)\n"
"v_mfma_f32_32x32x16_bf16 v[64:79], v[244:247], v[48:51], v[64:79]\n"
"s_cmp_lt_u32 s76, 1\n"
"s_cbranch_scc1 nocq_37_%=\n"
"v_mfma_f32_16x16x32_bf16 v[44:47], v[40:43], v[48:51], v[44:47]\n"
"nocq_37_%=:\n"
"ds_read_b64_tr_b16 v[60:61], v35 offset:16384\n"
"ds_read_b64_tr_b16 v[62:63], v35 offset:17408\n"
"s_waitcnt lgkmcnt(5)\n"
"v_mfma_f32_32x32x16_bf16 v[80:95], v[244:247], v[52:55], v[80:95]\n"
"global_load_lds_dwordx4 v31, s[36:37] offset:3072\n"
"s_add_u32 s34, s34, 128\n"
"s_addc_u32 s35, s35, 0\n"
"s_add_u32 s36, s36, 245760\n"
"s_addc_u32 s37, s37, 0\n"
"s_waitcnt vmcnt(4)\n"
"s_waitcnt lgkmcnt(0)\n"
"s_barrier\n"
"s_setprio 3\n"
"ds_read_b64_tr_b16 v[48:49], v32 offset:32768\n"
"ds_read_b64_tr_b16 v[50:51], v32 offset:33792\n"
"ds_read_b128 v[236:239], v36 offset:32768\n"
"v_mfma_f32_32x32x16_bf16 v[64:79], v[240:243], v[56:59], v[64:79]\n"
"ds_read_b64_tr_b16 v[52:53], v34 offset:32768\n"
"ds_read_b64_tr_b16 v[54:55], v34 offset:33792\n"
"v_mfma_f32_32x32x16_bf16 v[80:95], v[240:243], v[60:63], v[80:95]\n"
"s_add_u32 m0, s30, 0\n"
"s_add_u32 s29, s29, 1\n"
"global_load_lds_dwordx4 v28, s[34:35] offset:0\n"
"s_setprio 2\n"
"ds_read_b64_tr_b16 v[56:57], v33 offset:32768\n"
"ds_read_b64_tr_b16 v[58:59], v33 offset:33792\n"
"ds_read_b128 v[232:235], v37 offset:32768\n"
"s_waitcnt lgkmcnt(5)\n"
"v_mfma_f32_32x32x16_bf16 v[64:79], v[236:239], v[48:51], v[64:79]\n"
"s_cmp_lt_u32 s76, 1\n"
"s_cbranch_scc1 nocq_38_%=\n"
"v_mfma_f32_16x16x32_bf16 v[44:47], v[40:43], v[48:51], v[44:47]\n"
"nocq_38_%=:\n"
"ds_read_b64_tr_b16 v[60:61], v35 offset:32768\n"
"ds_read_b64_tr_b16 v[62:63], v35 offset:33792\n"
"s_waitcnt lgkmcnt(5)\n"
"v_mfma_f32_32x32x16_bf16 v[80:95], v[236:239], v[52:55], v[80:95]\n"
"global_load_lds_dwordx4 v29, s[34:35] offset:1024\n"
"s_setprio 1\n"
"ds_read_b64_tr_b16 v[48:49], v32 offset:49152\n"
"ds_read_b64_tr_b16 v[50:51], v32 offset:50176\n"
"ds_read_b128 v[228:231], v38 offset:32768\n"
"s_waitcnt lgkmcnt(5)\n"
"v_mfma_f32_32x32x16_bf16 v[64:79], v[232:235], v[56:59], v[64:79]\n"
"ds_read_b64_tr_b16 v[52:53], v34 offset:49152\n"
"ds_read_b64_tr_b16 v[54:55], v34 offset:50176\n"
"s_waitcnt lgkmcnt(5)\n"
"v_mfma_f32_32x32x16_bf16 v[80:95], v[232:235], v[60:63], v[80:95]\n"
"global_load_lds_dwordx4 v30, s[36:37] offset:2048\n"
"s_setprio 0\n"
"ds_read_b64_tr_b16 v[56:57], v33 offset:49152\n"
"ds_read_b64_tr_b16 v[58:59], v33 offset:50176\n"
"ds_read_b128 v[224:227], v39 offset:32768\n"
"s_waitcnt lgkmcnt(5)\n"
"v_mfma_f32_32x32x16_bf16 v[64:79], v[228:231], v[48:51], v[64:79]\n"
"s_cmp_lt_u32 s76, 1\n"
"s_cbranch_scc1 nocq_39_%=\n"
"v_mfma_f32_16x16x32_bf16 v[44:47], v[40:43], v[48:51], v[44:47]\n"
"nocq_39_%=:\n"
"ds_read_b64_tr_b16 v[60:61], v35 offset:49152\n"
"ds_read_b64_tr_b16 v[62:63], v35 offset:50176\n"
"s_waitcnt lgkmcnt(5)\n"
"v_mfma_f32_32x32x16_bf16 v[80:95], v[228:231], v[52:55], v[80:95]\n"
"global_load_lds_dwordx4 v31, s[36:37] offset:3072\n"
"s_mov_b32 s40, 128\n"
"s_mov_b32 s41, 245760\n"
"s_cmp_eq_u32 s29, 10\n"
"s_cmov_b32 s40, -1152\n"
"s_cmov_b32 s41, -2211840\n"
"s_cselect_b32 s42, -1, 0\n"
"s_add_u32 s34, s34, s40\n"
"s_addc_u32 s35, s35, s42\n"
"s_add_u32 s36, s36, s41\n"
"s_addc_u32 s37, s37, s42\n"
"s_waitcnt vmcnt(4)\n"
"s_waitcnt lgkmcnt(0)\n"
"s_barrier\n"
"s_setprio 3\n"
"v_add_u32 v32, 65536, v32\n"
"v_add_u32 v33, 65536, v33\n"
"ds_read_b64_tr_b16 v[48:49], v32 offset:0\n"
"ds_read_b64_tr_b16 v[50:51], v32 offset:1024\n"
"v_add_u32 v36, 65536, v36\n"
"v_add_u32 v37, 65536, v37\n"
"v_add_u32 v38, 65536, v38\n"
"v_add_u32 v39, 65536, v39\n"
"ds_read_b128 v[220:223], v36 offset:0\n"
"v_mfma_f32_32x32x16_bf16 v[64:79], v[224:227], v[56:59], v[64:79]\n"
"v_add_u32 v34, 65536, v34\n"
"v_add_u32 v35, 65536, v35\n"
"ds_read_b64_tr_b16 v[52:53], v34 offset:0\n"
"ds_read_b64_tr_b16 v[54:55], v34 offset:1024\n"
"v_mfma_f32_32x32x16_bf16 v[80:95], v[224:227], v[60:63], v[80:95]\n"
"s_add_u32 m0, s30, 32768\n"
"s_add_u32 s29, s29, 1\n"
"global_load_lds_dwordx4 v28, s[34:35] offset:0\n"
"s_setprio 2\n"
"s_cmp_lt_u32 s76, 1\n"
"s_cbranch_scc1 nocq_40_%=\n"
"ds_write_b32 v10, v44\n"
"nocq_40_%=:\n"
"ds_read_b64_tr_b16 v[56:57], v33 offset:0\n"
"ds_read_b64_tr_b16 v[58:59], v33 offset:1024\n"
"ds_read_b128 v[216:219], v37 offset:0\n"
"s_waitcnt lgkmcnt(5)\n"
"v_mfma_f32_32x32x16_bf16 v[64:79], v[220:223], v[48:51], v[64:79]\n"
"s_mov_b32 s44, m0\n"
"s_add_u32 m0, s33, 114688\n"
"s_nop 0\n"
"global_load_lds_dwordx4 v25, s[38:39] offset:0\n"
"s_mov_b32 m0, s44\n"
"ds_read_b64_tr_b16 v[60:61], v35 offset:0\n"
"ds_read_b64_tr_b16 v[62:63], v35 offset:1024\n"
"s_waitcnt lgkmcnt(5)\n"
"v_mfma_f32_32x32x16_bf16 v[80:95], v[220:223], v[52:55], v[80:95]\n"
"global_load_lds_dwordx4 v29, s[34:35] offset:1024\n"
"s_setprio 1\n"
"ds_read_b64_tr_b16 v[48:49], v32 offset:16384\n"
"ds_read_b64_tr_b16 v[50:51], v32 offset:17408\n"
"ds_read_b128 v[212:215], v38 offset:0\n"
"s_waitcnt lgkmcnt(5)\n"
"v_mfma_f32_32x32x16_bf16 v[64:79], v[216:219], v[56:59], v[64:79]\n"
"s_mov_b32 s44, m0\n"
"s_add_u32 m0, s33, 114688\n"
"s_nop 0\n"
"global_load_lds_dwordx4 v26, s[38:39] offset:1024\n"
"s_mov_b32 m0, s44\n"
"ds_read_b64_tr_b16 v[52:53], v34 offset:16384\n"
"ds_read_b64_tr_b16 v[54:55], v34 offset:17408\n"
"s_waitcnt lgkmcnt(5)\n"
"v_mfma_f32_32x32x16_bf16 v[80:95], v[216:219], v[60:63], v[80:95]\n"
"global_load_lds_dwordx4 v30, s[36:37] offset:2048\n"
"s_setprio 0\n"
"ds_read_b64_tr_b16 v[56:57], v33 offset:16384\n"
"ds_read_b64_tr_b16 v[58:59], v33 offset:17408\n"
"ds_read_b128 v[208:211], v39 offset:0\n"
"s_waitcnt lgkmcnt(5)\n"
"v_mfma_f32_32x32x16_bf16 v[64:79], v[212:215], v[48:51], v[64:79]\n"
"s_mov_b32 s44, m0\n"
"s_add_u32 m0, s33, 114688\n"
"s_nop 0\n"
"global_load_lds_dwordx4 v27, s[38:39] offset:2048\n"
"s_cmp_eq_u32 s28, 4\n"
"s_cselect_b32 s45, 1966080, 0\n"
"s_add_u32 s38, s38, 393216\n"
"s_addc_u32 s39, s39, 0\n"
"s_sub_u32 s38, s38, s45\n"
"s_subb_u32 s39, s39, 0\n"
"s_mov_b32 m0, s44\n"
"ds_read_b64_tr_b16 v[60:61], v35 offset:16384\n"
"ds_read_b64_tr_b16 v[62:63], v35 offset:17408\n"
"s_waitcnt lgkmcnt(5)\n"
"v_mfma_f32_32x32x16_bf16 v[80:95], v[212:215], v[52:55], v[80:95]\n"
"global_load_lds_dwordx4 v31, s[36:37] offset:3072\n"
"s_add_u32 s34, s34, 128\n"
"s_addc_u32 s35, s35, 0\n"
"s_add_u32 s36, s36, 245760\n"
"s_addc_u32 s37, s37, 0\n"
"s_waitcnt vmcnt(7)\n"
"s_waitcnt lgkmcnt(0)\n"
"s_barrier\n"
"s_cmp_lt_u32 s76, 2\n"
"s_cbranch_scc1 nocq_41_%=\n"
"ds_read2st64_b32 v[40:41], v11 offset0:0 offset1:2\n"
"nocq_41_%=:\n"
"s_setprio 3\n"
"v_subrev_u32 v32, 65536, v32\n"
"v_subrev_u32 v33, 65536, v33\n"
"ds_read_b64_tr_b16 v[48:49], v32 offset:0\n"
"ds_read_b64_tr_b16 v[50:51], v32 offset:1024\n"
"v_subrev_u32 v36, 65536, v36\n"
"v_subrev_u32 v37, 65536, v37\n"
"v_subrev_u32 v38, 65536, v38\n"
"v_subrev_u32 v39, 65536, v39\n"
"ds_read_b128 v[204:207], v36 offset:0\n"
"v_mfma_f32_32x32x16_bf16 v[64:79], v[208:211], v[56:59], v[64:79]\n"
"v_subrev_u32 v34, 65536, v34\n"
"v_subrev_u32 v35, 65536, v35\n"
"ds_read_b64_tr_b16 v[52:53], v34 offset:0\n"
"ds_read_b64_tr_b16 v[54:55], v34 offset:1024\n"
"v_mfma_f32_32x32x16_bf16 v[80:95], v[208:211], v[60:63], v[80:95]\n"
"s_add_u32 m0, s30, 65536\n"
"s_add_u32 s29, s29, 1\n"
"global_load_lds_dwordx4 v28, s[34:35] offset:0\n"
"s_setprio 2\n"
"ds_read_b64_tr_b16 v[56:57], v33 offset:0\n"
"ds_read_b64_tr_b16 v[58:59], v33 offset:1024\n"
"ds_read_b128 v[200:203], v37 offset:0\n"
"s_waitcnt lgkmcnt(5)\n"
"v_mfma_f32_32x32x16_bf16 v[64:79], v[204:207], v[48:51], v[64:79]\n"
"ds_read_b64_tr_b16 v[60:61], v35 offset:0\n"
"ds_read_b64_tr_b16 v[62:63], v35 offset:1024\n"
"s_waitcnt lgkmcnt(5)\n"
"v_mfma_f32_32x32x16_bf16 v[80:95], v[204:207], v[52:55], v[80:95]\n"
"global_load_lds_dwordx4 v29, s[34:35] offset:1024\n"
"s_setprio 1\n"
"ds_read_b64_tr_b16 v[48:49], v32 offset:16384\n"
"ds_read_b64_tr_b16 v[50:51], v32 offset:17408\n"
"ds_read_b128 v[196:199], v38 offset:0\n"
"s_waitcnt lgkmcnt(5)\n"
"v_mfma_f32_32x32x16_bf16 v[64:79], v[200:203], v[56:59], v[64:79]\n"
"ds_read_b64_tr_b16 v[52:53], v34 offset:16384\n"
"ds_read_b64_tr_b16 v[54:55], v34 offset:17408\n"
"s_waitcnt lgkmcnt(5)\n"
"v_mfma_f32_32x32x16_bf16 v[80:95], v[200:203], v[60:63], v[80:95]\n"
"global_load_lds_dwordx4 v30, s[36:37] offset:2048\n"
"s_setprio 0\n"
"ds_read_b64_tr_b16 v[56:57], v33 offset:16384\n"
"ds_read_b64_tr_b16 v[58:59], v33 offset:17408\n"
"ds_read_b128 v[192:195], v39 offset:0\n"
"s_waitcnt lgkmcnt(5)\n"
"v_mfma_f32_32x32x16_bf16 v[64:79], v[196:199], v[48:51], v[64:79]\n"
"ds_read_b64_tr_b16 v[60:61], v35 offset:16384\n"
"ds_read_b64_tr_b16 v[62:63], v35 offset:17408\n"
"s_waitcnt lgkmcnt(5)\n"
"v_mfma_f32_32x32x16_bf16 v[80:95], v[196:199], v[52:55], v[80:95]\n"
"global_load_lds_dwordx4 v31, s[36:37] offset:3072\n"
"s_mov_b32 s40, 128\n"
"s_mov_b32 s41, 245760\n"
"s_cmp_eq_u32 s29, 10\n"
"s_cmov_b32 s40, -1152\n"
"s_cmov_b32 s41, -2211840\n"
"s_cselect_b32 s42, -1, 0\n"
"s_add_u32 s34, s34, s40\n"
"s_addc_u32 s35, s35, s42\n"
"s_add_u32 s36, s36, s41\n"
"s_addc_u32 s37, s37, s42\n"
"s_waitcnt vmcnt(4)\n"
"s_waitcnt lgkmcnt(0)\n"
"s_barrier\n"
"s_cmp_lt_u32 s76, 2\n"
"s_cbranch_scc1 nocq_42_%=\n"
"v_add_f32 v40, v40, v41\n"
"v_mov_b32 v41, s24\n"
"v_mov_b32 v42, v40\n"
"v_mov_b32 v43, s25\n"
"s_mul_i32 s40, s26, 5\n"
"s_add_u32 s40, s40, s28\n"
"s_lshl_b32 s40, s40, 1\n"
"s_add_u32 s40, s40, s47\n"
"s_lshl_b32 s40, s40, 9\n"
"s_add_u32 s45, s40, 16842752\n"
"s_add_u32 s40, s18, s45\n"
"s_addc_u32 s41, s19, 0\n"
"v_permlane32_swap_b32 v40, v42\n"
"s_mov_b32 exec_hi, 0\n"
"global_store_dwordx4 v17, v[40:43], s[40:41] sc1\n"
"s_mov_b32 exec_hi, -1\n"
"nocq_42_%=:\n"
"s_setprio 3\n"
"ds_read_b64_tr_b16 v[48:49], v32 offset:32768\n"
"ds_read_b64_tr_b16 v[50:51], v32 offset:33792\n"
"ds_read_b128 v[188:191], v36 offset:32768\n"
"v_mfma_f32_32x32x16_bf16 v[64:79], v[192:195], v[56:59], v[64:79]\n"
"ds_read_b64_tr_b16 v[52:53], v34 offset:32768\n"
"ds_read_b64_tr_b16 v[54:55], v34 offset:33792\n"
"v_mfma_f32_32x32x16_bf16 v[80:95], v[192:195], v[60:63], v[80:95]\n"
"s_add_u32 m0, s30, 0\n"
"s_add_u32 s29, s29, 1\n"
"global_load_lds_dwordx4 v28, s[34:35] offset:0\n"
"s_setprio 2\n"
"ds_read_b64_tr_b16 v[56:57], v33 offset:32768\n"
"ds_read_b64_tr_b16 v[58:59], v33 offset:33792\n"
"ds_read_b128 v[184:187], v37 offset:32768\n"
"s_waitcnt lgkmcnt(5)\n"
"v_mfma_f32_32x32x16_bf16 v[64:79], v[188:191], v[48:51], v[64:79]\n"
"ds_read_b64_tr_b16 v[60:61], v35 offset:32768\n"
"ds_read_b64_tr_b16 v[62:63], v35 offset:33792\n"
"s_waitcnt lgkmcnt(5)\n"
"v_mfma_f32_32x32x16_bf16 v[80:95], v[188:191], v[52:55], v[80:95]\n"
"global_load_lds_dwordx4 v29, s[34:35] offset:1024\n"
"s_setprio 1\n"
"ds_read_b64_tr_b16 v[48:49], v32 offset:49152\n"
"ds_read_b64_tr_b16 v[50:51], v32 offset:50176\n"
"ds_read_b128 v[180:183], v38 offset:32768\n"
"s_waitcnt lgkmcnt(5)\n"
"v_mfma_f32_32x32x16_bf16 v[64:79], v[184:187], v[56:59], v[64:79]\n"
"ds_read_b64_tr_b16 v[52:53], v34 offset:49152\n"
"ds_read_b64_tr_b16 v[54:55], v34 offset:50176\n"
"s_waitcnt lgkmcnt(5)\n"
"v_mfma_f32_32x32x16_bf16 v[80:95], v[184:187], v[60:63], v[80:95]\n"
"global_load_lds_dwordx4 v30, s[36:37] offset:2048\n"
"s_setprio 0\n"
"ds_read_b64_tr_b16 v[56:57], v33 offset:49152\n"
"ds_read_b64_tr_b16 v[58:59], v33 offset:50176\n"
"ds_read_b128 v[176:179], v39 offset:32768\n"
"s_waitcnt lgkmcnt(5)\n"
"v_mfma_f32_32x32x16_bf16 v[64:79], v[180:183], v[48:51], v[64:79]\n"
"ds_read_b64_tr_b16 v[60:61], v35 offset:49152\n"
"ds_read_b64_tr_b16 v[62:63], v35 offset:50176\n"
"s_waitcnt lgkmcnt(5)\n"
"v_mfma_f32_32x32x16_bf16 v[80:95], v[180:183], v[52:55], v[80:95]\n"
"global_load_lds_dwordx4 v31, s[36:37] offset:3072\n"
"s_add_u32 s34, s34, 128\n"
"s_addc_u32 s35, s35, 0\n"
"s_add_u32 s36, s36, 245760\n"
"s_addc_u32 s37, s37, 0\n"
"s_waitcnt vmcnt(5)\n"
"s_cmp_eq_u32 s76, 2\n"
"s_cbranch_scc1 cqw_43_%=\n"
"s_waitcnt vmcnt(4)\n"
"cqw_43_%=:\n"
"s_waitcnt lgkmcnt(0)\n"
"s_barrier\n"
"s_setprio 3\n"
"v_add_u32 v32, 65536, v32\n"
"v_add_u32 v33, 65536, v33\n"
"ds_read_b64_tr_b16 v[48:49], v32 offset:0\n"
"ds_read_b64_tr_b16 v[50:51], v32 offset:1024\n"
"v_add_u32 v36, 65536, v36\n"
"v_add_u32 v37, 65536, v37\n"
"v_add_u32 v38, 65536, v38\n"
"v_add_u32 v39, 65536, v39\n"
"ds_read_b128 v[172:175], v36 offset:0\n"
"v_mfma_f32_32x32x16_bf16 v[64:79], v[176:179], v[56:59], v[64:79]\n"
"v_add_u32 v34, 65536, v34\n"
"v_add_u32 v35, 65536, v35\n"
"ds_read_b64_tr_b16 v[52:53], v34 offset:0\n"
"ds_read_b64_tr_b16 v[54:55], v34 offset:1024\n"
"v_mfma_f32_32x32x16_bf16 v[80:95], v[176:179], v[60:63], v[80:95]\n"
"s_add_u32 m0, s30, 32768\n"
"s_add_u32 s29, s29, 1\n"
"global_load_lds_dwordx4 v28, s[34:35] offset:0\n"
"s_setprio 2\n"
"ds_read_b64_tr_b16 v[56:57], v33 offset:0\n"
"ds_read_b64_tr_b16 v[58:59], v33 offset:1024\n"
"ds_read_b128 v[168:171], v37 offset:0\n"
"s_waitcnt lgkmcnt(5)\n"
"v_mfma_f32_32x32x16_bf16 v[64:79], v[172:175], v[48:51], v[64:79]\n"
"s_mov_b32 s44, m0\n"
"s_add_u32 m0, s33, 139264\n"
"s_nop 0\n"
"global_load_lds_dwordx4 v25, s[38:39] offset:0\n"
"s_mov_b32 m0, s44\n"
"ds_read_b64_tr_b16 v[60:61], v35 offset:0\n"
"ds_read_b64_tr_b16 v[62:63], v35 offset:1024\n"
"s_waitcnt lgkmcnt(5)\n"
"v_mfma_f32_32x32x16_bf16 v[80:95], v[172:175], v[52:55], v[80:95]\n"
"global_load_lds_dwordx4 v29, s[34:35] offset:1024\n"
"s_setprio 1\n"
"ds_read_b64_tr_b16 v[48:49], v32 offset:16384\n"
"ds_read_b64_tr_b16 v[50:51], v32 offset:17408\n"
"ds_read_b128 v[164:167], v38 offset:0\n"
"s_waitcnt lgkmcnt(5)\n"
"v_mfma_f32_32x32x16_bf16 v[64:79], v[168:171], v[56:59], v[64:79]\n"
"ds_read_b64_tr_b16 v[52:53], v34 offset:16384\n"
"ds_read_b64_tr_b16 v[54:55], v34 offset:17408\n"
"s_waitcnt lgkmcnt(5)\n"
"v_mfma_f32_32x32x16_bf16 v[80:95], v[168:171], v[60:63], v[80:95]\n"
"global_load_lds_dwordx4 v30, s[36:37] offset:2048\n"
"s_setprio 0\n"
"ds_read_b64_tr_b16 v[56:57], v33 offset:16384\n"
"ds_read_b64_tr_b16 v[58:59], v33 offset:17408\n"
"ds_read_b128 v[160:163], v39 offset:0\n"
"s_waitcnt lgkmcnt(5)\n"
"v_mfma_f32_32x32x16_bf16 v[64:79], v[164:167], v[48:51], v[64:79]\n"
"ds_read_b64_tr_b16 v[60:61], v35 offset:16384\n"
"ds_read_b64_tr_b16 v[62:63], v35 offset:17408\n"
"s_waitcnt lgkmcnt(5)\n"
"v_mfma_f32_32x32x16_bf16 v[80:95], v[164:167], v[52:55], v[80:95]\n"
"global_load_lds_dwordx4 v31, s[36:37] offset:3072\n"
"s_mov_b32 s40, 128\n"
"s_mov_b32 s41, 245760\n"
"s_cmp_eq_u32 s29, 10\n"
"s_cmov_b32 s40, -1152\n"
"s_cmov_b32 s41, -2211840\n"
"s_cselect_b32 s42, -1, 0\n"
"s_add_u32 s34, s34, s40\n"
"s_addc_u32 s35, s35, s42\n"
"s_add_u32 s36, s36, s41\n"
"s_addc_u32 s37, s37, s42\n"
"s_waitcnt vmcnt(5)\n"
"s_waitcnt lgkmcnt(0)\n"
"s_barrier\n"
"s_setprio 3\n"
"v_subrev_u32 v32, 65536, v32\n"
"v_subrev_u32 v33, 65536, v33\n"
"ds_read_b64_tr_b16 v[48:49], v32 offset:0\n"
"ds_read_b64_tr_b16 v[50:51], v32 offset:1024\n"
"v_subrev_u32 v36, 65536, v36\n"
"v_subrev_u32 v37, 65536, v37\n"
"v_subrev_u32 v38, 65536, v38\n"
"v_subrev_u32 v39, 65536, v39\n"
"ds_read_b128 v[156:159], v36 offset:0\n"
"v_mfma_f32_32x32x16_bf16 v[64:79], v[160:163], v[56:59], v[64:79]\n"
"v_subrev_u32 v34, 65536, v34\n"
"v_subrev_u32 v35, 65536, v35\n"
"ds_read_b64_tr_b16 v[52:53], v34 offset:0\n"
"ds_read_b64_tr_b16 v[54:55], v34 offset:1024\n"
"v_mfma_f32_32x32x16_bf16 v[80:95], v[160:163], v[60:63], v[80:95]\n"
"s_add_u32 m0, s30, 65536\n"
"s_add_u32 s29, s29, 1\n"
"global_load_lds_dwordx4 v28, s[34:35] offset:0\n"
"s_setprio 2\n"
"ds_read_b64_tr_b16 v[56:57], v33 offset:0\n"
"ds_read_b64_tr_b16 v[58:59], v33 offset:1024\n"
"ds_read_b128 v[152:155], v37 offset:0\n"
"s_waitcnt lgkmcnt(5)\n"
"v_mfma_f32_32x32x16_bf16 v[64:79], v[156:159], v[48:51], v[64:79]\n"
"ds_read_b64_tr_b16 v[60:61], v35 offset:0\n"
"ds_read_b64_tr_b16 v[62:63], v35 offset:1024\n"
"s_waitcnt lgkmcnt(5)\n"
"v_mfma_f32_32x32x16_bf16 v[80:95], v[156:159], v[52:55], v[80:95]\n"
"global_load_lds_dwordx4 v29, s[34:35] offset:1024\n"
"s_setprio 1\n"
"ds_read_b64_tr_b16 v[48:49], v32 offset:16384\n"
"ds_read_b64_tr_b16 v[50:51], v32 offset:17408\n"
"ds_read_b128 v[148:151], v38 offset:0\n"
"s_waitcnt lgkmcnt(5)\n"
"v_mfma_f32_32x32x16_bf16 v[64:79], v[152:155], v[56:59], v[64:79]\n"
"ds_read_b64_tr_b16 v[52:53], v34 offset:16384\n"
"ds_read_b64_tr_b16 v[54:55], v34 offset:17408\n"
"s_waitcnt lgkmcnt(5)\n"
"v_mfma_f32_32x32x16_bf16 v[80:95], v[152:155], v[60:63], v[80:95]\n"
"global_load_lds_dwordx4 v30, s[36:37] offset:2048\n"
"s_setprio 0\n"
"s_sub_u32 s40, 4, s46\n"
"s_lshl_b32 s40, s40, 10\n"
"v_mul_u32_u24 v4, s40, v2\n"
"s_lshl_b32 s40, s46, 10\n"
"s_mul_i32 s41, s26, 10\n"
"s_add_u32 s41, s41, s47\n"
"s_lshl_b32 s41, s41, 9\n"
"s_add_u32 s41, s40, s41\n"
"s_add_u32 s41, s41, 16842752\n"
"v_lshl_add_u32 v3, v1, 4, s41\n"
"v_add_u32 v41, v4, v3\n"
"ds_read_b64_tr_b16 v[56:57], v33 offset:16384\n"
"ds_read_b64_tr_b16 v[58:59], v33 offset:17408\n"
"ds_read_b128 v[144:147], v39 offset:0\n"
"s_waitcnt lgkmcnt(5)\n"
"v_mfma_f32_32x32x16_bf16 v[64:79], v[148:151], v[48:51], v[64:79]\n"
"ds_read_b64_tr_b16 v[60:61], v35 offset:16384\n"
"ds_read_b64_tr_b16 v[62:63], v35 offset:17408\n"
"s_waitcnt lgkmcnt(5)\n"
"v_mfma_f32_32x32x16_bf16 v[80:95], v[148:151], v[52:55], v[80:95]\n"
"global_load_lds_dwordx4 v31, s[36:37] offset:3072\n"
"s_add_u32 s34, s34, 128\n"
"s_addc_u32 s35, s35, 0\n"
"s_add_u32 s36, s36, 245760\n"
"s_addc_u32 s37, s37, 0\n"
"global_load_dwordx4 v[44:47], v41, s[18:19] sc1\n"
"s_waitcnt vmcnt(5)\n"
"s_waitcnt lgkmcnt(0)\n"
"s_barrier\n"
"s_setprio 3\n"
"ds_read_b64_tr_b16 v[48:49], v32 offset:32768\n"
"ds_read_b64_tr_b16 v[50:51], v32 offset:33792\n"
"ds_read_b128 v[140:143], v36 offset:32768\n"
"v_mfma_f32_32x32x16_bf16 v[64:79], v[144:147], v[56:59], v[64:79]\n"
"ds_read_b64_tr_b16 v[52:53], v34 offset:32768\n"
"ds_read_b64_tr_b16 v[54:55], v34 offset:33792\n"
"v_mfma_f32_32x32x16_bf16 v[80:95], v[144:147], v[60:63], v[80:95]\n"
"s_add_u32 m0, s30, 0\n"
"s_add_u32 s29, s29, 1\n"
"global_load_lds_dwordx4 v28, s[34:35] offset:0\n"
"s_setprio 2\n"
"ds_read_b64_tr_b16 v[56:57], v33 offset:32768\n"
"ds_read_b64_tr_b16 v[58:59], v33 offset:33792\n"
"ds_read_b128 v[136:139], v37 offset:32768\n"
"s_waitcnt lgkmcnt(5)\n"
"v_mfma_f32_32x32x16_bf16 v[64:79], v[140:143], v[48:51], v[64:79]\n"
"ds_read_b64_tr_b16 v[60:61], v35 offset:32768\n"
"ds_read_b64_tr_b16 v[62:63], v35 offset:33792\n"
"s_waitcnt lgkmcnt(5)\n"
"v_mfma_f32_32x32x16_bf16 v[80:95], v[140:143], v[52:55], v[80:95]\n"
"global_load_lds_dwordx4 v29, s[34:35] offset:1024\n"
"s_setprio 1\n"
"ds_read_b64_tr_b16 v[48:49], v32 offset:49152\n"
"ds_read_b64_tr_b16 v[50:51], v32 offset:50176\n"
"ds_read_b128 v[132:135], v38 offset:32768\n"
"s_waitcnt lgkmcnt(5)\n"
"v_mfma_f32_32x32x16_bf16 v[64:79], v[136:139], v[56:59], v[64:79]\n"
"ds_read_b64_tr_b16 v[52:53], v34 offset:49152\n"
"ds_read_b64_tr_b16 v[54:55], v34 offset:50176\n"
"s_waitcnt lgkmcnt(5)\n"
"v_mfma_f32_32x32x16_bf16 v[80:95], v[136:139], v[60:63], v[80:95]\n"
"global_load_lds_dwordx4 v30, s[36:37] offset:2048\n"
"s_setprio 0\n"
"s_waitcnt vmcnt(3)\n"
"v_cmp_ne_u32 vcc, s24, v45\n"
"v_cmp_ne_u32 s[42:43], s25, v47\n"
"s_nop 0\n"
"s_or_b64 vcc, vcc, s[42:43]\n"
"s_cmp_eq_u64 vcc, 0\n"
"s_cbranch_scc0 retry_44_%=\n"
"ok_45_%=:\n"
"ds_write2st64_b32 v10, v44, v46 offset0:0 offset1:8\n"
"v_xor_b32 v3, s46, v2\n"
"v_and_b32 v3, 1, v3\n"
"s_lshl_b32 s41, s47, 10\n"
"s_add_u32 s41, s41, 110592\n"
"v_lshl_add_u32 v4, v1, 2, s41\n"
"v_lshl_add_u32 v45, v3, 11, v4\n"
"ds_read_b64_tr_b16 v[56:57], v33 offset:49152\n"
"ds_read_b64_tr_b16 v[58:59], v33 offset:50176\n"
"ds_read_b128 v[128:131], v39 offset:32768\n"
"s_waitcnt lgkmcnt(6)\n"
"v_mfma_f32_32x32x16_bf16 v[64:79], v[132:135], v[48:51], v[64:79]\n"
"ds_read_b64_tr_b16 v[60:61], v35 offset:49152\n"
"ds_read_b64_tr_b16 v[62:63], v35 offset:50176\n"
"s_waitcnt lgkmcnt(6)\n"
"v_mfma_f32_32x32x16_bf16 v[80:95], v[132:135], v[52:55], v[80:95]\n"
"global_load_lds_dwordx4 v31, s[36:37] offset:3072\n"
"global_load_dwordx4 v[20:23], v13, s[18:19] sc1\n"
"s_waitcnt vmcnt(6)\n"
"s_waitcnt lgkmcnt(0)\n"
"s_barrier\n"
"ds_read2st64_b32 v[40:41], v45 offset0:0 offset1:1\n"
"ds_read2st64_b32 v[42:43], v45 offset0:2 offset1:3\n"
"ds_read_b32 v44, v45 offset:128\n"
"s_setprio 3\n"
"v_add_u32 v32, 65536, v32\n"
"v_add_u32 v33, 65536, v33\n"
"ds_read_b64_tr_b16 v[48:49], v32 offset:0\n"
"ds_read_b64_tr_b16 v[50:51], v32 offset:1024\n"
"v_add_u32 v36, 65536, v36\n"
"v_add_u32 v37, 65536, v37\n"
"v_add_u32 v38, 65536, v38\n"
"v_add_u32 v39, 65536, v39\n"
"ds_read_b128 v[124:127], v36 offset:0\n"
"v_mfma_f32_32x32x16_bf16 v[64:79], v[128:131], v[56:59], v[64:79]\n"
"s_add_u32 s34, s18, s55\n"
"s_addc_u32 s35, s19, 0\n"
"s_add_u32 s36, s18, s56\n"
"s_addc_u32 s37, s19, 0\n"
"ds_read_b64_tr_b16 v[52:53], v33 offset:0\n"
"ds_read_b64_tr_b16 v[54:55], v33 offset:1024\n"
"ds_read_b128 v[120:123], v37 offset:0\n"
"v_mfma_f32_32x32x16_bf16 v[80:95], v[128:131], v[60:63], v[80:95]\n"
"ds_read_b64_tr_b16 v[56:57], v32 offset:16384\n"
"ds_read_b64_tr_b16 v[58:59], v32 offset:17408\n"
"ds_read_b128 v[116:119], v38 offset:0\n"
"s_add_u32 m0, s33, 139264\n"
"s_nop 0\n"
"global_load_lds_dwordx4 v26, s[38:39] offset:1024\n"
"v_mov_b32 v4, 0\n"
"ds_write_b32 v4, v4 offset:61440\n"
"s_setprio 2\n"
"s_waitcnt lgkmcnt(7)\n"
"v_mfma_f32_32x32x16_bf16 v[64:79], v[124:127], v[48:51], v[64:79]\n"
"ds_read_b64_tr_b16 v[60:61], v33 offset:16384\n"
"ds_read_b64_tr_b16 v[62:63], v33 offset:17408\n"
"ds_read_b128 v[112:115], v39 offset:0\n"
"s_add_u32 m0, s33, 139264\n"
"s_nop 0\n"
"global_load_lds_dwordx4 v27, s[38:39] offset:2048\n"
"s_cmp_eq_u32 s28, 3\n"
"s_cselect_b32 s45, 1966080, 0\n"
"s_add_u32 s38, s38, 393216\n"
"s_addc_u32 s39, s39, 0\n"
"s_sub_u32 s38, s38, s45\n"
"s_subb_u32 s39, s39, 0\n"
"s_waitcnt vmcnt(3)\n"
"s_barrier\n"
"s_waitcnt lgkmcnt(7)\n"
"v_mfma_f32_32x32x16_bf16 v[64:79], v[120:123], v[52:55], v[64:79]\n"
"v_subrev_u32 v32, 65536, v32\n"
"v_subrev_u32 v33, 65536, v33\n"
"ds_read_b64_tr_b16 v[48:49], v32 offset:0\n"
"ds_read_b64_tr_b16 v[50:51], v32 offset:1024\n"
"v_subrev_u32 v36, 65536, v36\n"
"v_subrev_u32 v37, 65536, v37\n"
"v_subrev_u32 v38, 65536, v38\n"
"v_subrev_u32 v39, 65536, v39\n"
"ds_read_b128 v[108:111], v36 offset:0\n"
"s_add_u32 m0, s33, 32768\n"
"s_nop 0\n"
"global_load_lds_dwordx4 v25, s[38:39] offset:0\n"
"v_add_f32 v40, v40, v41\n"
"v_add_f32 v42, v42, v43\n"
"v_add_f32 v40, v40, v44\n"
"v_add_f32 v40, v40, v42\n"
"v_and_b32 v41, 0xffff0000, v40\n"
"v_sub_f32 v40, v40, v41\n"
"v_cvt_pk_bf16_f32 v10, v41, v41\n"
"v_cvt_pk_bf16_f32 v11, v40, v40\n"
"s_waitcnt lgkmcnt(7)\n"
"v_mfma_f32_32x32x16_bf16 v[64:79], v[116:119], v[56:59], v[64:79]\n"
"ds_read_b64_tr_b16 v[52:53], v33 offset:0\n"
"ds_read_b64_tr_b16 v[54:55], v33 offset:1024\n"
"ds_read_b128 v[104:107], v37 offset:0\n"
"s_add_u32 m0, s33, 32768\n"
"s_nop 0\n"
"global_load_lds_dwordx4 v26, s[38:39] offset:1024\n"
"s_setprio 1\n"
"s_waitcnt lgkmcnt(6)\n"
"v_mfma_f32_32x32x16_bf16 v[64:79], v[112:115], v[60:63], v[64:79]\n"
"ds_read_b64_tr_b16 v[56:57], v32 offset:16384\n"
"ds_read_b64_tr_b16 v[58:59], v32 offset:17408\n"
"ds_read_b128 v[100:103], v38 offset:0\n"
"s_add_u32 m0, s33, 32768\n"
"s_nop 0\n"
"global_load_lds_dwordx4 v27, s[38:39] offset:2048\n"
"s_cmp_eq_u32 s28, 2\n"
"s_cselect_b32 s45, 1966080, 0\n"
"s_add_u32 s38, s38, 393216\n"
"s_addc_u32 s39, s39, 0\n"
"s_sub_u32 s38, s38, s45\n"
"s_subb_u32 s39, s39, 0\n"
"s_waitcnt lgkmcnt(6)\n"
"v_mfma_f32_32x32x16_bf16 v[64:79], v[108:111], v[48:51], v[64:79]\n"
"ds_read_b64_tr_b16 v[60:61], v33 offset:16384\n"
"ds_read_b64_tr_b16 v[62:63], v33 offset:17408\n"
"ds_read_b128 v[96:99], v39 offset:0\n"
"s_waitcnt lgkmcnt(6)\n"
"v_mfma_f32_32x32x16_bf16 v[64:79], v[104:107], v[52:55], v[64:79]\n"
"v_add_u32 v34, 65536, v34\n"
"v_add_u32 v35, 65536, v35\n"
"ds_read_b64_tr_b16 v[48:49], v34 offset:0\n"
"ds_read_b64_tr_b16 v[50:51], v34 offset:1024\n"
"s_lshl_b32 s45, s23, 10\n"
"s_add_u32 s40, s98, s45\n"
"s_addc_u32 s41, s99, 0\n"
"s_add_u32 m0, s45, 98304\n"
"s_nop 0\n"
"global_load_lds_dwordx4 v24, s[40:41]\n"
"novt_46_%=:\n"
"s_waitcnt vmcnt(6)\n"
"v_cmp_ne_u32 vcc, s24, v21\n"
"v_cmp_ne_u32 s[42:43], s25, v23\n"
"s_nop 0\n"
"s_or_b64 vcc, vcc, s[42:43]\n"
"s_cmp_eq_u64 vcc, 0\n"
"s_cbranch_scc0 retry_47_%=\n"
"ok_48_%=:\n"
"v_xor_b32 v3, 0x80000000, v20\n"
"v_and_b32 v4, 0xffff0000, v3\n"
"v_sub_f32 v5, v3, v4\n"
"v_cmp_eq_u32 vcc, 0, v2\n"
"v_cvt_pk_bf16_f32 v5, v4, v5\n"
"s_nop 0\n"
"v_cndmask_b32 v18, 0, v5, vcc\n"
"v_cndmask_b32 v19, 0, v5, vcc\n"
"v_cndmask_b32 v14, v5, 0, vcc\n"
"v_cndmask_b32 v15, v5, 0, vcc\n"
"s_setprio 0\n"
"s_lshl_b32 s41, s23, 8\n"
"s_add_u32 s41, s41, 57344\n"
"v_lshl_add_u32 v3, v0, 2, s41\n"
"ds_write_b32 v3, v22\n"
"s_lshl_b32 s41, s23, 8\n"
"s_add_u32 s41, s41, 57344\n"
"v_lshl_add_u32 v3, v2, 4, s41\n"
"ds_read_b128 v[28:31], v3 offset:0\n"
"ds_read_b128 v[36:39], v3 offset:32\n"
"ds_read_b128 v[40:43], v3 offset:64\n"
"ds_read_b128 v[44:47], v3 offset:96\n"
"s_waitcnt lgkmcnt(10)\n"
"v_mfma_f32_32x32x16_bf16 v[64:79], v[100:103], v[56:59], v[64:79]\n"
"ds_read_b64_tr_b16 v[52:53], v35 offset:0\n"
"ds_read_b64_tr_b16 v[54:55], v35 offset:1024\n"
"v_mfma_f32_32x32x8_bf16 v[64:79], v[18:19], v[10:11], v[64:79]\n"
"s_waitcnt lgkmcnt(9)\n"
"v_mfma_f32_32x32x16_bf16 v[64:79], v[96:99], v[60:63], v[64:79]\n"
"ds_read_b64_tr_b16 v[56:57], v34 offset:16384\n"
"ds_read_b64_tr_b16 v[58:59], v34 offset:17408\n"
"s_cmp_eq_u32 s47, 0\n"
"s_cbranch_scc1 novt_49_%=\n"
"s_lshl_b32 s41, s23, 10\n"
"s_add_u32 s45, s41, 4096\n"
"s_add_u32 s40, s98, s45\n"
"s_addc_u32 s41, s99, 0\n"
"s_add_u32 m0, s45, 98304\n"
"s_nop 0\n"
"global_load_lds_dwordx4 v24, s[40:41]\n"
"novt_49_%=:\n"
"s_waitcnt lgkmcnt(9)\n"
"v_mfma_f32_32x32x16_bf16 v[80:95], v[124:127], v[48:51], v[80:95]\n"
"ds_read_b64_tr_b16 v[60:61], v35 offset:16384\n"
"ds_read_b64_tr_b16 v[62:63], v35 offset:17408\n"
"v_mfma_f32_32x32x8_bf16 v[80:95], v[14:15], v[10:11], v[80:95]\n"
"s_waitcnt lgkmcnt(4)\n"
"v_mfma_f32_32x32x16_bf16 v[80:95], v[120:123], v[52:55], v[80:95]\n"
"v_subrev_u32 v34, 65536, v34\n"
"v_subrev_u32 v35, 65536, v35\n"
"ds_read_b64_tr_b16 v[48:49], v34 offset:0\n"
"ds_read_b64_tr_b16 v[50:51], v34 offset:1024\n"
"s_waitcnt lgkmcnt(4)\n"
"v_mfma_f32_32x32x16_bf16 v[80:95], v[116:119], v[56:59], v[80:95]\n"
"ds_read_b64_tr_b16 v[52:53], v35 offset:0\n"
"ds_read_b64_tr_b16 v[54:55], v35 offset:1024\n"
"v_mul_f32 v64, v64, v28\n"
"v_mul_f32 v65, v65, v29\n"
"v_mul_f32 v66, v66, v30\n"
"v_mul_f32 v67, v67, v31\n"
"v_mul_f32 v68, v68, v36\n"
"v_mul_f32 v69, v69, v37\n"
"s_waitcnt lgkmcnt(4)\n"
"v_mfma_f32_32x32x16_bf16 v[80:95], v[112:115], v[60:63], v[80:95]\n"
"ds_read_b64_tr_b16 v[56:57], v34 offset:16384\n"
"ds_read_b64_tr_b16 v[58:59], v34 offset:17408\n"
"v_mul_f32 v70, v70, v38\n"
"v_mul_f32 v71, v71, v39\n"
"v_mul_f32 v72, v72, v40\n"
"v_mul_f32 v73, v73, v41\n"
"v_mul_f32 v74, v74, v42\n"
"v_mul_f32 v75, v75, v43\n"
"s_waitcnt lgkmcnt(4)\n"
"v_mfma_f32_32x32x16_bf16 v[80:95], v[108:111], v[48:51], v[80:95]\n"
"ds_read_b64_tr_b16 v[60:61], v35 offset:16384\n"
"ds_read_b64_tr_b16 v[62:63], v35 offset:17408\n"
"v_mul_f32 v76, v76, v44\n"
"v_mul_f32 v77, v77, v45\n"
"v_mul_f32 v78, v78, v46\n"
"v_mul_f32 v79, v79, v47\n"
"v_cvt_pk_bf16_f32 v64, v64, v65\n"
"v_cvt_pk_bf16_f32 v65, v66, v67\n"
"s_waitcnt lgkmcnt(4)\n"
"v_mfma_f32_32x32x16_bf16 v[80:95], v[104:107], v[52:55], v[80:95]\n"
"v_cvt_pk_bf16_f32 v66, v68, v69\n"
"v_cvt_pk_bf16_f32 v67, v70, v71\n"
"v_cvt_pk_bf16_f32 v68, v72, v73\n"
"v_cvt_pk_bf16_f32 v69, v74, v75\n"
"v_cvt_pk_bf16_f32 v70, v76, v77\n"
"v_cvt_pk_bf16_f32 v71, v78, v79\n"
"s_waitcnt lgkmcnt(2)\n"
"v_mfma_f32_32x32x16_bf16 v[80:95], v[100:103], v[56:59], v[80:95]\n"
"v_permlane32_swap_b32 v64, v66\n"
"v_permlane32_swap_b32 v65, v67\n"
"v_permlane32_swap_b32 v68, v70\n"
"v_permlane32_swap_b32 v69, v71\n"
"global_store_dwordx4 v16, v[64:67], s[34:35] sc1\n"
"s_add_u32 s34, s34, 20480\n"
"s_addc_u32 s35, s35, 0\n"
"global_store_dwordx4 v16, v[68:71], s[34:35] sc1\n"
"s_waitcnt lgkmcnt(0)\n"
"v_mfma_f32_32x32x16_bf16 v[80:95], v[96:99], v[60:63], v[80:95]\n"
"s_nop 11\n"
"v_mul_f32 v80, v80, v28\n"
"v_mul_f32 v81, v81, v29\n"
"v_mul_f32 v82, v82, v30\n"
"v_mul_f32 v83, v83, v31\n"
"v_mul_f32 v84, v84, v36\n"
"v_mul_f32 v85, v85, v37\n"
"v_mul_f32 v86, v86, v38\n"
"v_mul_f32 v87, v87, v39\n"
"v_mul_f32 v88, v88, v40\n"
"v_mul_f32 v89, v89, v41\n"
"v_mul_f32 v90, v90, v42\n"
"v_mul_f32 v91, v91, v43\n"
"v_mul_f32 v92, v92, v44\n"
"v_mul_f32 v93, v93, v45\n"
"v_mul_f32 v94, v94, v46\n"
"v_mul_f32 v95, v95, v47\n"
"v_cvt_pk_bf16_f32 v80, v80, v81\n"
"v_cvt_pk_bf16_f32 v81, v82, v83\n"
"v_cvt_pk_bf16_f32 v82, v84, v85\n"
"v_cvt_pk_bf16_f32 v83, v86, v87\n"
"v_cvt_pk_bf16_f32 v84, v88, v89\n"
"v_cvt_pk_bf16_f32 v85, v90, v91\n"
"v_cvt_pk_bf16_f32 v86, v92, v93\n"
"v_cvt_pk_bf16_f32 v87, v94, v95\n"
"v_permlane32_swap_b32 v80, v82\n"
"v_permlane32_swap_b32 v81, v83\n"
"v_permlane32_swap_b32 v84, v86\n"
"v_permlane32_swap_b32 v85, v87\n"
"global_store_dwordx4 v16, v[80:83], s[36:37] sc1\n"
"s_add_u32 s36, s36, 20480\n"
"s_addc_u32 s37, s37, 0\n"
"global_store_dwordx4 v16, v[84:87], s[36:37] sc1\n"
"p2_5_%=:\n"
"s_cmp_eq_u32 s47, 1\n"
"s_cbranch_scc1 p2b_6_%=\n"
"v_add_u32 v10, 98304, v24\n"
"ds_read_b64_tr_b16 v[48:49], v6 offset:0\n"
"ds_read_b64_tr_b16 v[50:51], v6 offset:768\n"
"ds_read_b64_tr_b16 v[52:53], v8 offset:0\n"
"ds_read_b64_tr_b16 v[54:55], v8 offset:768\n"
"ds_read_b64_tr_b16 v[56:57], v7 offset:0\n"
"ds_read_b64_tr_b16 v[58:59], v7 offset:768\n"
"ds_read_b64_tr_b16 v[60:61], v9 offset:0\n"
"ds_read_b64_tr_b16 v[62:63], v9 offset:768\n"
"s_waitcnt lgkmcnt(4)\n"
"v_mfma_f32_32x32x16_bf16 v[64:79], v[48:51], v[252:255], 0\n"
"ds_read_b64_tr_b16 v[32:33], v6 offset:6144\n"
"ds_read_b64_tr_b16 v[34:35], v6 offset:6912\n"
"v_mfma_f32_32x32x16_bf16 v[80:95], v[52:55], v[252:255], 0\n"
"ds_read_b64_tr_b16 v[36:37], v8 offset:6144\n"
"ds_read_b64_tr_b16 v[38:39], v8 offset:6912\n"
"s_waitcnt lgkmcnt(4)\n"
"v_mfma_f32_32x32x16_bf16 v[64:79], v[56:59], v[248:251], v[64:79]\n"
"ds_read_b64_tr_b16 v[48:49], v7 offset:6144\n"
"ds_read_b64_tr_b16 v[50:51], v7 offset:6912\n"
"v_mfma_f32_32x32x16_bf16 v[80:95], v[60:63], v[248:251], v[80:95]\n"
"ds_read_b64_tr_b16 v[52:53], v9 offset:6144\n"
"ds_read_b64_tr_b16 v[54:55], v9 offset:6912\n"
"s_waitcnt lgkmcnt(4)\n"
"v_mfma_f32_32x32x16_bf16 v[64:79], v[32:35], v[244:247], v[64:79]\n"
"ds_read_b64_tr_b16 v[56:57], v6 offset:12288\n"
"ds_read_b64_tr_b16 v[58:59], v6 offset:13056\n"
"v_mfma_f32_32x32x16_bf16 v[80:95], v[36:39], v[244:247], v[80:95]\n"
"ds_read_b64_tr_b16 v[60:61], v8 offset:12288\n"
"ds_read_b64_tr_b16 v[62:63], v8 offset:13056\n"
"s_waitcnt lgkmcnt(4)\n"
"v_mfma_f32_32x32x16_bf16 v[64:79], v[48:51], v[240:243], v[64:79]\n"
"ds_read_b64_tr_b16 v[32:33], v7 offset:12288\n"
"ds_read_b64_tr_b16 v[34:35], v7 offset:13056\n"
"v_mfma_f32_32x32x16_bf16 v[80:95], v[52:55], v[240:243], v[80:95]\n"
"ds_read_b64_tr_b16 v[36:37], v9 offset:12288\n"
"ds_read_b64_tr_b16 v[38:39], v9 offset:13056\n"
"s_waitcnt lgkmcnt(4)\n"
"v_mfma_f32_32x32x16_bf16 v[64:79], v[56:59], v[236:239], v[64:79]\n"
"ds_read_b64_tr_b16 v[48:49], v6 offset:18432\n"
"ds_read_b64_tr_b16 v[50:51], v6 offset:19200\n"
"v_mfma_f32_32x32x16_bf16 v[80:95], v[60:63], v[236:239], v[80:95]\n"
"ds_read_b64_tr_b16 v[52:53], v8 offset:18432\n"
"ds_read_b64_tr_b16 v[54:55], v8 offset:19200\n"
"s_waitcnt vmcnt(4)\n"
"s_barrier\n"
"s_waitcnt lgkmcnt(4)\n"
"v_mfma_f32_32x32x16_bf16 v[64:79], v[32:35], v[232:235], v[64:79]\n"
"ds_read_b64_tr_b16 v[56:57], v7 offset:18432\n"
"ds_read_b64_tr_b16 v[58:59], v7 offset:19200\n"
"v_mfma_f32_32x32x16_bf16 v[80:95], v[36:39], v[232:235], v[80:95]\n"
"ds_read_b64_tr_b16 v[60:61], v9 offset:18432\n"
"ds_read_b64_tr_b16 v[62:63], v9 offset:19200\n"
"s_waitcnt lgkmcnt(4)\n"
"v_mfma_f32_32x32x16_bf16 v[64:79], v[48:51], v[228:231], v[64:79]\n"
"ds_read_b64_tr_b16 v[32:33], v6 offset:24576\n"
"ds_read_b64_tr_b16 v[34:35], v6 offset:25344\n"
"v_mfma_f32_32x32x16_bf16 v[80:95], v[52:55], v[228:231], v[80:95]\n"
"ds_read_b64_tr_b16 v[36:37], v8 offset:24576\n"
"ds_read_b64_tr_b16 v[38:39], v8 offset:25344\n"
"s_waitcnt vmcnt(0)\n"
"v_mov_b32 v4, 0\n"
"v_mov_b32 v5, 1\n"
"s_mov_b64 exec, 1\n"
"ds_add_u32 v4, v5 offset:61440\n"
"s_mov_b64 exec, -1\n"
"s_waitcnt lgkmcnt(5)\n"
"v_mfma_f32_32x32x16_bf16 v[64:79], v[56:59], v[224:227], v[64:79]\n"
"ds_read_b64_tr_b16 v[48:49], v7 offset:24576\n"
"ds_read_b64_tr_b16 v[50:51], v7 offset:25344\n"
"v_mfma_f32_32x32x16_bf16 v[80:95], v[60:63], v[224:227], v[80:95]\n"
"ds_read_b64_tr_b16 v[52:53], v9 offset:24576\n"
"ds_read_b64_tr_b16 v[54:55], v9 offset:25344\n"
"s_waitcnt lgkmcnt(5)\n"
"v_mfma_f32_32x32x16_bf16 v[64:79], v[32:35], v[220:223], v[64:79]\n"
"ds_read_b64_tr_b16 v[56:57], v6 offset:30720\n"
"ds_read_b64_tr_b16 v[58:59], v6 offset:31488\n"
"v_mfma_f32_32x32x16_bf16 v[80:95], v[36:39], v[220:223], v[80:95]\n"
"ds_read_b64_tr_b16 v[60:61], v8 offset:30720\n"
"ds_read_b64_tr_b16 v[62:63], v8 offset:31488\n"
"ds_read_b128 v[224:227], v10 offset:0\n"
"s_waitcnt lgkmcnt(5)\n"
"v_mfma_f32_32x32x16_bf16 v[64:79], v[48:51], v[216:219], v[64:79]\n"
"ds_read_b64_tr_b16 v[32:33], v7 offset:30720\n"
"ds_read_b64_tr_b16 v[34:35], v7 offset:31488\n"
"v_mfma_f32_32x32x16_bf16 v[80:95], v[52:55], v[216:219], v[80:95]\n"
"ds_read_b64_tr_b16 v[36:37], v9 offset:30720\n"
"ds_read_b64_tr_b16 v[38:39], v9 offset:31488\n"
"ds_read_b128 v[228:231], v10 offset:1024\n"
"s_waitcnt lgkmcnt(6)\n"
"v_mfma_f32_32x32x16_bf16 v[64:79], v[56:59], v[212:215], v[64:79]\n"
"ds_read_b64_tr_b16 v[48:49], v6 offset:36864\n"
"ds_read_b64_tr_b16 v[50:51], v6 offset:37632\n"
"v_mfma_f32_32x32x16_bf16 v[80:95], v[60:63], v[212:215], v[80:95]\n"
"ds_read_b64_tr_b16 v[52:53], v8 offset:36864\n"
"ds_read_b64_tr_b16 v[54:55], v8 offset:37632\n"
"ds_read_b128 v[232:235], v10 offset:2048\n"
"s_waitcnt lgkmcnt(6)\n"
"v_mfma_f32_32x32x16_bf16 v[64:79], v[32:35], v[208:211], v[64:79]\n"
"ds_read_b64_tr_b16 v[56:57], v7 offset:36864\n"
"ds_read_b64_tr_b16 v[58:59], v7 offset:37632\n"
"v_mfma_f32_32x32x16_bf16 v[80:95], v[36:39], v[208:211], v[80:95]\n"
"ds_read_b64_tr_b16 v[60:61], v9 offset:36864\n"
"ds_read_b64_tr_b16 v[62:63], v9 offset:37632\n"
"ds_read_b128 v[236:239], v10 offset:3072\n"
"s_waitcnt lgkmcnt(6)\n"
"v_mfma_f32_32x32x16_bf16 v[64:79], v[48:51], v[204:207], v[64:79]\n"
"ds_read_b64_tr_b16 v[32:33], v6 offset:43008\n"
"ds_read_b64_tr_b16 v[34:35], v6 offset:43776\n"
"v_mfma_f32_32x32x16_bf16 v[80:95], v[52:55], v[204:207], v[80:95]\n"
"ds_read_b64_tr_b16 v[36:37], v8 offset:43008\n"
"ds_read_b64_tr_b16 v[38:39], v8 offset:43776\n"
"ds_read_b128 v[248:251], v10 offset:4096\n"
"s_waitcnt lgkmcnt(6)\n"
"v_mfma_f32_32x32x16_bf16 v[64:79], v[56:59], v[200:203], v[64:79]\n"
"ds_read_b64_tr_b16 v[48:49], v7 offset:43008\n"
"ds_read_b64_tr_b16 v[50:51], v7 offset:43776\n"
"v_mfma_f32_32x32x16_bf16 v[80:95], v[60:63], v[200:203], v[80:95]\n"
"ds_read_b64_tr_b16 v[52:53], v9 offset:43008\n"
"ds_read_b64_tr_b16 v[54:55], v9 offset:43776\n"
"ds_read_b128 v[252:255], v10 offset:5120\n"
"s_waitcnt lgkmcnt(6)\n"
"v_mfma_f32_32x32x16_bf16 v[64:79], v[32:35], v[196:199], v[64:79]\n"
"v_subrev_u32 v6, 81920, v6\n"
"v_subrev_u32 v7, 81920, v7\n"
"v_subrev_u32 v8, 81920, v8\n"
"v_subrev_u32 v9, 81920, v9\n"
"ds_read_b64_tr_b16 v[56:57], v6 offset:0\n"
"ds_read_b64_tr_b16 v[58:59], v6 offset:768\n"
"v_mfma_f32_32x32x16_bf16 v[80:95], v[36:39], v[196:199], v[80:95]\n"
"ds_read_b64_tr_b16 v[60:61], v8 offset:0\n"
"ds_read_b64_tr_b16 v[62:63], v8 offset:768\n"
"s_waitcnt lgkmcnt(5)\n"
"v_mfma_f32_32x32x16_bf16 v[64:79], v[48:51], v[192:195], v[64:79]\n"
"ds_read_b64_tr_b16 v[32:33], v7 offset:0\n"
"ds_read_b64_tr_b16 v[34:35], v7 offset:768\n"
"v_mfma_f32_32x32x16_bf16 v[80:95], v[52:55], v[192:195], v[80:95]\n"
"ds_read_b64_tr_b16 v[36:37], v9 offset:0\n"
"ds_read_b64_tr_b16 v[38:39], v9 offset:768\n"
"s_waitcnt lgkmcnt(4)\n"
"v_mfma_f32_32x32x16_bf16 v[64:79], v[56:59], v[188:191], v[64:79]\n"
"ds_read_b64_tr_b16 v[48:49], v6 offset:6144\n"
"ds_read_b64_tr_b16 v[50:51], v6 offset:6912\n"
"v_mfma_f32_32x32x16_bf16 v[80:95], v[60:63], v[188:191], v[80:95]\n"
"ds_read_b64_tr_b16 v[52:53], v8 offset:6144\n"
"ds_read_b64_tr_b16 v[54:55], v8 offset:6912\n"
"s_waitcnt lgkmcnt(4)\n"
"v_mfma_f32_32x32x16_bf16 v[64:79], v[32:35], v[184:187], v[64:79]\n"
"ds_read_b64_tr_b16 v[56:57], v7 offset:6144\n"
"ds_read_b64_tr_b16 v[58:59], v7 offset:6912\n"
"v_mfma_f32_32x32x16_bf16 v[80:95], v[36:39], v[184:187], v[80:95]\n"
"ds_read_b64_tr_b16 v[60:61], v9 offset:6144\n"
"ds_read_b64_tr_b16 v[62:63], v9 offset:6912\n"
"s_waitcnt lgkmcnt(4)\n"
"v_mfma_f32_32x32x16_bf16 v[64:79], v[48:51], v[180:183], v[64:79]\n"
"ds_read_b64_tr_b16 v[32:33], v6 offset:12288\n"
"ds_read_b64_tr_b16 v[34:35], v6 offset:13056\n"
"v_mfma_f32_32x32x16_bf16 v[80:95], v[52:55], v[180:183], v[80:95]\n"
"ds_read_b64_tr_b16 v[36:37], v8 offset:12288\n"
"ds_read_b64_tr_b16 v[38:39], v8 offset:13056\n"
"v_mov_b32 v192, 0\n"
"v_mov_b32 v193, 0\n"
"s_waitcnt lgkmcnt(4)\n"
"v_mfma_f32_32x32x16_bf16 v[64:79], v[56:59], v[176:179], v[64:79]\n"
"ds_read_b64_tr_b16 v[48:49], v7 offset:12288\n"
"ds_read_b64_tr_b16 v[50:51], v7 offset:13056\n"
"v_mfma_f32_32x32x16_bf16 v[80:95], v[60:63], v[176:179], v[80:95]\n"
"ds_read_b64_tr_b16 v[52:53], v9 offset:12288\n"
"ds_read_b64_tr_b16 v[54:55], v9 offset:13056\n"
"v_mov_b32 v194, 0\n"
"v_mov_b32 v195, 0\n"
"s_waitcnt lgkmcnt(4)\n"
"v_mfma_f32_32x32x16_bf16 v[64:79], v[32:35], v[172:175], v[64:79]\n"
"ds_read_b64_tr_b16 v[56:57], v6 offset:18432\n"
"ds_read_b64_tr_b16 v[58:59], v6 offset:19200\n"
"v_mfma_f32_32x32x16_bf16 v[80:95], v[36:39], v[172:175], v[80:95]\n"
"ds_read_b64_tr_b16 v[60:61], v8 offset:18432\n"
"ds_read_b64_tr_b16 v[62:63], v8 offset:19200\n"
"global_load_dwordx4 v[44:47], v12, s[18:19] sc1\n"
"v_mov_b32 v196, 0\n"
"v_mov_b32 v197, 0\n"
"s_waitcnt lgkmcnt(4)\n"
"v_mfma_f32_32x32x16_bf16 v[64:79], v[48:51], v[168:171], v[64:79]\n"
"ds_read_b64_tr_b16 v[32:33], v7 offset:18432\n"
"ds_read_b64_tr_b16 v[34:35], v7 offset:19200\n"
"v_mfma_f32_32x32x16_bf16 v[80:95], v[52:55], v[168:171], v[80:95]\n"
"ds_read_b64_tr_b16 v[36:37], v9 offset:18432\n"
"ds_read_b64_tr_b16 v[38:39], v9 offset:19200\n"
"v_mov_b32 v198, 0\n"
"v_mov_b32 v199, 0\n"
"s_barrier\n"
"s_waitcnt lgkmcnt(4)\n"
"v_mfma_f32_32x32x16_bf16 v[64:79], v[56:59], v[164:167], v[64:79]\n"
"ds_read_b64_tr_b16 v[48:49], v6 offset:32768\n"
"ds_read_b64_tr_b16 v[50:51], v6 offset:33536\n"
"v_mfma_f32_32x32x16_bf16 v[80:95], v[60:63], v[164:167], v[80:95]\n"
"ds_read_b64_tr_b16 v[52:53], v8 offset:32768\n"
"ds_read_b64_tr_b16 v[54:55], v8 offset:33536\n"
"v_mov_b32 v200, 0\n"
"v_mov_b32 v201, 0\n"
"s_waitcnt lgkmcnt(4)\n"
"v_mfma_f32_32x32x16_bf16 v[64:79], v[32:35], v[160:163], v[64:79]\n"
"ds_read_b64_tr_b16 v[56:57], v7 offset:32768\n"
"ds_read_b64_tr_b16 v[58:59], v7 offset:33536\n"
"v_mfma_f32_32x32x16_bf16 v[80:95], v[36:39], v[160:163], v[80:95]\n"
"ds_read_b64_tr_b16 v[60:61], v9 offset:32768\n"
"ds_read_b64_tr_b16 v[62:63], v9 offset:33536\n"
"v_mov_b32 v202, 0\n"
"v_mov_b32 v203, 0\n"
"s_waitcnt lgkmcnt(4)\n"
"v_mfma_f32_32x32x16_bf16 v[64:79], v[48:51], v[156:159], v[64:79]\n"
"ds_read_b64_tr_b16 v[32:33], v6 offset:38912\n"
"ds_read_b64_tr_b16 v[34:35], v6 offset:39680\n"
"v_mfma_f32_32x32x16_bf16 v[80:95], v[52:55], v[156:159], v[80:95]\n"
"ds_read_b64_tr_b16 v[36:37], v8 offset:38912\n"
"ds_read_b64_tr_b16 v[38:39], v8 offset:39680\n"
"v_mov_b32 v204, 0\n"
"v_mov_b32 v205, 0\n"
"s_waitcnt lgkmcnt(4)\n"
"v_mfma_f32_32x32x16_bf16 v[64:79], v[56:59], v[152:155], v[64:79]\n"
"ds_read_b64_tr_b16 v[48:49], v7 offset:38912\n"
"ds_read_b64_tr_b16 v[50:51], v7 offset:39680\n"
"v_mfma_f32_32x32x16_bf16 v[80:95], v[60:63], v[152:155], v[80:95]\n"
"ds_read_b64_tr_b16 v[52:53], v9 offset:38912\n"
"ds_read_b64_tr_b16 v[54:55], v9 offset:39680\n"
"v_mov_b32 v206, 0\n"
"v_mov_b32 v207, 0\n"
"s_barrier\n"
"s_waitcnt lgkmcnt(4)\n"
"v_mfma_f32_32x32x16_bf16 v[64:79], v[32:35], v[148:151], v[64:79]\n"
"ds_read_b64_tr_b16 v[56:57], v6 offset:45056\n"
"ds_read_b64_tr_b16 v[58:59], v6 offset:45824\n"
"v_mfma_f32_32x32x16_bf16 v[80:95], v[36:39], v[148:151], v[80:95]\n"
"ds_read_b64_tr_b16 v[60:61], v8 offset:45056\n"
"ds_read_b64_tr_b16 v[62:63], v8 offset:45824\n"
"v_mov_b32 v208, 0\n"
"v_mov_b32 v209, 0\n"
"s_waitcnt lgkmcnt(4)\n"
"v_mfma_f32_32x32x16_bf16 v[64:79], v[48:51], v[144:147], v[64:79]\n"
"ds_read_b64_tr_b16 v[32:33], v7 offset:45056\n"
"ds_read_b64_tr_b16 v[34:35], v7 offset:45824\n"
"v_mfma_f32_32x32x16_bf16 v[80:95], v[52:55], v[144:147], v[80:95]\n"
"ds_read_b64_tr_b16 v[36:37], v9 offset:45056\n"
"ds_read_b64_tr_b16 v[38:39], v9 offset:45824\n"
"v_mov_b32 v210, 0\n"
"v_mov_b32 v211, 0\n"
"s_waitcnt lgkmcnt(4)\n"
"v_mfma_f32_32x32x16_bf16 v[64:79], v[56:59], v[140:143], v[64:79]\n"
"ds_read_b64_tr_b16 v[48:49], v6 offset:51200\n"
"ds_read_b64_tr_b16 v[50:51], v6 offset:51968\n"
"v_mfma_f32_32x32x16_bf16 v[80:95], v[60:63], v[140:143], v[80:95]\n"
"ds_read_b64_tr_b16 v[52:53], v8 offset:51200\n"
"ds_read_b64_tr_b16 v[54:55], v8 offset:51968\n"
"v_mov_b32 v212, 0\n"
"v_mov_b32 v213, 0\n"
"s_waitcnt lgkmcnt(4)\n"
"v_mfma_f32_32x32x16_bf16 v[64:79], v[32:35], v[136:139], v[64:79]\n"
"ds_read_b64_tr_b16 v[56:57], v7 offset:51200\n"
"ds_read_b64_tr_b16 v[58:59], v7 offset:51968\n"
"v_mfma_f32_32x32x16_bf16 v[80:95], v[36:39], v[136:139], v[80:95]\n"
"ds_read_b64_tr_b16 v[60:61], v9 offset:51200\n"
"ds_read_b64_tr_b16 v[62:63], v9 offset:51968\n"
"v_mov_b32 v214, 0\n"
"v_mov_b32 v215, 0\n"
"s_waitcnt lgkmcnt(4)\n"
"v_mfma_f32_32x32x16_bf16 v[64:79], v[48:51], v[132:135], v[64:79]\n"
"v_subrev_u32 v6, 32768, v6\n"
"v_subrev_u32 v7, 32768, v7\n"
"v_subrev_u32 v8, 32768, v8\n"
"v_subrev_u32 v9, 32768, v9\n"
"ds_read_b64_tr_b16 v[32:33], v6 offset:0\n"
"ds_read_b64_tr_b16 v[34:35], v6 offset:768\n"
"v_mfma_f32_32x32x16_bf16 v[80:95], v[52:55], v[132:135], v[80:95]\n"
"ds_read_b64_tr_b16 v[36:37], v8 offset:0\n"
"ds_read_b64_tr_b16 v[38:39], v8 offset:768\n"
"v_mov_b32 v216, 0\n"
"v_mov_b32 v217, 0\n"
"s_waitcnt lgkmcnt(4)\n"
"v_mfma_f32_32x32x16_bf16 v[64:79], v[56:59], v[128:131], v[64:79]\n"
"ds_read_b64_tr_b16 v[48:49], v7 offset:0\n"
"ds_read_b64_tr_b16 v[50:51], v7 offset:768\n"
"v_mfma_f32_32x32x16_bf16 v[80:95], v[60:63], v[128:131], v[80:95]\n"
"ds_read_b64_tr_b16 v[52:53], v9 offset:0\n"
"ds_read_b64_tr_b16 v[54:55], v9 offset:768\n"
"v_mov_b32 v218, 0\n"
"v_mov_b32 v219, 0\n"
"s_waitcnt lgkmcnt(4)\n"
"v_mfma_f32_32x32x16_bf16 v[64:79], v[32:35], v[124:127], v[64:79]\n"
"ds_read_b64_tr_b16 v[56:57], v6 offset:6144\n"
"ds_read_b64_tr_b16 v[58:59], v6 offset:6912\n"
"v_mfma_f32_32x32x16_bf16 v[80:95], v[36:39], v[124:127], v[80:95]\n"
"ds_read_b64_tr_b16 v[60:61], v8 offset:6144\n"
"ds_read_b64_tr_b16 v[62:63], v8 offset:6912\n"
"v_mov_b32 v220, 0\n"
"v_mov_b32 v221, 0\n"
"s_waitcnt lgkmcnt(4)\n"
"v_mfma_f32_32x32x16_bf16 v[64:79], v[48:51], v[120:123], v[64:79]\n"
"ds_read_b64_tr_b16 v[32:33], v7 offset:6144\n"
"ds_read_b64_tr_b16 v[34:35], v7 offset:6912\n"
"v_mfma_f32_32x32x16_bf16 v[80:95], v[52:55], v[120:123], v[80:95]\n"
"ds_read_b64_tr_b16 v[36:37], v9 offset:6144\n"
"ds_read_b64_tr_b16 v[38:39], v9 offset:6912\n"
"v_mov_b32 v222, 0\n"
"v_mov_b32 v223, 0\n"
"s_waitcnt lgkmcnt(4)\n"
"v_mfma_f32_32x32x16_bf16 v[64:79], v[56:59], v[116:119], v[64:79]\n"
"ds_read_b64_tr_b16 v[48:49], v6 offset:12288\n"
"ds_read_b64_tr_b16 v[50:51], v6 offset:13056\n"
"v_mfma_f32_32x32x16_bf16 v[80:95], v[60:63], v[116:119], v[80:95]\n"
"ds_read_b64_tr_b16 v[52:53], v8 offset:12288\n"
"ds_read_b64_tr_b16 v[54:55], v8 offset:13056\n"
"v_mov_b32 v242, s24\n"
"v_mov_b32 v243, s25\n"
"s_waitcnt vmcnt(0)\n"
"v_cmp_ne_u32 vcc, s24, v45\n"
"v_cmp_ne_u32 s[42:43], s25, v47\n"
"s_nop 0\n"
"s_or_b64 vcc, vcc, s[42:43]\n"
"s_cmp_eq_u64 vcc, 0\n"
"s_cbranch_scc0 retry_50_%=\n"
"ok_51_%=:\n"
"v_mov_b32 v45, v46\n"
"s_waitcnt lgkmcnt(4)\n"
"v_mfma_f32_32x32x16_bf16 v[64:79], v[32:35], v[112:115], v[64:79]\n"
"ds_read_b64_tr_b16 v[56:57], v7 offset:12288\n"
"ds_read_b64_tr_b16 v[58:59], v7 offset:13056\n"
"v_mfma_f32_32x32x16_bf16 v[80:95], v[36:39], v[112:115], v[80:95]\n"
"ds_read_b64_tr_b16 v[60:61], v9 offset:12288\n"
"ds_read_b64_tr_b16 v[62:63], v9 offset:13056\n"
"v_mov_b32 v240, 0xdddddddd\n"
"s_add_u32 s41, s33, 114688\n"
"v_add_u32 v244, s41, v24\n"
"v_mfma_f32_32x32x8_bf16 v[64:79], v[44:45], v[18:19], v[64:79]\n"
"v_mfma_f32_32x32x8_bf16 v[80:95], v[44:45], v[14:15], v[80:95]\n"
"s_waitcnt lgkmcnt(4)\n"
"v_mfma_f32_32x32x16_bf16 v[64:79], v[48:51], v[108:111], v[64:79]\n"
"ds_read_b64_tr_b16 v[32:33], v6 offset:18432\n"
"ds_read_b64_tr_b16 v[34:35], v6 offset:19200\n"
"v_mfma_f32_32x32x16_bf16 v[80:95], v[52:55], v[108:111], v[80:95]\n"
"ds_read_b64_tr_b16 v[36:37], v8 offset:18432\n"
"ds_read_b64_tr_b16 v[38:39], v8 offset:19200\n"
"v_add_u32 v245, 12288, v244\n"
"v_add_u32 v188, s33, v253\n"
"s_waitcnt lgkmcnt(4)\n"
"v_mfma_f32_32x32x16_bf16 v[64:79], v[56:59], v[104:107], v[64:79]\n"
"ds_read_b64_tr_b16 v[48:49], v7 offset:18432\n"
"ds_read_b64_tr_b16 v[50:51], v7 offset:19200\n"
"v_mfma_f32_32x32x16_bf16 v[80:95], v[60:63], v[104:107], v[80:95]\n"
"ds_read_b64_tr_b16 v[52:53], v9 offset:18432\n"
"ds_read_b64_tr_b16 v[54:55], v9 offset:19200\n"
"v_xor_b32 v189, 512, v188\n"
"v_add_u32 v190, s33, v254\n"
"s_waitcnt lgkmcnt(4)\n"
"v_mfma_f32_32x32x16_bf16 v[64:79], v[32:35], v[100:103], v[64:79]\n"
"v_mfma_f32_32x32x16_bf16 v[80:95], v[36:39], v[100:103], v[80:95]\n"
"v_xor_b32 v191, 512, v190\n"
"v_mov_b32 v184, v17\n"
"s_waitcnt lgkmcnt(0)\n"
"v_mfma_f32_32x32x16_bf16 v[64:79], v[48:51], v[96:99], v[64:79]\n"
"v_mfma_f32_32x32x16_bf16 v[80:95], v[52:55], v[96:99], v[80:95]\n"
"s_add_u32 s34, s18, s59\n"
"s_addc_u32 s35, s19, 0\n"
"s_add_u32 s34, s34, 4096\n"
"s_addc_u32 s35, s35, 0\n"
"s_nop 6\n"
"v_cvt_pk_bf16_f32 v168, v64, v65\n"
"v_cvt_pk_bf16_f32 v169, v66, v67\n"
"v_cvt_pk_bf16_f32 v170, v68, v69\n"
"v_cvt_pk_bf16_f32 v171, v70, v71\n"
"v_cvt_pk_bf16_f32 v172, v72, v73\n"
"v_cvt_pk_bf16_f32 v173, v74, v75\n"
"v_cvt_pk_bf16_f32 v174, v76, v77\n"
"v_cvt_pk_bf16_f32 v175, v78, v79\n"
"v_pk_max_i16 v168, v168, 0\n"
"v_pk_max_i16 v169, v169, 0\n"
"v_pk_max_i16 v170, v170, 0\n"
"v_pk_max_i16 v171, v171, 0\n"
"v_pk_max_i16 v172, v172, 0\n"
"v_pk_max_i16 v173, v173, 0\n"
"v_pk_max_i16 v174, v174, 0\n"
"v_pk_max_i16 v175, v175, 0\n"
"v_permlane32_swap_b32 v168, v170\n"
"v_permlane32_swap_b32 v169, v171\n"
"v_permlane32_swap_b32 v172, v174\n"
"v_permlane32_swap_b32 v173, v175\n"
"v_cvt_pk_bf16_f32 v176, v80, v81\n"
"v_cvt_pk_bf16_f32 v177, v82, v83\n"
"v_cvt_pk_bf16_f32 v178, v84, v85\n"
"v_cvt_pk_bf16_f32 v179, v86, v87\n"
"v_cvt_pk_bf16_f32 v180, v88, v89\n"
"v_cvt_pk_bf16_f32 v181, v90, v91\n"
"v_cvt_pk_bf16_f32 v182, v92, v93\n"
"v_cvt_pk_bf16_f32 v183, v94, v95\n"
"v_pk_max_i16 v176, v176, 0\n"
"v_pk_max_i16 v177, v177, 0\n"
"v_pk_max_i16 v178, v178, 0\n"
"v_pk_max_i16 v179, v179, 0\n"
"v_pk_max_i16 v180, v180, 0\n"
"v_pk_max_i16 v181, v181, 0\n"
"v_pk_max_i16 v182, v182, 0\n"
"v_pk_max_i16 v183, v183, 0\n"
"v_permlane32_swap_b32 v176, v178\n"
"v_permlane32_swap_b32 v177, v179\n"
"v_permlane32_swap_b32 v180, v182\n"
"v_permlane32_swap_b32 v181, v183\n"
"s_branch att_7_%=\n"
"p2b_6_%=:\n"
"s_setprio 1\n"
"v_add_u32 v10, 104448, v24\n"
"ds_read_b64_tr_b16 v[48:49], v6 offset:0\n"
"ds_read_b64_tr_b16 v[50:51], v6 offset:768\n"
"ds_read_b64_tr_b16 v[52:53], v7 offset:0\n"
"ds_read_b64_tr_b16 v[54:55], v7 offset:768\n"
"ds_read_b64_tr_b16 v[56:57], v6 offset:6144\n"
"ds_read_b64_tr_b16 v[58:59], v6 offset:6912\n"
"s_waitcnt lgkmcnt(4)\n"
"v_mfma_f32_32x32x16_bf16 v[64:79], v[48:51], v[252:255], 0\n"
"ds_read_b64_tr_b16 v[60:61], v7 offset:6144\n"
"ds_read_b64_tr_b16 v[62:63], v7 offset:6912\n"
"s_waitcnt lgkmcnt(4)\n"
"v_mfma_f32_32x32x16_bf16 v[64:79], v[52:55], v[248:251], v[64:79]\n"
"ds_read_b64_tr_b16 v[48:49], v6 offset:12288\n"
"ds_read_b64_tr_b16 v[50:51], v6 offset:13056\n"
"s_waitcnt lgkmcnt(4)\n"
"v_mfma_f32_32x32x16_bf16 v[64:79], v[56:59], v[244:247], v[64:79]\n"
"ds_read_b64_tr_b16 v[52:53], v7 offset:12288\n"
"ds_read_b64_tr_b16 v[54:55], v7 offset:13056\n"
"s_waitcnt lgkmcnt(4)\n"
"v_mfma_f32_32x32x16_bf16 v[64:79], v[60:63], v[240:243], v[64:79]\n"
"ds_read_b64_tr_b16 v[56:57], v6 offset:18432\n"
"ds_read_b64_tr_b16 v[58:59], v6 offset:19200\n"
"s_waitcnt lgkmcnt(4)\n"
"v_mfma_f32_32x32x16_bf16 v[64:79], v[48:51], v[236:239], v[64:79]\n"
"ds_read_b64_tr_b16 v[60:61], v7 offset:18432\n"
"ds_read_b64_tr_b16 v[62:63], v7 offset:19200\n"
"s_waitcnt vmcnt(4)\n"
"s_barrier\n"
"s_waitcnt lgkmcnt(4)\n"
"v_mfma_f32_32x32x16_bf16 v[64:79], v[52:55], v[232:235], v[64:79]\n"
"ds_read_b64_tr_b16 v[48:49], v6 offset:24576\n"
"ds_read_b64_tr_b16 v[50:51], v6 offset:25344\n"
"s_waitcnt lgkmcnt(4)\n"
"v_mfma_f32_32x32x16_bf16 v[64:79], v[56:59], v[228:231], v[64:79]\n"
"ds_read_b64_tr_b16 v[52:53], v7 offset:24576\n"
"ds_read_b64_tr_b16 v[54:55], v7 offset:25344\n"
"s_waitcnt lgkmcnt(4)\n"
"v_mfma_f32_32x32x16_bf16 v[64:79], v[60:63], v[224:227], v[64:79]\n"
"ds_read_b64_tr_b16 v[56:57], v6 offset:30720\n"
"ds_read_b64_tr_b16 v[58:59], v6 offset:31488\n"
"s_waitcnt lgkmcnt(4)\n"
"v_mfma_f32_32x32x16_bf16 v[64:79], v[48:51], v[220:223], v[64:79]\n"
"ds_read_b64_tr_b16 v[60:61], v7 offset:30720\n"
"ds_read_b64_tr_b16 v[62:63], v7 offset:31488\n"
"ds_read_b128 v[224:227], v10 offset:0\n"
"s_waitcnt lgkmcnt(5)\n"
"v_mfma_f32_32x32x16_bf16 v[64:79], v[52:55], v[216:219], v[64:79]\n"
"ds_read_b64_tr_b16 v[48:49], v6 offset:36864\n"
"ds_read_b64_tr_b16 v[50:51], v6 offset:37632\n"
"ds_read_b128 v[228:231], v10 offset:1024\n"
"s_waitcnt lgkmcnt(6)\n"
"v_mfma_f32_32x32x16_bf16 v[64:79], v[56:59], v[212:215], v[64:79]\n"
"ds_read_b64_tr_b16 v[52:53], v7 offset:36864\n"
"ds_read_b64_tr_b16 v[54:55], v7 offset:37632\n"
"ds_read_b128 v[232:235], v10 offset:2048\n"
"s_waitcnt lgkmcnt(7)\n"
"v_mfma_f32_32x32x16_bf16 v[64:79], v[60:63], v[208:211], v[64:79]\n"
"ds_read_b64_tr_b16 v[56:57], v6 offset:43008\n"
"ds_read_b64_tr_b16 v[58:59], v6 offset:43776\n"
"ds_read_b128 v[236:239], v10 offset:3072\n"
"s_waitcnt lgkmcnt(7)\n"
"v_mfma_f32_32x32x16_bf16 v[64:79], v[48:51], v[204:207], v[64:79]\n"
"ds_read_b64_tr_b16 v[60:61], v7 offset:43008\n"
"ds_read_b64_tr_b16 v[62:63], v7 offset:43776\n"
"ds_read_b128 v[248:251], v10 offset:4096\n"
"s_waitcnt lgkmcnt(7)\n"
"v_mfma_f32_32x32x16_bf16 v[64:79], v[52:55], v[200:203], v[64:79]\n"
"v_subrev_u32 v6, 81920, v6\n"
"v_subrev_u32 v7, 81920, v7\n"
"ds_read_b64_tr_b16 v[48:49], v6 offset:0\n"
"ds_read_b64_tr_b16 v[50:51], v6 offset:768\n"
"ds_read_b128 v[252:255], v10 offset:5120\n"
"s_waitcnt lgkmcnt(7)\n"
"v_mfma_f32_32x32x16_bf16 v[64:79], v[56:59], v[196:199], v[64:79]\n"
"ds_read_b64_tr_b16 v[52:53], v7 offset:0\n"
"ds_read_b64_tr_b16 v[54:55], v7 offset:768\n"
"s_waitcnt lgkmcnt(6)\n"
"v_mfma_f32_32x32x16_bf16 v[64:79], v[60:63], v[192:195], v[64:79]\n"
"ds_read_b64_tr_b16 v[56:57], v6 offset:6144\n"
"ds_read_b64_tr_b16 v[58:59], v6 offset:6912\n"
"s_waitcnt lgkmcnt(5)\n"
"v_mfma_f32_32x32x16_bf16 v[64:79], v[48:51], v[188:191], v[64:79]\n"
"ds_read_b64_tr_b16 v[60:61], v7 offset:6144\n"
"ds_read_b64_tr_b16 v[62:63], v7 offset:6912\n"
"s_waitcnt lgkmcnt(4)\n"
"v_mfma_f32_32x32x16_bf16 v[64:79], v[52:55], v[184:187], v[64:79]\n"
"ds_read_b64_tr_b16 v[48:49], v6 offset:12288\n"
"ds_read_b64_tr_b16 v[50:51], v6 offset:13056\n"
"s_waitcnt lgkmcnt(4)\n"
"v_mfma_f32_32x32x16_bf16 v[64:79], v[56:59], v[180:183], v[64:79]\n"
"ds_read_b64_tr_b16 v[52:53], v7 offset:12288\n"
"ds_read_b64_tr_b16 v[54:55], v7 offset:13056\n"
"s_waitcnt lgkmcnt(4)\n"
"v_mfma_f32_32x32x16_bf16 v[64:79], v[60:63], v[176:179], v[64:79]\n"
"ds_read_b64_tr_b16 v[56:57], v6 offset:18432\n"
"ds_read_b64_tr_b16 v[58:59], v6 offset:19200\n"
"s_waitcnt lgkmcnt(4)\n"
"v_mfma_f32_32x32x16_bf16 v[64:79], v[48:51], v[172:175], v[64:79]\n"
"ds_read_b64_tr_b16 v[60:61], v7 offset:18432\n"
"ds_read_b64_tr_b16 v[62:63], v7 offset:19200\n"
"s_waitcnt lgkmcnt(4)\n"
"v_mfma_f32_32x32x16_bf16 v[64:79], v[52:55], v[168:171], v[64:79]\n"
"s_waitcnt lgkmcnt(2)\n"
"v_mfma_f32_32x32x16_bf16 v[64:79], v[56:59], v[164:167], v[64:79]\n"
"s_waitcnt lgkmcnt(0)\n"
"v_mfma_f32_32x32x16_bf16 v[64:79], v[60:63], v[160:163], v[64:79]\n"
"s_sub_u32 s40, s38, 196608\n"
"s_subb_u32 s41, s39, 0\n"
"s_add_u32 m0, s33, 53248\n"
"s_nop 0\n"
"global_load_lds_dwordx4 v25, s[40:41] offset:0\n"
"global_load_lds_dwordx4 v26, s[40:41] offset:1024\n"
"global_load_lds_dwordx4 v27, s[40:41] offset:2048\n"
"s_add_u32 m0, s33, 65536\n"
"s_nop 0\n"
"global_load_lds_dwordx4 v25, s[38:39] offset:0\n"
"global_load_lds_dwordx4 v26, s[38:39] offset:1024\n"
"global_load_lds_dwordx4 v27, s[38:39] offset:2048\n"
"s_cmp_eq_u32 s28, 1\n"
"s_cselect_b32 s45, 1966080, 0\n"
"s_add_u32 s38, s38, 393216\n"
"s_addc_u32 s39, s39, 0\n"
"s_sub_u32 s38, s38, s45\n"
"s_subb_u32 s39, s39, 0\n"
"s_waitcnt vmcnt(6)\n"
"v_mov_b32 v4, 0\n"
"v_mov_b32 v5, 1\n"
"s_mov_b64 exec, 1\n"
"ds_add_u32 v4, v5 offset:61440\n"
"s_mov_b64 exec, -1\n"
"s_cmp_lg_u32 s23, 4\n"
"s_cbranch_scc1 onew_52_%=\n"
"cnt_53_%=:\n"
"ds_read_b32 v5, v4 offset:61440\n"
"s_waitcnt lgkmcnt(0)\n"
"v_cmp_ne_u32 vcc, 8, v5\n"
"s_nop 0\n"
"s_cmp_eq_u64 vcc, 0\n"
"s_cbranch_scc1 ok_54_%=\n"
"s_sleep 1\n"
"s_sub_u32 s31, s31, 1\n"
"s_cbranch_scc0 cnt_53_%=\n"
"s_mov_b32 s31, 0\n"
"ok_54_%=:\n"
"v_lshl_add_u32 v3, v0, 3, s66\n"
"v_mov_b32 v4, s24\n"
"v_mov_b32 v5, s25\n"
"s_mov_b64 exec, 0x1\n"
"global_store_dwordx2 v3, v[4:5], s[18:19] sc1\n"
"s_mov_b64 exec, -1\n"
"onew_52_%=:\n"
"s_sub_u32 s40, s38, 196608\n"
"s_subb_u32 s41, s39, 0\n"
"s_sub_u32 m0, s33, 12288\n"
"s_nop 0\n"
"global_load_lds_dwordx4 v25, s[40:41] offset:0\n"
"global_load_lds_dwordx4 v26, s[40:41] offset:1024\n"
"global_load_lds_dwordx4 v27, s[40:41] offset:2048\n"
"s_add_u32 m0, s33, 0\n"
"s_nop 0\n"
"global_load_lds_dwordx4 v25, s[38:39] offset:0\n"
"global_load_lds_dwordx4 v26, s[38:39] offset:1024\n"
"global_load_lds_dwordx4 v27, s[38:39] offset:2048\n"
"global_load_dwordx4 v[44:47], v12, s[18:19] sc1\n"
"s_add_u32 s34, s18, s59\n"
"s_addc_u32 s35, s19, 0\n"
"s_add_u32 s38, s18, s60\n"
"s_addc_u32 s39, s19, 0\n"
"s_add_u32 s36, s18, s61\n"
"s_addc_u32 s37, s19, 0\n"
"v_and_b32 v4, 1, v0\n"
"v_cmp_eq_u32 vcc, 0, v4\n"
"v_mov_b32 v4, 0x3f803f80\n"
"s_nop 0\n"
"v_cndmask_b32 v32, 0, v4, vcc\n"
"v_cndmask_b32 v33, 0, v4, vcc\n"
"v_cndmask_b32 v34, 0, v4, vcc\n"
"v_cndmask_b32 v35, 0, v4, vcc\n"
"v_mov_b32 v192, 0\n"
"v_mov_b32 v193, 0\n"
"v_mov_b32 v194, 0\n"
"v_mov_b32 v195, 0\n"
"v_mov_b32 v196, 0\n"
"v_mov_b32 v197, 0\n"
"v_mov_b32 v198, 0\n"
"v_mov_b32 v199, 0\n"
"v_mov_b32 v200, 0\n"
"v_mov_b32 v201, 0\n"
"v_mov_b32 v202, 0\n"
"v_mov_b32 v203, 0\n"
"v_mov_b32 v204, 0\n"
"v_mov_b32 v205, 0\n"
"v_mov_b32 v206, 0\n"
"v_mov_b32 v207, 0\n"
"v_mov_b32 v208, 0\n"
"v_mov_b32 v209, 0\n"
"v_mov_b32 v210, 0\n"
"v_mov_b32 v211, 0\n"
"v_mov_b32 v212, 0\n"
"v_mov_b32 v213, 0\n"
"v_mov_b32 v214, 0\n"
"v_mov_b32 v215, 0\n"
"v_mov_b32 v216, 0\n"
"v_mov_b32 v217, 0\n"
"v_mov_b32 v218, 0\n"
"v_mov_b32 v219, 0\n"
"v_mov_b32 v220, 0\n"
"v_mov_b32 v221, 0\n"
"v_mov_b32 v222, 0\n"
"v_mov_b32 v223, 0\n"
"v_mov_b32 v242, s24\n"
"v_mov_b32 v243, s25\n"
"v_mov_b32 v240, 0xdddddddd\n"
"s_add_u32 s41, s33, 114688\n"
"v_add_u32 v244, s41, v24\n"
"v_add_u32 v245, -12288, v244\n"
"s_sub_u32 s41, s33, 12288\n"
"v_add_u32 v188, s41, v253\n"
"v_xor_b32 v189, 512, v188\n"
"s_sub_u32 s41, s33, 12288\n"
"v_add_u32 v190, s41, v254\n"
"v_xor_b32 v191, 512, v190\n"
"v_mov_b32 v184, v17\n"
"s_waitcnt vmcnt(7)\n"
"s_barrier\n"
"ds_read_b64_tr_b16 v[48:49], v6 offset:32768\n"
"ds_read_b64_tr_b16 v[50:51], v6 offset:33536\n"
"ds_read_b64_tr_b16 v[52:53], v7 offset:32768\n"
"ds_read_b64_tr_b16 v[54:55], v7 offset:33536\n"
"ds_read_b64_tr_b16 v[56:57], v6 offset:38912\n"
"ds_read_b64_tr_b16 v[58:59], v6 offset:39680\n"
"ds_write_b128 v255, v[32:35] offset:0\n"
"ds_write_b128 v255, v[32:35] offset:24576\n"
"s_waitcnt lgkmcnt(6)\n"
"v_mfma_f32_32x32x16_bf16 v[64:79], v[48:51], v[156:159], v[64:79]\n"
"ds_read_b64_tr_b16 v[60:61], v7 offset:38912\n"
"ds_read_b64_tr_b16 v[62:63], v7 offset:39680\n"
"s_waitcnt lgkmcnt(6)\n"
"v_mfma_f32_32x32x16_bf16 v[64:79], v[52:55], v[152:155], v[64:79]\n"
"ds_read_b64_tr_b16 v[48:49], v6 offset:45056\n"
"ds_read_b64_tr_b16 v[50:51], v6 offset:45824\n"
"s_waitcnt lgkmcnt(6)\n"
"v_mfma_f32_32x32x16_bf16 v[64:79], v[56:59], v[148:151], v[64:79]\n"
"ds_read_b64_tr_b16 v[52:53], v7 offset:45056\n"
"ds_read_b64_tr_b16 v[54:55], v7 offset:45824\n"
"s_waitcnt lgkmcnt(4)\n"
"v_mfma_f32_32x32x16_bf16 v[64:79], v[60:63], v[144:147], v[64:79]\n"
"ds_read_b64_tr_b16 v[56:57], v6 offset:51200\n"
"ds_read_b64_tr_b16 v[58:59], v6 offset:51968\n"
"s_waitcnt lgkmcnt(4)\n"
"v_mfma_f32_32x32x16_bf16 v[64:79], v[48:51], v[140:143], v[64:79]\n"
"ds_read_b64_tr_b16 v[60:61], v7 offset:51200\n"
"ds_read_b64_tr_b16 v[62:63], v7 offset:51968\n"
"s_waitcnt vmcnt(1)\n"
"s_barrier\n"
"s_waitcnt lgkmcnt(4)\n"
"v_mfma_f32_32x32x16_bf16 v[64:79], v[52:55], v[136:139], v[64:79]\n"
"v_subrev_u32 v6, 32768, v6\n"
"v_subrev_u32 v7, 32768, v7\n"
"ds_read_b64_tr_b16 v[48:49], v6 offset:0\n"
"ds_read_b64_tr_b16 v[50:51], v6 offset:768\n"
"s_waitcnt lgkmcnt(4)\n"
"v_mfma_f32_32x32x16_bf16 v[64:79], v[56:59], v[132:135], v[64:79]\n"
"ds_read_b64_tr_b16 v[52:53], v7 offset:0\n"
"ds_read_b64_tr_b16 v[54:55], v7 offset:768\n"
"s_waitcnt lgkmcnt(4)\n"
"v_mfma_f32_32x32x16_bf16 v[64:79], v[60:63], v[128:131], v[64:79]\n"
"ds_read_b64_tr_b16 v[56:57], v6 offset:6144\n"
"ds_read_b64_tr_b16 v[58:59], v6 offset:6912\n"
"s_waitcnt lgkmcnt(4)\n"
"v_mfma_f32_32x32x16_bf16 v[64:79], v[48:51], v[124:127], v[64:79]\n"
"ds_read_b64_tr_b16 v[60:61], v7 offset:6144\n"
"ds_read_b64_tr_b16 v[62:63], v7 offset:6912\n"
"s_waitcnt lgkmcnt(4)\n"
"v_mfma_f32_32x32x16_bf16 v[64:79], v[52:55], v[120:123], v[64:79]\n"
"ds_read_b64_tr_b16 v[48:49], v6 offset:12288\n"
"ds_read_b64_tr_b16 v[50:51], v6 offset:13056\n"
"s_waitcnt lgkmcnt(4)\n"
"v_mfma_f32_32x32x16_bf16 v[64:79], v[56:59], v[116:119], v[64:79]\n"
"ds_read_b64_tr_b16 v[52:53], v7 offset:12288\n"
"ds_read_b64_tr_b16 v[54:55], v7 offset:13056\n"
"s_waitcnt vmcnt(0)\n"
"v_cmp_ne_u32 vcc, s24, v45\n"
"v_cmp_ne_u32 s[42:43], s25, v47\n"
"s_nop 0\n"
"s_or_b64 vcc, vcc, s[42:43]\n"
"s_cmp_eq_u64 vcc, 0\n"
"s_cbranch_scc0 retry_56_%=\n"
"ok_57_%=:\n"
"v_mov_b32 v45, v46\n"
"s_waitcnt lgkmcnt(4)\n"
"v_mfma_f32_32x32x16_bf16 v[64:79], v[60:63], v[112:115], v[64:79]\n"
"ds_read_b64_tr_b16 v[56:57], v6 offset:18432\n"
"ds_read_b64_tr_b16 v[58:59], v6 offset:19200\n"
"v_mfma_f32_32x32x8_bf16 v[64:79], v[44:45], v[18:19], v[64:79]\n"
"s_waitcnt lgkmcnt(4)\n"
"v_mfma_f32_32x32x16_bf16 v[64:79], v[48:51], v[108:111], v[64:79]\n"
"ds_read_b64_tr_b16 v[60:61], v7 offset:18432\n"
"ds_read_b64_tr_b16 v[62:63], v7 offset:19200\n"
"s_waitcnt lgkmcnt(4)\n"
"v_mfma_f32_32x32x16_bf16 v[64:79], v[52:55], v[104:107], v[64:79]\n"
"s_waitcnt lgkmcnt(2)\n"
"v_mfma_f32_32x32x16_bf16 v[64:79], v[56:59], v[100:103], v[64:79]\n"
"s_cmp_lg_u32 s23, 4\n"
"s_cbranch_scc1 nopl_58_%=\n"
"global_load_dwordx2 v[28:29], v252, s[18:19] sc1\n"
"global_load_dwordx2 v[30:31], v251, s[18:19] sc1\n"
"nopl_58_%=:\n"
"s_waitcnt lgkmcnt(0)\n"
"v_mfma_f32_32x32x16_bf16 v[64:79], v[60:63], v[96:99], v[64:79]\n"
"s_setprio 0\n"
"s_cmp_lg_u32 s23, 4\n"
"s_cbranch_scc1 nopoll_59_%=\n"
"s_waitcnt vmcnt(0)\n"
"v_cmp_ne_u64 vcc, s[24:25], v[28:29]\n"
"v_cmp_ne_u64 s[42:43], s[24:25], v[30:31]\n"
"s_nop 0\n"
"s_or_b64 vcc, vcc, s[42:43]\n"
"s_nop 0\n"
"s_cmp_eq_u64 vcc, 0\n"
"s_cbranch_scc0 retry_60_%=\n"
"ok_61_%=:\n"
"s_mov_b32 m0, 114688\n"
"s_nop 0\n"
"global_load_lds_dwordx4 v248, s[34:35] offset:0 sc1\n"
"global_load_lds_dwordx4 v249, s[34:35] offset:1024 sc1\n"
"global_load_lds_dwordx4 v250, s[34:35] offset:2048 sc1\n"
"s_mov_b32 m0, 98304\n"
"s_nop 0\n"
"global_load_lds_dwordx4 v248, s[38:39] offset:0 sc1\n"
"global_load_lds_dwordx4 v249, s[38:39] offset:1024 sc1\n"
"global_load_lds_dwordx4 v250, s[38:39] offset:2048 sc1\n"
"s_mov_b32 m0, 117760\n"
"s_add_u32 s34, s34, 1024\n"
"s_addc_u32 s35, s35, 0\n"
"global_load_lds_dwordx4 v248, s[34:35] offset:0 sc1\n"
"global_load_lds_dwordx4 v249, s[34:35] offset:1024 sc1\n"
"global_load_lds_dwordx4 v250, s[34:35] offset:2048 sc1\n"
"s_mov_b32 m0, 101376\n"
"s_add_u32 s38, s38, 1024\n"
"s_addc_u32 s39, s39, 0\n"
"global_load_lds_dwordx4 v248, s[38:39] offset:0 sc1\n"
"global_load_lds_dwordx4 v249, s[38:39] offset:1024 sc1\n"
"global_load_lds_dwordx4 v250, s[38:39] offset:2048 sc1\n"
"s_mov_b32 m0, 120832\n"
"s_add_u32 s34, s34, 1024\n"
"s_addc_u32 s35, s35, 0\n"
"global_load_lds_dwordx4 v248, s[34:35] offset:0 sc1\n"
"global_load_lds_dwordx4 v249, s[34:35] offset:1024 sc1\n"
"global_load_lds_dwordx4 v250, s[34:35] offset:2048 sc1\n"
"s_mov_b32 m0, 104448\n"
"s_add_u32 s38, s38, 1024\n"
"s_addc_u32 s39, s39, 0\n"
"global_load_lds_dwordx4 v248, s[38:39] offset:0 sc1\n"
"global_load_lds_dwordx4 v249, s[38:39] offset:1024 sc1\n"
"global_load_lds_dwordx4 v250, s[38:39] offset:2048 sc1\n"
"s_mov_b32 m0, 123904\n"
"s_add_u32 s34, s34, 1024\n"
"s_addc_u32 s35, s35, 0\n"
"global_load_lds_dwordx4 v248, s[34:35] offset:0 sc1\n"
"global_load_lds_dwordx4 v249, s[34:35] offset:1024 sc1\n"
"global_load_lds_dwordx4 v250, s[34:35] offset:2048 sc1\n"
"s_mov_b32 m0, 107520\n"
"s_add_u32 s38, s38, 1024\n"
"s_addc_u32 s39, s39, 0\n"
"global_load_lds_dwordx4 v248, s[38:39] offset:0 sc1\n"
"global_load_lds_dwordx4 v249, s[38:39] offset:1024 sc1\n"
"global_load_lds_dwordx4 v250, s[38:39] offset:2048 sc1\n"
"s_mov_b32 m0, 126976\n"
"s_mov_b64 s[40:41], s[36:37]\n"
"s_mov_b64 exec, s[48:49]\n"
"global_load_lds_dwordx4 v229, s[40:41] offset:0 sc1\n"
"s_mov_b64 exec, s[50:51]\n"
"global_load_lds_dwordx4 v230, s[40:41] offset:1024 sc1\n"
"s_mov_b64 exec, s[52:53]\n"
"global_load_lds_dwordx4 v231, s[40:41] offset:2048 sc1\n"
"s_mov_b32 m0, 133120\n"
"s_add_u32 s40, s36, 163840\n"
"s_addc_u32 s41, s37, 0\n"
"s_mov_b64 exec, s[48:49]\n"
"global_load_lds_dwordx4 v229, s[40:41] offset:0 sc1\n"
"s_mov_b64 exec, s[50:51]\n"
"global_load_lds_dwordx4 v230, s[40:41] offset:1024 sc1\n"
"s_mov_b64 exec, s[52:53]\n"
"global_load_lds_dwordx4 v231, s[40:41] offset:2048 sc1\n"
"s_mov_b32 m0, 130048\n"
"s_add_u32 s40, s36, 81920\n"
"s_addc_u32 s41, s37, 0\n"
"s_mov_b64 exec, s[48:49]\n"
"global_load_lds_dwordx4 v229, s[40:41] offset:0 sc1\n"
"s_mov_b64 exec, s[50:51]\n"
"global_load_lds_dwordx4 v230, s[40:41] offset:1024 sc1\n"
"s_mov_b64 exec, s[52:53]\n"
"global_load_lds_dwordx4 v231, s[40:41] offset:2048 sc1\n"
"s_mov_b32 m0, 136192\n"
"s_add_u32 s40, s36, 245760\n"
"s_addc_u32 s41, s37, 0\n"
"s_mov_b64 exec, s[48:49]\n"
"global_load_lds_dwordx4 v229, s[40:41] offset:0 sc1\n"
"s_mov_b64 exec, s[50:51]\n"
"global_load_lds_dwordx4 v230, s[40:41] offset:1024 sc1\n"
"s_mov_b64 exec, s[52:53]\n"
"global_load_lds_dwordx4 v231, s[40:41] offset:2048 sc1\n"
"s_mov_b64 exec, -1\n"
"nopoll_59_%=:\n"
"s_nop 7\n"
"s_add_u32 s34, s36, 327680\n"
"s_addc_u32 s35, s37, 0\n"
"v_cvt_pk_bf16_f32 v168, v64, v65\n"
"v_cvt_pk_bf16_f32 v169, v66, v67\n"
"v_cvt_pk_bf16_f32 v170, v68, v69\n"
"v_cvt_pk_bf16_f32 v171, v70, v71\n"
"v_cvt_pk_bf16_f32 v172, v72, v73\n"
"v_cvt_pk_bf16_f32 v173, v74, v75\n"
"v_cvt_pk_bf16_f32 v174, v76, v77\n"
"v_cvt_pk_bf16_f32 v175, v78, v79\n"
"v_pk_max_i16 v168, v168, 0\n"
"v_pk_max_i16 v169, v169, 0\n"
"v_pk_max_i16 v170, v170, 0\n"
"v_pk_max_i16 v171, v171, 0\n"
"v_pk_max_i16 v172, v172, 0\n"
"v_pk_max_i16 v173, v173, 0\n"
"v_pk_max_i16 v174, v174, 0\n"
"v_pk_max_i16 v175, v175, 0\n"
"v_permlane32_swap_b32 v168, v170\n"
"v_permlane32_swap_b32 v169, v171\n"
"v_permlane32_swap_b32 v172, v174\n"
"v_permlane32_swap_b32 v173, v175\n"
"s_setprio 0\n"
"s_waitcnt vmcnt(0) lgkmcnt(0)\n"
"att_7_%=:\n"
"v_cmp_eq_u32 vcc, 0, v2\n"
"s_barrier\n"
"ds_read_b128 v[48:51], v189\n"
"ds_read_b64 v[56:57], v191 offset:2048\n"
"ds_read_b128 v[64:67], v232 offset:0\n"
"ds_read2st64_b64 v[68:71], v234 offset0:0 offset1:6\n"
"ds_read_b128 v[52:55], v188\n"
"ds_read_b64 v[60:61], v190 offset:2048\n"
"ds_read_b128 v[72:75], v233 offset:0\n"
"ds_read_b128 v[80:83], v233 offset:768\n"
"s_add_u32 m0, s33, 139264\n"
"s_mov_b64 exec, s[48:49]\n"
"global_load_lds_dwordx4 v229, s[34:35] offset:0 sc1\n"
"s_mov_b64 exec, -1\n"
"s_add_u32 s38, s18, s62\n"
"s_addc_u32 s39, s19, 0\n"
"s_waitcnt lgkmcnt(5)\n"
"v_mfma_f32_32x32x16_bf16 v[16:31], v[64:67], v[48:51], 0\n"
"v_cndmask_b32 v58, v56, 0, vcc\n"
"v_cndmask_b32 v56, 0, v56, vcc\n"
"v_cndmask_b32 v59, v57, 0, vcc\n"
"v_cndmask_b32 v57, 0, v57, vcc\n"
"s_waitcnt lgkmcnt(2)\n"
"s_nop 0\n"
"v_mfma_f32_32x32x8_bf16 v[16:31], v[68:69], v[56:57], v[16:31]\n"
"v_cndmask_b32 v62, v60, 0, vcc\n"
"v_cndmask_b32 v60, 0, v60, vcc\n"
"v_cndmask_b32 v63, v61, 0, vcc\n"
"v_cndmask_b32 v61, 0, v61, vcc\n"
"s_add_u32 m0, s33, 139264\n"
"s_mov_b64 exec, s[50:51]\n"
"global_load_lds_dwordx4 v230, s[34:35] offset:1024 sc1\n"
"s_mov_b64 exec, -1\n"
"s_waitcnt lgkmcnt(0)\n"
"s_setprio 3\n"
"s_nop 1\n"
"v_exp_f32 v16, v16\n"
"v_exp_f32 v17, v17\n"
"v_exp_f32 v18, v18\n"
"ds_read_b128 v[76:79], v233 offset:1536\n"
"ds_read_b128 v[84:87], v233 offset:2304\n"
"s_add_u32 m0, s33, 139264\n"
"s_mov_b64 exec, s[52:53]\n"
"global_load_lds_dwordx4 v231, s[34:35] offset:2048 sc1\n"
"s_mov_b64 exec, -1\n"
"s_add_u32 s34, s34, s54\n"
"s_addc_u32 s35, s35, 0\n"
"v_exp_f32 v19, v19\n"
"v_exp_f32 v20, v20\n"
"v_exp_f32 v21, v21\n"
"v_mfma_f32_32x32x16_bf16 v[32:47], v[64:67], v[52:55], 0\n"
"ds_read_b128 v[64:67], v232 offset:512\n"
"v_exp_f32 v22, v22\n"
"v_exp_f32 v23, v23\n"
"v_exp_f32 v24, v24\n"
"v_mfma_f32_32x32x8_bf16 v[32:47], v[68:69], v[60:61], v[32:47]\n"
"v_exp_f32 v25, v25\n"
"v_exp_f32 v26, v26\n"
"v_exp_f32 v27, v27\n"
"v_exp_f32 v28, v28\n"
"v_smfmac_f32_32x32x32_bf16 v[192:207], v[72:75], v[16:23], v240\n"
"v_exp_f32 v29, v29\n"
"v_exp_f32 v30, v30\n"
"v_exp_f32 v31, v31\n"
"v_exp_f32 v32, v32\n"
"v_exp_f32 v33, v33\n"
"v_exp_f32 v34, v34\n"
"v_smfmac_f32_32x32x32_bf16 v[192:207], v[80:83], v[24:31], v240\n"
"s_waitcnt lgkmcnt(0)\n"
"s_lshr_b32 s30, s30, 2\n"
"v_exp_f32 v35, v35\n"
"v_exp_f32 v36, v36\n"
"v_exp_f32 v37, v37\n"
"v_mfma_f32_32x32x16_bf16 v[16:31], v[64:67], v[48:51], 0\n"
"v_exp_f32 v38, v38\n"
"v_exp_f32 v39, v39\n"
"v_exp_f32 v40, v40\n"
"v_mfma_f32_32x32x8_bf16 v[16:31], v[68:69], v[58:59], v[16:31]\n"
"v_exp_f32 v41, v41\n"
"v_exp_f32 v42, v42\n"
"v_exp_f32 v43, v43\n"
"v_exp_f32 v44, v44\n"
"v_smfmac_f32_32x32x32_bf16 v[208:223], v[72:75], v[32:39], v240\n"
"s_add_u32 s36, s18, s57\n"
"s_addc_u32 s37, s19, 0\n"
"global_store_dwordx4 v184, v[168:171], s[36:37] sc1\n"
"noh_62_%=:\n"
"v_exp_f32 v45, v45\n"
"v_exp_f32 v46, v46\n"
"v_exp_f32 v47, v47\n"
"v_exp_f32 v16, v16\n"
"v_exp_f32 v17, v17\n"
"v_exp_f32 v18, v18\n"
"v_smfmac_f32_32x32x32_bf16 v[208:223], v[80:83], v[40:47], v240\n"
"ds_read_b128 v[72:75], v233 offset:3072\n"
"ds_read_b128 v[80:83], v233 offset:3840\n"
"v_exp_f32 v19, v19\n"
"v_exp_f32 v20, v20\n"
"v_exp_f32 v21, v21\n"
"v_mfma_f32_32x32x16_bf16 v[32:47], v[64:67], v[52:55], 0\n"
"ds_read_b128 v[64:67], v232 offset:3072\n"
"v_exp_f32 v22, v22\n"
"v_exp_f32 v23, v23\n"
"v_exp_f32 v24, v24\n"
"v_mfma_f32_32x32x8_bf16 v[32:47], v[68:69], v[62:63], v[32:47]\n"
"v_exp_f32 v25, v25\n"
"v_exp_f32 v26, v26\n"
"v_exp_f32 v27, v27\n"
"v_exp_f32 v28, v28\n"
"v_smfmac_f32_32x32x32_bf16 v[192:207], v[76:79], v[16:23], v240\n"
"s_add_u32 s36, s18, s57\n"
"s_addc_u32 s37, s19, 0\n"
"s_add_u32 s36, s36, 65536\n"
"s_addc_u32 s37, s37, 0\n"
"global_store_dwordx4 v184, v[172:175], s[36:37] sc1\n"
"noh_63_%=:\n"
"v_exp_f32 v29, v29\n"
"v_exp_f32 v30, v30\n"
"v_exp_f32 v31, v31\n"
"v_exp_f32 v32, v32\n"
"v_exp_f32 v33, v33\n"
"v_exp_f32 v34, v34\n"
"v_smfmac_f32_32x32x32_bf16 v[192:207], v[84:87], v[24:31], v240\n"
"s_waitcnt lgkmcnt(0)\n"
"v_exp_f32 v35, v35\n"
"v_exp_f32 v36, v36\n"
"v_exp_f32 v37, v37\n"
"v_mfma_f32_32x32x16_bf16 v[16:31], v[64:67], v[48:51], 0\n"
"s_setprio 2\n"
"v_exp_f32 v38, v38\n"
"v_exp_f32 v39, v39\n"
"v_exp_f32 v40, v40\n"
"v_mfma_f32_32x32x8_bf16 v[16:31], v[70:71], v[56:57], v[16:31]\n"
"v_exp_f32 v41, v41\n"
"v_exp_f32 v42, v42\n"
"v_exp_f32 v43, v43\n"
"v_exp_f32 v44, v44\n"
"v_smfmac_f32_32x32x32_bf16 v[208:223], v[76:79], v[32:39], v240\n"
"s_cmp_eq_u32 s47, 1\n"
"s_cbranch_scc1 noh_64_%=\n"
"s_add_u32 s36, s18, s58\n"
"s_addc_u32 s37, s19, 0\n"
"global_store_dwordx4 v184, v[176:179], s[36:37] sc1\n"
"noh_64_%=:\n"
"v_exp_f32 v45, v45\n"
"v_exp_f32 v46, v46\n"
"v_exp_f32 v47, v47\n"
"v_exp_f32 v16, v16\n"
"v_exp_f32 v17, v17\n"
"v_exp_f32 v18, v18\n"
"v_smfmac_f32_32x32x32_bf16 v[208:223], v[84:87], v[40:47], v240\n"
"ds_read_b128 v[76:79], v233 offset:4608\n"
"ds_read_b128 v[84:87], v233 offset:5376\n"
"v_exp_f32 v19, v19\n"
"v_exp_f32 v20, v20\n"
"v_exp_f32 v21, v21\n"
"v_mfma_f32_32x32x16_bf16 v[32:47], v[64:67], v[52:55], 0\n"
"ds_read_b128 v[64:67], v232 offset:3584\n"
"s_setprio 1\n"
"v_exp_f32 v22, v22\n"
"v_exp_f32 v23, v23\n"
"v_exp_f32 v24, v24\n"
"v_mfma_f32_32x32x8_bf16 v[32:47], v[70:71], v[60:61], v[32:47]\n"
"v_exp_f32 v25, v25\n"
"v_exp_f32 v26, v26\n"
"v_exp_f32 v27, v27\n"
"v_exp_f32 v28, v28\n"
"v_smfmac_f32_32x32x32_bf16 v[192:207], v[72:75], v[16:23], v240\n"
"s_cmp_eq_u32 s47, 1\n"
"s_cbranch_scc1 noh_65_%=\n"
"s_add_u32 s36, s18, s58\n"
"s_addc_u32 s37, s19, 0\n"
"s_add_u32 s36, s36, 65536\n"
"s_addc_u32 s37, s37, 0\n"
"global_store_dwordx4 v184, v[180:183], s[36:37] sc1\n"
"noh_65_%=:\n"
"v_exp_f32 v29, v29\n"
"v_exp_f32 v30, v30\n"
"v_exp_f32 v31, v31\n"
"v_exp_f32 v32, v32\n"
"v_exp_f32 v33, v33\n"
"v_exp_f32 v34, v34\n"
"v_smfmac_f32_32x32x32_bf16 v[192:207], v[80:83], v[24:31], v240\n"
"s_waitcnt lgkmcnt(0)\n"
"v_exp_f32 v35, v35\n"
"v_exp_f32 v36, v36\n"
"v_exp_f32 v37, v37\n"
"v_mfma_f32_32x32x16_bf16 v[16:31], v[64:67], v[48:51], 0\n"
"s_setprio 0\n"
"v_exp_f32 v38, v38\n"
"v_exp_f32 v39, v39\n"
"v_exp_f32 v40, v40\n"
"v_mfma_f32_32x32x8_bf16 v[16:31], v[70:71], v[58:59], v[16:31]\n"
"v_exp_f32 v41, v41\n"
"v_exp_f32 v42, v42\n"
"v_exp_f32 v43, v43\n"
"v_exp_f32 v44, v44\n"
"v_smfmac_f32_32x32x32_bf16 v[208:223], v[72:75], v[32:39], v240\n"
"v_exp_f32 v45, v45\n"
"v_exp_f32 v46, v46\n"
"v_exp_f32 v47, v47\n"
"s_waitcnt vmcnt(2)\n"
"s_barrier\n"
"s_setprio 3\n"
"v_exp_f32 v16, v16\n"
"v_exp_f32 v17, v17\n"
"v_exp_f32 v18, v18\n"
"v_smfmac_f32_32x32x32_bf16 v[208:223], v[80:83], v[40:47], v240\n"
"ds_read_b128 v[72:75], v233 offset:24576\n"
"ds_read_b128 v[80:83], v233 offset:25344\n"
"s_add_u32 m0, s33, 114688\n"
"s_mov_b64 exec, s[48:49]\n"
"global_load_lds_dwordx4 v229, s[34:35] offset:0 sc1\n"
"s_mov_b64 exec, -1\n"
"v_exp_f32 v19, v19\n"
"v_exp_f32 v20, v20\n"
"v_exp_f32 v21, v21\n"
"v_mfma_f32_32x32x16_bf16 v[32:47], v[64:67], v[52:55], 0\n"
"ds_read_b128 v[64:67], v232 offset:24576\n"
"v_exp_f32 v22, v22\n"
"v_exp_f32 v23, v23\n"
"v_exp_f32 v24, v24\n"
"v_mfma_f32_32x32x8_bf16 v[32:47], v[70:71], v[62:63], v[32:47]\n"
"v_exp_f32 v25, v25\n"
"v_exp_f32 v26, v26\n"
"v_exp_f32 v27, v27\n"
"v_exp_f32 v28, v28\n"
"v_smfmac_f32_32x32x32_bf16 v[192:207], v[76:79], v[16:23], v240\n"
"ds_read2st64_b64 v[68:71], v234 offset0:48 offset1:54\n"
"s_add_u32 m0, s33, 114688\n"
"s_mov_b64 exec, s[50:51]\n"
"global_load_lds_dwordx4 v230, s[34:35] offset:1024 sc1\n"
"s_mov_b64 exec, -1\n"
"v_exp_f32 v29, v29\n"
"v_exp_f32 v30, v30\n"
"v_exp_f32 v31, v31\n"
"v_exp_f32 v32, v32\n"
"v_exp_f32 v33, v33\n"
"v_exp_f32 v34, v34\n"
"v_smfmac_f32_32x32x32_bf16 v[192:207], v[84:87], v[24:31], v240\n"
"s_waitcnt lgkmcnt(0)\n"
"s_add_u32 m0, s33, 114688\n"
"s_mov_b64 exec, s[52:53]\n"
"global_load_lds_dwordx4 v231, s[34:35] offset:2048 sc1\n"
"s_mov_b64 exec, -1\n"
"s_add_u32 s34, s34, s54\n"
"s_addc_u32 s35, s35, 0\n"
"v_exp_f32 v35, v35\n"
"v_exp_f32 v36, v36\n"
"v_exp_f32 v37, v37\n"
"v_mfma_f32_32x32x16_bf16 v[16:31], v[64:67], v[48:51], 0\n"
"v_exp_f32 v38, v38\n"
"v_exp_f32 v39, v39\n"
"v_exp_f32 v40, v40\n"
"v_mfma_f32_32x32x8_bf16 v[16:31], v[68:69], v[56:57], v[16:31]\n"
"v_exp_f32 v41, v41\n"
"v_exp_f32 v42, v42\n"
"v_exp_f32 v43, v43\n"
"v_exp_f32 v44, v44\n"
"v_smfmac_f32_32x32x32_bf16 v[208:223], v[76:79], v[32:39], v240\n"
"v_exp_f32 v45, v45\n"
"v_exp_f32 v46, v46\n"
"v_exp_f32 v47, v47\n"
"v_exp_f32 v16, v16\n"
"v_exp_f32 v17, v17\n"
"v_exp_f32 v18, v18\n"
"v_smfmac_f32_32x32x32_bf16 v[208:223], v[84:87], v[40:47], v240\n"
"ds_read_b128 v[76:79], v233 offset:26112\n"
"ds_read_b128 v[84:87], v233 offset:26880\n"
"v_exp_f32 v19, v19\n"
"v_exp_f32 v20, v20\n"
"v_exp_f32 v21, v21\n"
"v_mfma_f32_32x32x16_bf16 v[32:47], v[64:67], v[52:55], 0\n"
"ds_read_b128 v[64:67], v232 offset:25088\n"
"v_exp_f32 v22, v22\n"
"v_exp_f32 v23, v23\n"
"v_exp_f32 v24, v24\n"
"v_mfma_f32_32x32x8_bf16 v[32:47], v[68:69], v[60:61], v[32:47]\n"
"v_exp_f32 v25, v25\n"
"v_exp_f32 v26, v26\n"
"v_exp_f32 v27, v27\n"
"v_exp_f32 v28, v28\n"
"v_smfmac_f32_32x32x32_bf16 v[192:207], v[72:75], v[16:23], v240\n"
"v_exp_f32 v29, v29\n"
"v_exp_f32 v30, v30\n"
"v_exp_f32 v31, v31\n"
"v_exp_f32 v32, v32\n"
"v_exp_f32 v33, v33\n"
"v_exp_f32 v34, v34\n"
"v_smfmac_f32_32x32x32_bf16 v[192:207], v[80:83], v[24:31], v240\n"
"s_waitcnt lgkmcnt(0)\n"
"global_load_dwordx4 v[250:253], v228, s[8:9]\n"
"v_exp_f32 v35, v35\n"
"v_exp_f32 v36, v36\n"
"v_exp_f32 v37, v37\n"
"v_mfma_f32_32x32x16_bf16 v[16:31], v[64:67], v[48:51], 0\n"
"v_exp_f32 v38, v38\n"
"v_exp_f32 v39, v39\n"
"v_exp_f32 v40, v40\n"
"v_mfma_f32_32x32x8_bf16 v[16:31], v[68:69], v[58:59], v[16:31]\n"
"v_exp_f32 v41, v41\n"
"v_exp_f32 v42, v42\n"
"v_exp_f32 v43, v43\n"
"v_exp_f32 v44, v44\n"
"v_smfmac_f32_32x32x32_bf16 v[208:223], v[72:75], v[32:39], v240\n"
"global_load_dwordx2 v[254:255], v238, s[8:9]\n"
"v_exp_f32 v45, v45\n"
"v_exp_f32 v46, v46\n"
"v_exp_f32 v47, v47\n"
"v_exp_f32 v16, v16\n"
"v_exp_f32 v17, v17\n"
"v_exp_f32 v18, v18\n"
"v_smfmac_f32_32x32x32_bf16 v[208:223], v[80:83], v[40:47], v240\n"
"ds_read_b128 v[72:75], v233 offset:27648\n"
"ds_read_b128 v[80:83], v233 offset:28416\n"
"v_exp_f32 v19, v19\n"
"v_exp_f32 v20, v20\n"
"v_exp_f32 v21, v21\n"
"v_mfma_f32_32x32x16_bf16 v[32:47], v[64:67], v[52:55], 0\n"
"ds_read_b128 v[64:67], v232 offset:27648\n"
"v_exp_f32 v22, v22\n"
"v_exp_f32 v23, v23\n"
"v_exp_f32 v24, v24\n"
"v_mfma_f32_32x32x8_bf16 v[32:47], v[68:69], v[62:63], v[32:47]\n"
"v_exp_f32 v25, v25\n"
"v_exp_f32 v26, v26\n"
"v_exp_f32 v27, v27\n"
"v_exp_f32 v28, v28\n"
"v_smfmac_f32_32x32x32_bf16 v[192:207], v[76:79], v[16:23], v240\n"
"v_exp_f32 v29, v29\n"
"v_exp_f32 v30, v30\n"
"v_exp_f32 v31, v31\n"
"v_exp_f32 v32, v32\n"
"v_exp_f32 v33, v33\n"
"v_exp_f32 v34, v34\n"
"v_smfmac_f32_32x32x32_bf16 v[192:207], v[84:87], v[24:31], v240\n"
"s_waitcnt lgkmcnt(0)\n"
"v_exp_f32 v35, v35\n"
"v_exp_f32 v36, v36\n"
"v_exp_f32 v37, v37\n"
"v_mfma_f32_32x32x16_bf16 v[16:31], v[64:67], v[48:51], 0\n"
"s_setprio 2\n"
"v_exp_f32 v38, v38\n"
"v_exp_f32 v39, v39\n"
"v_exp_f32 v40, v40\n"
"v_mfma_f32_32x32x8_bf16 v[16:31], v[70:71], v[56:57], v[16:31]\n"
"v_exp_f32 v41, v41\n"
"v_exp_f32 v42, v42\n"
"v_exp_f32 v43, v43\n"
"v_exp_f32 v44, v44\n"
"v_smfmac_f32_32x32x32_bf16 v[208:223], v[76:79], v[32:39], v240\n"
"v_exp_f32 v45, v45\n"
"v_exp_f32 v46, v46\n"
"v_exp_f32 v47, v47\n"
"v_exp_f32 v16, v16\n"
"v_exp_f32 v17, v17\n"
"v_exp_f32 v18, v18\n"
"v_smfmac_f32_32x32x32_bf16 v[208:223], v[84:87], v[40:47], v240\n"
"ds_read_b128 v[76:79], v233 offset:29184\n"
"ds_read_b128 v[84:87], v233 offset:29952\n"
"v_exp_f32 v19, v19\n"
"v_exp_f32 v20, v20\n"
"v_exp_f32 v21, v21\n"
"v_mfma_f32_32x32x16_bf16 v[32:47], v[64:67], v[52:55], 0\n"
"ds_read_b128 v[64:67], v232 offset:28160\n"
"s_setprio 1\n"
"v_exp_f32 v22, v22\n"
"v_exp_f32 v23, v23\n"
"v_exp_f32 v24, v24\n"
"v_mfma_f32_32x32x8_bf16 v[32:47], v[70:71], v[60:61], v[32:47]\n"
"v_exp_f32 v25, v25\n"
"v_exp_f32 v26, v26\n"
"v_exp_f32 v27, v27\n"
"v_exp_f32 v28, v28\n"
"v_smfmac_f32_32x32x32_bf16 v[192:207], v[72:75], v[16:23], v240\n"
"v_exp_f32 v29, v29\n"
"v_exp_f32 v30, v30\n"
"v_exp_f32 v31, v31\n"
"v_exp_f32 v32, v32\n"
"v_exp_f32 v33, v33\n"
"v_exp_f32 v34, v34\n"
"v_smfmac_f32_32x32x32_bf16 v[192:207], v[80:83], v[24:31], v240\n"
"s_waitcnt lgkmcnt(0)\n"
"v_exp_f32 v35, v35\n"
"v_exp_f32 v36, v36\n"
"v_exp_f32 v37, v37\n"
"v_mfma_f32_32x32x16_bf16 v[16:31], v[64:67], v[48:51], 0\n"
"s_setprio 0\n"
"v_exp_f32 v38, v38\n"
"v_exp_f32 v39, v39\n"
"v_exp_f32 v40, v40\n"
"v_mfma_f32_32x32x8_bf16 v[16:31], v[70:71], v[58:59], v[16:31]\n"
"v_exp_f32 v41, v41\n"
"v_exp_f32 v42, v42\n"
"v_exp_f32 v43, v43\n"
"v_exp_f32 v44, v44\n"
"v_smfmac_f32_32x32x32_bf16 v[208:223], v[72:75], v[32:39], v240\n"
"v_exp_f32 v45, v45\n"
"v_exp_f32 v46, v46\n"
"v_exp_f32 v47, v47\n"
"s_waitcnt vmcnt(2)\n"
"s_barrier\n"
"s_setprio 3\n"
"v_exp_f32 v16, v16\n"
"v_exp_f32 v17, v17\n"
"v_exp_f32 v18, v18\n"
"v_smfmac_f32_32x32x32_bf16 v[208:223], v[80:83], v[40:47], v240\n"
"ds_read_b128 v[72:75], v233 offset:0\n"
"ds_read_b128 v[80:83], v233 offset:768\n"
"s_cmp_lg_u32 s23, 0\n"
"s_cbranch_scc1 nofl_66_%=\n"
"v_lshl_add_u32 v3, v0, 3, s67\n"
"s_mov_b64 exec, 0x1\n"
"global_store_dwordx2 v3, v[242:243], s[18:19] sc1\n"
"s_mov_b64 exec, -1\n"
"nofl_66_%=:\n"
"s_add_u32 m0, s33, 139264\n"
"s_mov_b64 exec, s[48:49]\n"
"global_load_lds_dwordx4 v229, s[34:35] offset:0 sc1\n"
"s_mov_b64 exec, -1\n"
"v_exp_f32 v19, v19\n"
"v_exp_f32 v20, v20\n"
"v_exp_f32 v21, v21\n"
"v_mfma_f32_32x32x16_bf16 v[32:47], v[64:67], v[52:55], 0\n"
"ds_read_b128 v[64:67], v232 offset:0\n"
"v_exp_f32 v22, v22\n"
"v_exp_f32 v23, v23\n"
"v_exp_f32 v24, v24\n"
"v_mfma_f32_32x32x8_bf16 v[32:47], v[70:71], v[62:63], v[32:47]\n"
"v_exp_f32 v25, v25\n"
"v_exp_f32 v26, v26\n"
"v_exp_f32 v27, v27\n"
"v_exp_f32 v28, v28\n"
"v_smfmac_f32_32x32x32_bf16 v[192:207], v[76:79], v[16:23], v240\n"
"ds_read2st64_b64 v[68:71], v234 offset0:0 offset1:6\n"
"s_add_u32 m0, s33, 139264\n"
"s_mov_b64 exec, s[50:51]\n"
"global_load_lds_dwordx4 v230, s[34:35] offset:1024 sc1\n"
"s_mov_b64 exec, -1\n"
"v_exp_f32 v29, v29\n"
"v_exp_f32 v30, v30\n"
"v_exp_f32 v31, v31\n"
"v_exp_f32 v32, v32\n"
"v_exp_f32 v33, v33\n"
"v_exp_f32 v34, v34\n"
"v_smfmac_f32_32x32x32_bf16 v[192:207], v[84:87], v[24:31], v240\n"
"s_waitcnt lgkmcnt(0)\n"
"s_add_u32 m0, s33, 139264\n"
"s_mov_b64 exec, s[52:53]\n"
"global_load_lds_dwordx4 v231, s[34:35] offset:2048 sc1\n"
"s_mov_b64 exec, -1\n"
"s_add_u32 s34, s34, s54\n"
"s_addc_u32 s35, s35, 0\n"
"v_exp_f32 v35, v35\n"
"v_exp_f32 v36, v36\n"
"v_exp_f32 v37, v37\n"
"v_mfma_f32_32x32x16_bf16 v[16:31], v[64:67], v[48:51], 0\n"
"v_exp_f32 v38, v38\n"
"v_exp_f32 v39, v39\n"
"v_exp_f32 v40, v40\n"
"v_mfma_f32_32x32x8_bf16 v[16:31], v[68:69], v[56:57], v[16:31]\n"
"v_exp_f32 v41, v41\n"
"v_exp_f32 v42, v42\n"
"v_exp_f32 v43, v43\n"
"v_exp_f32 v44, v44\n"
"v_smfmac_f32_32x32x32_bf16 v[208:223], v[76:79], v[32:39], v240\n"
"v_exp_f32 v45, v45\n"
"v_exp_f32 v46, v46\n"
"v_exp_f32 v47, v47\n"
"v_exp_f32 v16, v16\n"
"v_exp_f32 v17, v17\n"
"v_exp_f32 v18, v18\n"
"v_smfmac_f32_32x32x32_bf16 v[208:223], v[84:87], v[40:47], v240\n"
"ds_read_b128 v[76:79], v233 offset:1536\n"
"ds_read_b128 v[84:87], v233 offset:2304\n"
"s_cmp_ge_u32 s22, 170\n"
"s_cbranch_scc1 norl_67_%=\n"
"s_mul_i32 s41, s22, 3856\n"
"s_lshr_b32 s41, s41, 16\n"
"s_mul_i32 s41, s41, 17\n"
"s_sub_u32 s45, s22, s41\n"
"s_cmp_lt_u32 s45, 12\n"
"s_cselect_b64 s[36:37], s[16:17], s[12:13]\n"
"s_lshl_b32 m0, s30, 1\n"
"s_and_b32 s45, s77, -64\n"
"s_add_u32 s36, s36, s45\n"
"s_addc_u32 s37, s37, 0\n"
"s_and_b32 s45, s77, 63\n"
"v_lshrrev_b32 v4, 4, v0\n"
"s_lshr_b32 s41, s45, 4\n"
"s_mul_i32 s41, s41, 20480\n"
"v_mul_u32_u24 v4, s41, v4\n"
"v_add_u32 v3, v4, v224\n"
"global_load_lds_dwordx4 v3, s[36:37]\n"
"s_mul_i32 s41, s45, 5120\n"
"s_sub_u32 s45, s41, 1024\n"
"s_add_u32 s36, s36, s45\n"
"s_addc_u32 s37, s37, 0\n"
"global_load_lds_dwordx4 v3, s[36:37] offset:1024\n"
"norl_67_%=:\n"
"v_exp_f32 v19, v19\n"
"v_exp_f32 v20, v20\n"
"v_exp_f32 v21, v21\n"
"v_mfma_f32_32x32x16_bf16 v[32:47], v[64:67], v[52:55], 0\n"
"ds_read_b128 v[64:67], v232 offset:512\n"
"v_exp_f32 v22, v22\n"
"v_exp_f32 v23, v23\n"
"v_exp_f32 v24, v24\n"
"v_mfma_f32_32x32x8_bf16 v[32:47], v[68:69], v[60:61], v[32:47]\n"
"v_exp_f32 v25, v25\n"
"v_exp_f32 v26, v26\n"
"v_exp_f32 v27, v27\n"
"v_exp_f32 v28, v28\n"
"v_smfmac_f32_32x32x32_bf16 v[192:207], v[72:75], v[16:23], v240\n"
"v_exp_f32 v29, v29\n"
"v_exp_f32 v30, v30\n"
"v_exp_f32 v31, v31\n"
"v_exp_f32 v32, v32\n"
"v_exp_f32 v33, v33\n"
"v_exp_f32 v34, v34\n"
"v_smfmac_f32_32x32x32_bf16 v[192:207], v[80:83], v[24:31], v240\n"
"s_waitcnt lgkmcnt(0)\n"
"v_exp_f32 v35, v35\n"
"v_exp_f32 v36, v36\n"
"v_exp_f32 v37, v37\n"
"v_mfma_f32_32x32x16_bf16 v[16:31], v[64:67], v[48:51], 0\n"
"v_exp_f32 v38, v38\n"
"v_exp_f32 v39, v39\n"
"v_exp_f32 v40, v40\n"
"v_mfma_f32_32x32x8_bf16 v[16:31], v[68:69], v[58:59], v[16:31]\n"
"v_exp_f32 v41, v41\n"
"v_exp_f32 v42, v42\n"
"v_exp_f32 v43, v43\n"
"v_exp_f32 v44, v44\n"
"v_smfmac_f32_32x32x32_bf16 v[208:223], v[72:75], v[32:39], v240\n"
"v_exp_f32 v45, v45\n"
"v_exp_f32 v46, v46\n"
"v_exp_f32 v47, v47\n"
"v_exp_f32 v16, v16\n"
"v_exp_f32 v17, v17\n"
"v_exp_f32 v18, v18\n"
"v_smfmac_f32_32x32x32_bf16 v[208:223], v[80:83], v[40:47], v240\n"
"ds_read_b128 v[72:75], v233 offset:3072\n"
"ds_read_b128 v[80:83], v233 offset:3840\n"
"v_exp_f32 v19, v19\n"
"v_exp_f32 v20, v20\n"
"v_exp_f32 v21, v21\n"
"v_mfma_f32_32x32x16_bf16 v[32:47], v[64:67], v[52:55], 0\n"
"ds_read_b128 v[64:67], v232 offset:3072\n"
"v_exp_f32 v22, v22\n"
"v_exp_f32 v23, v23\n"
"v_exp_f32 v24, v24\n"
"v_mfma_f32_32x32x8_bf16 v[32:47], v[68:69], v[62:63], v[32:47]\n"
"v_exp_f32 v25, v25\n"
"v_exp_f32 v26, v26\n"
"v_exp_f32 v27, v27\n"
"v_exp_f32 v28, v28\n"
"v_smfmac_f32_32x32x32_bf16 v[192:207], v[76:79], v[16:23], v240\n"
"v_exp_f32 v29, v29\n"
"v_exp_f32 v30, v30\n"
"v_exp_f32 v31, v31\n"
"v_exp_f32 v32, v32\n"
"v_exp_f32 v33, v33\n"
"v_exp_f32 v34, v34\n"
"v_smfmac_f32_32x32x32_bf16 v[192:207], v[84:87], v[24:31], v240\n"
"s_waitcnt lgkmcnt(0)\n"
"v_exp_f32 v35, v35\n"
"v_exp_f32 v36, v36\n"
"v_exp_f32 v37, v37\n"
"v_mfma_f32_32x32x16_bf16 v[16:31], v[64:67], v[48:51], 0\n"
"s_setprio 2\n"
"v_exp_f32 v38, v38\n"
"v_exp_f32 v39, v39\n"
"v_exp_f32 v40, v40\n"
"v_mfma_f32_32x32x8_bf16 v[16:31], v[70:71], v[56:57], v[16:31]\n"
"v_exp_f32 v41, v41\n"
"v_exp_f32 v42, v42\n"
"v_exp_f32 v43, v43\n"
"v_exp_f32 v44, v44\n"
"v_smfmac_f32_32x32x32_bf16 v[208:223], v[76:79], v[32:39], v240\n"
"v_exp_f32 v45, v45\n"
"v_exp_f32 v46, v46\n"
"v_exp_f32 v47, v47\n"
"v_exp_f32 v16, v16\n"
"v_exp_f32 v17, v17\n"
"v_exp_f32 v18, v18\n"
"v_smfmac_f32_32x32x32_bf16 v[208:223], v[84:87], v[40:47], v240\n"
"ds_read_b128 v[76:79], v233 offset:4608\n"
"ds_read_b128 v[84:87], v233 offset:5376\n"
"v_exp_f32 v19, v19\n"
"v_exp_f32 v20, v20\n"
"v_exp_f32 v21, v21\n"
"v_mfma_f32_32x32x16_bf16 v[32:47], v[64:67], v[52:55], 0\n"
"ds_read_b128 v[64:67], v232 offset:3584\n"
"s_setprio 1\n"
"v_exp_f32 v22, v22\n"
"v_exp_f32 v23, v23\n"
"v_exp_f32 v24, v24\n"
"v_mfma_f32_32x32x8_bf16 v[32:47], v[70:71], v[60:61], v[32:47]\n"
"v_exp_f32 v25, v25\n"
"v_exp_f32 v26, v26\n"
"v_exp_f32 v27, v27\n"
"v_exp_f32 v28, v28\n"
"v_smfmac_f32_32x32x32_bf16 v[192:207], v[72:75], v[16:23], v240\n"
"v_exp_f32 v29, v29\n"
"v_exp_f32 v30, v30\n"
"v_exp_f32 v31, v31\n"
"v_exp_f32 v32, v32\n"
"v_exp_f32 v33, v33\n"
"v_exp_f32 v34, v34\n"
"v_smfmac_f32_32x32x32_bf16 v[192:207], v[80:83], v[24:31], v240\n"
"s_waitcnt lgkmcnt(0)\n"
"v_exp_f32 v35, v35\n"
"v_exp_f32 v36, v36\n"
"v_exp_f32 v37, v37\n"
"v_mfma_f32_32x32x16_bf16 v[16:31], v[64:67], v[48:51], 0\n"
"s_setprio 0\n"
"v_exp_f32 v38, v38\n"
"v_exp_f32 v39, v39\n"
"v_exp_f32 v40, v40\n"
"v_mfma_f32_32x32x8_bf16 v[16:31], v[70:71], v[58:59], v[16:31]\n"
"v_exp_f32 v41, v41\n"
"v_exp_f32 v42, v42\n"
"v_exp_f32 v43, v43\n"
"v_exp_f32 v44, v44\n"
"v_smfmac_f32_32x32x32_bf16 v[208:223], v[72:75], v[32:39], v240\n"
"v_exp_f32 v45, v45\n"
"v_exp_f32 v46, v46\n"
"v_exp_f32 v47, v47\n"
"s_waitcnt vmcnt(0)\n"
"s_barrier\n"
"s_setprio 3\n"
"v_exp_f32 v16, v16\n"
"v_exp_f32 v17, v17\n"
"v_exp_f32 v18, v18\n"
"v_smfmac_f32_32x32x32_bf16 v[208:223], v[80:83], v[40:47], v240\n"
"ds_read_b128 v[72:75], v233 offset:24576\n"
"ds_read_b128 v[80:83], v233 offset:25344\n"
"s_add_u32 m0, s33, 114688\n"
"s_mov_b64 exec, s[48:49]\n"
"global_load_lds_dwordx4 v229, s[34:35] offset:0 sc1\n"
"s_mov_b64 exec, -1\n"
"v_exp_f32 v19, v19\n"
"v_exp_f32 v20, v20\n"
"v_exp_f32 v21, v21\n"
"v_mfma_f32_32x32x16_bf16 v[32:47], v[64:67], v[52:55], 0\n"
"ds_read_b128 v[64:67], v232 offset:24576\n"
"v_exp_f32 v22, v22\n"
"v_exp_f32 v23, v23\n"
"v_exp_f32 v24, v24\n"
"v_mfma_f32_32x32x8_bf16 v[32:47], v[70:71], v[62:63], v[32:47]\n"
"v_exp_f32 v25, v25\n"
"v_exp_f32 v26, v26\n"
"v_exp_f32 v27, v27\n"
"v_exp_f32 v28, v28\n"
"v_smfmac_f32_32x32x32_bf16 v[192:207], v[76:79], v[16:23], v240\n"
"ds_read2st64_b64 v[68:71], v234 offset0:48 offset1:54\n"
"s_add_u32 m0, s33, 114688\n"
"s_mov_b64 exec, s[50:51]\n"
"global_load_lds_dwordx4 v230, s[34:35] offset:1024 sc1\n"
"s_mov_b64 exec, -1\n"
"v_exp_f32 v29, v29\n"
"v_exp_f32 v30, v30\n"
"v_exp_f32 v31, v31\n"
"v_exp_f32 v32, v32\n"
"v_exp_f32 v33, v33\n"
"v_exp_f32 v34, v34\n"
"v_smfmac_f32_32x32x32_bf16 v[192:207], v[84:87], v[24:31], v240\n"
"s_waitcnt lgkmcnt(0)\n"
"s_add_u32 m0, s33, 114688\n"
"s_mov_b64 exec, s[52:53]\n"
"global_load_lds_dwordx4 v231, s[34:35] offset:2048 sc1\n"
"s_mov_b64 exec, -1\n"
"s_add_u32 s34, s34, s54\n"
"s_addc_u32 s35, s35, 0\n"
"v_exp_f32 v35, v35\n"
"v_exp_f32 v36, v36\n"
"v_exp_f32 v37, v37\n"
"v_mfma_f32_32x32x16_bf16 v[16:31], v[64:67], v[48:51], 0\n"
"v_exp_f32 v38, v38\n"
"v_exp_f32 v39, v39\n"
"v_exp_f32 v40, v40\n"
"v_mfma_f32_32x32x8_bf16 v[16:31], v[68:69], v[56:57], v[16:31]\n"
"v_exp_f32 v41, v41\n"
"v_exp_f32 v42, v42\n"
"v_exp_f32 v43, v43\n"
"v_exp_f32 v44, v44\n"
"v_smfmac_f32_32x32x32_bf16 v[208:223], v[76:79], v[32:39], v240\n"
"v_exp_f32 v45, v45\n"
"v_exp_f32 v46, v46\n"
"v_exp_f32 v47, v47\n"
"v_exp_f32 v16, v16\n"
"v_exp_f32 v17, v17\n"
"v_exp_f32 v18, v18\n"
"v_smfmac_f32_32x32x32_bf16 v[208:223], v[84:87], v[40:47], v240\n"
"ds_read_b128 v[76:79], v233 offset:26112\n"
"ds_read_b128 v[84:87], v233 offset:26880\n"
"s_cmp_ge_u32 s22, 170\n"
"s_cbranch_scc1 norl_68_%=\n"
"s_waitcnt vmcnt(3)\n"
"s_lshl_b32 s41, s30, 1\n"
"v_add_u32 v3, s41, v225\n"
"ds_read_b64_tr_b16 v[88:89], v3 offset:0\n"
"ds_read_b64_tr_b16 v[90:91], v3 offset:256\n"
"ds_read_b64_tr_b16 v[92:93], v3 offset:32\n"
"ds_read_b64_tr_b16 v[94:95], v3 offset:288\n"
"norl_68_%=:\n"
"v_exp_f32 v19, v19\n"
"v_exp_f32 v20, v20\n"
"v_exp_f32 v21, v21\n"
"v_mfma_f32_32x32x16_bf16 v[32:47], v[64:67], v[52:55], 0\n"
"ds_read_b128 v[64:67], v232 offset:25088\n"
"v_exp_f32 v22, v22\n"
"v_exp_f32 v23, v23\n"
"v_exp_f32 v24, v24\n"
"v_mfma_f32_32x32x8_bf16 v[32:47], v[68:69], v[60:61], v[32:47]\n"
"v_exp_f32 v25, v25\n"
"v_exp_f32 v26, v26\n"
"v_exp_f32 v27, v27\n"
"v_exp_f32 v28, v28\n"
"v_smfmac_f32_32x32x32_bf16 v[192:207], v[72:75], v[16:23], v240\n"
"v_exp_f32 v29, v29\n"
"v_exp_f32 v30, v30\n"
"v_exp_f32 v31, v31\n"
"v_exp_f32 v32, v32\n"
"v_exp_f32 v33, v33\n"
"v_exp_f32 v34, v34\n"
"v_smfmac_f32_32x32x32_bf16 v[192:207], v[80:83], v[24:31], v240\n"
"s_waitcnt lgkmcnt(0)\n"
"s_cmp_ge_u32 s22, 170\n"
"s_cbranch_scc1 norl_69_%=\n"
"s_add_u32 s36, s18, s73\n"
"s_addc_u32 s37, s19, 0\n"
"global_store_dwordx4 v226, v[88:91], s[36:37] sc1\n"
"norl_69_%=:\n"
"v_exp_f32 v35, v35\n"
"v_exp_f32 v36, v36\n"
"v_exp_f32 v37, v37\n"
"v_mfma_f32_32x32x16_bf16 v[16:31], v[64:67], v[48:51], 0\n"
"v_exp_f32 v38, v38\n"
"v_exp_f32 v39, v39\n"
"v_exp_f32 v40, v40\n"
"v_mfma_f32_32x32x8_bf16 v[16:31], v[68:69], v[58:59], v[16:31]\n"
"v_exp_f32 v41, v41\n"
"v_exp_f32 v42, v42\n"
"v_exp_f32 v43, v43\n"
"v_exp_f32 v44, v44\n"
"v_smfmac_f32_32x32x32_bf16 v[208:223], v[72:75], v[32:39], v240\n"
"s_cmp_ge_u32 s22, 170\n"
"s_cbranch_scc1 norl_70_%=\n"
"s_add_u32 s36, s18, s74\n"
"s_addc_u32 s37, s19, 0\n"
"global_store_dwordx4 v226, v[92:95], s[36:37] sc1\n"
"norl_70_%=:\n"
"v_exp_f32 v45, v45\n"
"v_exp_f32 v46, v46\n"
"v_exp_f32 v47, v47\n"
"v_exp_f32 v16, v16\n"
"v_exp_f32 v17, v17\n"
"v_exp_f32 v18, v18\n"
"v_smfmac_f32_32x32x32_bf16 v[208:223], v[80:83], v[40:47], v240\n"
"ds_read_b128 v[72:75], v233 offset:27648\n"
"ds_read_b128 v[80:83], v233 offset:28416\n"
"v_exp_f32 v19, v19\n"
"v_exp_f32 v20, v20\n"
"v_exp_f32 v21, v21\n"
"v_mfma_f32_32x32x16_bf16 v[32:47], v[64:67], v[52:55], 0\n"
"ds_read_b128 v[64:67], v232 offset:27648\n"
"v_exp_f32 v22, v22\n"
"v_exp_f32 v23, v23\n"
"v_exp_f32 v24, v24\n"
"v_mfma_f32_32x32x8_bf16 v[32:47], v[68:69], v[62:63], v[32:47]\n"
"v_exp_f32 v25, v25\n"
"v_exp_f32 v26, v26\n"
"v_exp_f32 v27, v27\n"
"v_exp_f32 v28, v28\n"
"v_smfmac_f32_32x32x32_bf16 v[192:207], v[76:79], v[16:23], v240\n"
"v_exp_f32 v29, v29\n"
"v_exp_f32 v30, v30\n"
"v_exp_f32 v31, v31\n"
"v_exp_f32 v32, v32\n"
"v_exp_f32 v33, v33\n"
"v_exp_f32 v34, v34\n"
"v_smfmac_f32_32x32x32_bf16 v[192:207], v[84:87], v[24:31], v240\n"
"s_waitcnt lgkmcnt(0)\n"
"v_exp_f32 v35, v35\n"
"v_exp_f32 v36, v36\n"
"v_exp_f32 v37, v37\n"
"v_mfma_f32_32x32x16_bf16 v[16:31], v[64:67], v[48:51], 0\n"
"s_setprio 2\n"
"v_exp_f32 v38, v38\n"
"v_exp_f32 v39, v39\n"
"v_exp_f32 v40, v40\n"
"v_mfma_f32_32x32x8_bf16 v[16:31], v[70:71], v[56:57], v[16:31]\n"
"v_exp_f32 v41, v41\n"
"v_exp_f32 v42, v42\n"
"v_exp_f32 v43, v43\n"
"v_exp_f32 v44, v44\n"
"v_smfmac_f32_32x32x32_bf16 v[208:223], v[76:79], v[32:39], v240\n"
"v_exp_f32 v45, v45\n"
"v_exp_f32 v46, v46\n"
"v_exp_f32 v47, v47\n"
"v_exp_f32 v16, v16\n"
"v_exp_f32 v17, v17\n"
"v_exp_f32 v18, v18\n"
"v_smfmac_f32_32x32x32_bf16 v[208:223], v[84:87], v[40:47], v240\n"
"ds_read_b128 v[76:79], v233 offset:29184\n"
"ds_read_b128 v[84:87], v233 offset:29952\n"
"s_cmp_lg_u32 s23, 0\n"
"s_cbranch_scc1 onew_71_%=\n"
"s_add_u32 s44, s18, s69\n"
"s_addc_u32 s45, s19, 0\n"
"global_load_dwordx2 v[246:247], v239, s[44:45] sc1\n"
"onew_71_%=:\n"
"v_exp_f32 v19, v19\n"
"v_exp_f32 v20, v20\n"
"v_exp_f32 v21, v21\n"
"v_mfma_f32_32x32x16_bf16 v[32:47], v[64:67], v[52:55], 0\n"
"ds_read_b128 v[64:67], v232 offset:28160\n"
"s_setprio 1\n"
"v_exp_f32 v22, v22\n"
"v_exp_f32 v23, v23\n"
"v_exp_f32 v24, v24\n"
"v_mfma_f32_32x32x8_bf16 v[32:47], v[70:71], v[60:61], v[32:47]\n"
"v_exp_f32 v25, v25\n"
"v_exp_f32 v26, v26\n"
"v_exp_f32 v27, v27\n"
"v_exp_f32 v28, v28\n"
"v_smfmac_f32_32x32x32_bf16 v[192:207], v[72:75], v[16:23], v240\n"
"v_exp_f32 v29, v29\n"
"v_exp_f32 v30, v30\n"
"v_exp_f32 v31, v31\n"
"v_exp_f32 v32, v32\n"
"v_exp_f32 v33, v33\n"
"v_exp_f32 v34, v34\n"
"v_smfmac_f32_32x32x32_bf16 v[192:207], v[80:83], v[24:31], v240\n"
"s_waitcnt lgkmcnt(0)\n"
"v_exp_f32 v35, v35\n"
"v_exp_f32 v36, v36\n"
"v_exp_f32 v37, v37\n"
"v_mfma_f32_32x32x16_bf16 v[16:31], v[64:67], v[48:51], 0\n"
"s_setprio 0\n"
"v_exp_f32 v38, v38\n"
"v_exp_f32 v39, v39\n"
"v_exp_f32 v40, v40\n"
"v_mfma_f32_32x32x8_bf16 v[16:31], v[70:71], v[58:59], v[16:31]\n"
"v_exp_f32 v41, v41\n"
"v_exp_f32 v42, v42\n"
"v_exp_f32 v43, v43\n"
"v_exp_f32 v44, v44\n"
"v_smfmac_f32_32x32x32_bf16 v[208:223], v[72:75], v[32:39], v240\n"
"v_exp_f32 v45, v45\n"
"v_exp_f32 v46, v46\n"
"v_exp_f32 v47, v47\n"
"s_cmp_lg_u32 s23, 0\n"
"s_cbranch_scc1 onew_72_%=\n"
"s_waitcnt vmcnt(0)\n"
"v_cmp_ne_u64 vcc, s[24:25], v[246:247]\n"
"s_nop 0\n"
"s_cmp_eq_u64 vcc, 0\n"
"s_cbranch_scc0 retry_73_%=\n"
"ok_74_%=:\n"
"onew_72_%=:\n"
"s_waitcnt vmcnt(0)\n"
"s_barrier\n"
"s_setprio 3\n"
"v_exp_f32 v16, v16\n"
"v_exp_f32 v17, v17\n"
"v_exp_f32 v18, v18\n"
"v_smfmac_f32_32x32x32_bf16 v[208:223], v[80:83], v[40:47], v240\n"
"ds_read_b128 v[72:75], v233 offset:0\n"
"ds_read_b128 v[80:83], v233 offset:768\n"
"s_lshl_b32 s41, s22, 3\n"
"s_add_u32 s45, s41, 16946176\n"
"s_cmp_lg_u32 s23, 0\n"
"s_cbranch_scc1 nofl_75_%=\n"
"v_lshl_add_u32 v3, v0, 3, s45\n"
"s_mov_b64 exec, 0x1\n"
"global_store_dwordx2 v3, v[242:243], s[18:19] sc1\n"
"s_mov_b64 exec, -1\n"
"nofl_75_%=:\n"
"s_add_u32 m0, s33, 139264\n"
"s_mov_b64 exec, s[48:49]\n"
"global_load_lds_dwordx4 v229, s[34:35] offset:0 sc1\n"
"s_mov_b64 exec, -1\n"
"v_exp_f32 v19, v19\n"
"v_exp_f32 v20, v20\n"
"v_exp_f32 v21, v21\n"
"v_mfma_f32_32x32x16_bf16 v[32:47], v[64:67], v[52:55], 0\n"
"ds_read_b128 v[64:67], v232 offset:0\n"
"v_exp_f32 v22, v22\n"
"v_exp_f32 v23, v23\n"
"v_exp_f32 v24, v24\n"
"v_mfma_f32_32x32x8_bf16 v[32:47], v[70:71], v[62:63], v[32:47]\n"
"v_exp_f32 v25, v25\n"
"v_exp_f32 v26, v26\n"
"v_exp_f32 v27, v27\n"
"v_exp_f32 v28, v28\n"
"v_smfmac_f32_32x32x32_bf16 v[192:207], v[76:79], v[16:23], v240\n"
"ds_read2st64_b64 v[68:71], v234 offset0:0 offset1:6\n"
"s_add_u32 m0, s33, 139264\n"
"s_mov_b64 exec, s[50:51]\n"
"global_load_lds_dwordx4 v230, s[34:35] offset:1024 sc1\n"
"s_mov_b64 exec, -1\n"
"v_exp_f32 v29, v29\n"
"v_exp_f32 v30, v30\n"
"v_exp_f32 v31, v31\n"
"v_exp_f32 v32, v32\n"
"v_exp_f32 v33, v33\n"
"v_exp_f32 v34, v34\n"
"v_smfmac_f32_32x32x32_bf16 v[192:207], v[84:87], v[24:31], v240\n"
"s_waitcnt lgkmcnt(0)\n"
"s_add_u32 m0, s33, 139264\n"
"s_mov_b64 exec, s[52:53]\n"
"global_load_lds_dwordx4 v231, s[34:35] offset:2048 sc1\n"
"s_mov_b64 exec, -1\n"
"s_add_u32 s34, s34, s54\n"
"s_addc_u32 s35, s35, 0\n"
"v_exp_f32 v35, v35\n"
"v_exp_f32 v36, v36\n"
"v_exp_f32 v37, v37\n"
"v_mfma_f32_32x32x16_bf16 v[16:31], v[64:67], v[48:51], 0\n"
"v_exp_f32 v38, v38\n"
"v_exp_f32 v39, v39\n"
"v_exp_f32 v40, v40\n"
"v_mfma_f32_32x32x8_bf16 v[16:31], v[68:69], v[56:57], v[16:31]\n"
"v_exp_f32 v41, v41\n"
"v_exp_f32 v42, v42\n"
"v_exp_f32 v43, v43\n"
"v_exp_f32 v44, v44\n"
"v_smfmac_f32_32x32x32_bf16 v[208:223], v[76:79], v[32:39], v240\n"
"s_add_u32 s44, s18, s71\n"
"s_addc_u32 s45, s19, 0\n"
"v_and_b32 v3, 0xf0, v226\n"
"global_load_dword v241, v3, s[44:45] sc1\n"
"v_exp_f32 v45, v45\n"
"v_exp_f32 v46, v46\n"
"v_exp_f32 v47, v47\n"
"v_exp_f32 v16, v16\n"
"v_exp_f32 v17, v17\n"
"v_exp_f32 v18, v18\n"
"v_smfmac_f32_32x32x32_bf16 v[208:223], v[84:87], v[40:47], v240\n"
"ds_read_b128 v[76:79], v233 offset:1536\n"
"ds_read_b128 v[84:87], v233 offset:2304\n"
"global_load_dwordx4 v[96:99], v235, s[38:39] sc1\n"
"s_add_u32 s38, s38, 131072\n"
"s_addc_u32 s39, s39, 0\n"
"v_exp_f32 v19, v19\n"
"v_exp_f32 v20, v20\n"
"v_exp_f32 v21, v21\n"
"v_mfma_f32_32x32x16_bf16 v[32:47], v[64:67], v[52:55], 0\n"
"ds_read_b128 v[64:67], v232 offset:512\n"
"v_exp_f32 v22, v22\n"
"v_exp_f32 v23, v23\n"
"v_exp_f32 v24, v24\n"
"v_mfma_f32_32x32x8_bf16 v[32:47], v[68:69], v[60:61], v[32:47]\n"
"v_exp_f32 v25, v25\n"
"v_exp_f32 v26, v26\n"
"v_exp_f32 v27, v27\n"
"v_exp_f32 v28, v28\n"
"v_smfmac_f32_32x32x32_bf16 v[192:207], v[72:75], v[16:23], v240\n"
"global_load_dwordx4 v[100:103], v235, s[38:39] sc1\n"
"s_add_u32 s38, s38, 131072\n"
"s_addc_u32 s39, s39, 0\n"
"v_exp_f32 v29, v29\n"
"v_exp_f32 v30, v30\n"
"v_exp_f32 v31, v31\n"
"v_exp_f32 v32, v32\n"
"v_exp_f32 v33, v33\n"
"v_exp_f32 v34, v34\n"
"v_smfmac_f32_32x32x32_bf16 v[192:207], v[80:83], v[24:31], v240\n"
"s_waitcnt lgkmcnt(0)\n"
"global_load_dwordx4 v[104:107], v235, s[38:39] sc1\n"
"s_add_u32 s38, s38, 131072\n"
"s_addc_u32 s39, s39, 0\n"
"v_exp_f32 v35, v35\n"
"v_exp_f32 v36, v36\n"
"v_exp_f32 v37, v37\n"
"v_mfma_f32_32x32x16_bf16 v[16:31], v[64:67], v[48:51], 0\n"
"v_exp_f32 v38, v38\n"
"v_exp_f32 v39, v39\n"
"v_exp_f32 v40, v40\n"
"v_mfma_f32_32x32x8_bf16 v[16:31], v[68:69], v[58:59], v[16:31]\n"
"v_exp_f32 v41, v41\n"
"v_exp_f32 v42, v42\n"
"v_exp_f32 v43, v43\n"
"v_exp_f32 v44, v44\n"
"v_smfmac_f32_32x32x32_bf16 v[208:223], v[72:75], v[32:39], v240\n"
"global_load_dwordx4 v[108:111], v235, s[38:39] sc1\n"
"s_add_u32 s38, s38, 131072\n"
"s_addc_u32 s39, s39, 0\n"
"v_exp_f32 v45, v45\n"
"v_exp_f32 v46, v46\n"
"v_exp_f32 v47, v47\n"
"v_exp_f32 v16, v16\n"
"v_exp_f32 v17, v17\n"
"v_exp_f32 v18, v18\n"
"v_smfmac_f32_32x32x32_bf16 v[208:223], v[80:83], v[40:47], v240\n"
"ds_read_b128 v[72:75], v233 offset:3072\n"
"ds_read_b128 v[80:83], v233 offset:3840\n"
"global_load_dwordx4 v[112:115], v235, s[38:39] sc1\n"
"s_add_u32 s38, s38, 131072\n"
"s_addc_u32 s39, s39, 0\n"
"v_exp_f32 v19, v19\n"
"v_exp_f32 v20, v20\n"
"v_exp_f32 v21, v21\n"
"v_mfma_f32_32x32x16_bf16 v[32:47], v[64:67], v[52:55], 0\n"
"ds_read_b128 v[64:67], v232 offset:3072\n"
"v_exp_f32 v22, v22\n"
"v_exp_f32 v23, v23\n"
"v_exp_f32 v24, v24\n"
"v_mfma_f32_32x32x8_bf16 v[32:47], v[68:69], v[62:63], v[32:47]\n"
"v_exp_f32 v25, v25\n"
"v_exp_f32 v26, v26\n"
"v_exp_f32 v27, v27\n"
"v_exp_f32 v28, v28\n"
"v_smfmac_f32_32x32x32_bf16 v[192:207], v[76:79], v[16:23], v240\n"
"global_load_dwordx4 v[116:119], v235, s[38:39] sc1\n"
"s_add_u32 s38, s38, 131072\n"
"s_addc_u32 s39, s39, 0\n"
"v_exp_f32 v29, v29\n"
"v_exp_f32 v30, v30\n"
"v_exp_f32 v31, v31\n"
"v_exp_f32 v32, v32\n"
"v_exp_f32 v33, v33\n"
"v_exp_f32 v34, v34\n"
"v_smfmac_f32_32x32x32_bf16 v[192:207], v[84:87], v[24:31], v240\n"
"s_waitcnt lgkmcnt(0)\n"
"global_load_dwordx4 v[120:123], v235, s[38:39] sc1\n"
"s_add_u32 s38, s38, 131072\n"
"s_addc_u32 s39, s39, 0\n"
"v_exp_f32 v35, v35\n"
"v_exp_f32 v36, v36\n"
"v_exp_f32 v37, v37\n"
"v_mfma_f32_32x32x16_bf16 v[16:31], v[64:67], v[48:51], 0\n"
"s_setprio 2\n"
"v_exp_f32 v38, v38\n"
"v_exp_f32 v39, v39\n"
"v_exp_f32 v40, v40\n"
"v_mfma_f32_32x32x8_bf16 v[16:31], v[70:71], v[56:57], v[16:31]\n"
"v_exp_f32 v41, v41\n"
"v_exp_f32 v42, v42\n"
"v_exp_f32 v43, v43\n"
"v_exp_f32 v44, v44\n"
"v_smfmac_f32_32x32x32_bf16 v[208:223], v[76:79], v[32:39], v240\n"
"global_load_dwordx4 v[124:127], v235, s[38:39] sc1\n"
"s_add_u32 s38, s38, 131072\n"
"s_addc_u32 s39, s39, 0\n"
"v_exp_f32 v45, v45\n"
"v_exp_f32 v46, v46\n"
"v_exp_f32 v47, v47\n"
"v_exp_f32 v16, v16\n"
"v_exp_f32 v17, v17\n"
"v_exp_f32 v18, v18\n"
"v_smfmac_f32_32x32x32_bf16 v[208:223], v[84:87], v[40:47], v240\n"
"ds_read_b128 v[76:79], v233 offset:4608\n"
"ds_read_b128 v[84:87], v233 offset:5376\n"
"global_load_dwordx4 v[128:131], v235, s[38:39] sc1\n"
"s_add_u32 s38, s38, 131072\n"
"s_addc_u32 s39, s39, 0\n"
"v_exp_f32 v19, v19\n"
"v_exp_f32 v20, v20\n"
"v_exp_f32 v21, v21\n"
"v_mfma_f32_32x32x16_bf16 v[32:47], v[64:67], v[52:55], 0\n"
"ds_read_b128 v[64:67], v232 offset:3584\n"
"s_setprio 1\n"
"v_exp_f32 v22, v22\n"
"v_exp_f32 v23, v23\n"
"v_exp_f32 v24, v24\n"
"v_mfma_f32_32x32x8_bf16 v[32:47], v[70:71], v[60:61], v[32:47]\n"
"v_exp_f32 v25, v25\n"
"v_exp_f32 v26, v26\n"
"v_exp_f32 v27, v27\n"
"v_exp_f32 v28, v28\n"
"v_smfmac_f32_32x32x32_bf16 v[192:207], v[72:75], v[16:23], v240\n"
"global_load_dwordx4 v[132:135], v235, s[38:39] sc1\n"
"s_add_u32 s38, s38, 131072\n"
"s_addc_u32 s39, s39, 0\n"
"v_exp_f32 v29, v29\n"
"v_exp_f32 v30, v30\n"
"v_exp_f32 v31, v31\n"
"v_exp_f32 v32, v32\n"
"v_exp_f32 v33, v33\n"
"v_exp_f32 v34, v34\n"
"v_smfmac_f32_32x32x32_bf16 v[192:207], v[80:83], v[24:31], v240\n"
"s_waitcnt lgkmcnt(0)\n"
"global_load_dwordx4 v[136:139], v235, s[38:39] sc1\n"
"s_add_u32 s38, s38, 131072\n"
"s_addc_u32 s39, s39, 0\n"
"v_exp_f32 v35, v35\n"
"v_exp_f32 v36, v36\n"
"v_exp_f32 v37, v37\n"
"v_mfma_f32_32x32x16_bf16 v[16:31], v[64:67], v[48:51], 0\n"
"s_setprio 0\n"
"v_exp_f32 v38, v38\n"
"v_exp_f32 v39, v39\n"
"v_exp_f32 v40, v40\n"
"v_mfma_f32_32x32x8_bf16 v[16:31], v[70:71], v[58:59], v[16:31]\n"
"v_exp_f32 v41, v41\n"
"v_exp_f32 v42, v42\n"
"v_exp_f32 v43, v43\n"
"v_exp_f32 v44, v44\n"
"v_smfmac_f32_32x32x32_bf16 v[208:223], v[72:75], v[32:39], v240\n"
"global_load_dwordx4 v[140:143], v235, s[38:39] sc1\n"
"s_add_u32 s38, s38, 131072\n"
"s_addc_u32 s39, s39, 0\n"
"v_exp_f32 v45, v45\n"
"v_exp_f32 v46, v46\n"
"v_exp_f32 v47, v47\n"
"s_waitcnt vmcnt(13)\n"
"s_barrier\n"
"s_setprio 3\n"
"v_exp_f32 v16, v16\n"
"v_exp_f32 v17, v17\n"
"v_exp_f32 v18, v18\n"
"v_smfmac_f32_32x32x32_bf16 v[208:223], v[80:83], v[40:47], v240\n"
"ds_read_b128 v[72:75], v233 offset:24576\n"
"ds_read_b128 v[80:83], v233 offset:25344\n"
"s_add_u32 m0, s33, 114688\n"
"s_mov_b64 exec, s[48:49]\n"
"global_load_lds_dwordx4 v229, s[34:35] offset:0 sc1\n"
"s_mov_b64 exec, -1\n"
"v_exp_f32 v19, v19\n"
"v_exp_f32 v20, v20\n"
"v_exp_f32 v21, v21\n"
"v_mfma_f32_32x32x16_bf16 v[32:47], v[64:67], v[52:55], 0\n"
"ds_read_b128 v[64:67], v232 offset:24576\n"
"v_exp_f32 v22, v22\n"
"v_exp_f32 v23, v23\n"
"v_exp_f32 v24, v24\n"
"v_mfma_f32_32x32x8_bf16 v[32:47], v[70:71], v[62:63], v[32:47]\n"
"v_exp_f32 v25, v25\n"
"v_exp_f32 v26, v26\n"
"v_exp_f32 v27, v27\n"
"v_exp_f32 v28, v28\n"
"v_smfmac_f32_32x32x32_bf16 v[192:207], v[76:79], v[16:23], v240\n"
"ds_read2st64_b64 v[68:71], v234 offset0:48 offset1:54\n"
"s_add_u32 m0, s33, 114688\n"
"s_mov_b64 exec, s[50:51]\n"
"global_load_lds_dwordx4 v230, s[34:35] offset:1024 sc1\n"
"s_mov_b64 exec, -1\n"
"v_exp_f32 v29, v29\n"
"v_exp_f32 v30, v30\n"
"v_exp_f32 v31, v31\n"
"v_exp_f32 v32, v32\n"
"v_exp_f32 v33, v33\n"
"v_exp_f32 v34, v34\n"
"v_smfmac_f32_32x32x32_bf16 v[192:207], v[84:87], v[24:31], v240\n"
"s_waitcnt lgkmcnt(0)\n"
"s_add_u32 m0, s33, 114688\n"
"s_mov_b64 exec, s[52:53]\n"
"global_load_lds_dwordx4 v231, s[34:35] offset:2048 sc1\n"
"s_mov_b64 exec, -1\n"
"s_add_u32 s34, s34, s54\n"
"s_addc_u32 s35, s35, 0\n"
"v_exp_f32 v35, v35\n"
"v_exp_f32 v36, v36\n"
"v_exp_f32 v37, v37\n"
"v_mfma_f32_32x32x16_bf16 v[16:31], v[64:67], v[48:51], 0\n"
"v_exp_f32 v38, v38\n"
"v_exp_f32 v39, v39\n"
"v_exp_f32 v40, v40\n"
"v_mfma_f32_32x32x8_bf16 v[16:31], v[68:69], v[56:57], v[16:31]\n"
"v_exp_f32 v41, v41\n"
"v_exp_f32 v42, v42\n"
"v_exp_f32 v43, v43\n"
"v_exp_f32 v44, v44\n"
"v_smfmac_f32_32x32x32_bf16 v[208:223], v[76:79], v[32:39], v240\n"
"global_load_dwordx4 v[144:147], v235, s[38:39] sc1\n"
"s_add_u32 s38, s38, 131072\n"
"s_addc_u32 s39, s39, 0\n"
"v_exp_f32 v45, v45\n"
"v_exp_f32 v46, v46\n"
"v_exp_f32 v47, v47\n"
"v_exp_f32 v16, v16\n"
"v_exp_f32 v17, v17\n"
"v_exp_f32 v18, v18\n"
"v_smfmac_f32_32x32x32_bf16 v[208:223], v[84:87], v[40:47], v240\n"
"ds_read_b128 v[76:79], v233 offset:26112\n"
"ds_read_b128 v[84:87], v233 offset:26880\n"
"global_load_dwordx4 v[148:151], v235, s[38:39] sc1\n"
"s_add_u32 s38, s38, 131072\n"
"s_addc_u32 s39, s39, 0\n"
"v_exp_f32 v19, v19\n"
"v_exp_f32 v20, v20\n"
"v_exp_f32 v21, v21\n"
"v_mfma_f32_32x32x16_bf16 v[32:47], v[64:67], v[52:55], 0\n"
"ds_read_b128 v[64:67], v232 offset:25088\n"
"v_exp_f32 v22, v22\n"
"v_exp_f32 v23, v23\n"
"v_exp_f32 v24, v24\n"
"v_mfma_f32_32x32x8_bf16 v[32:47], v[68:69], v[60:61], v[32:47]\n"
"v_exp_f32 v25, v25\n"
"v_exp_f32 v26, v26\n"
"v_exp_f32 v27, v27\n"
"v_exp_f32 v28, v28\n"
"v_smfmac_f32_32x32x32_bf16 v[192:207], v[72:75], v[16:23], v240\n"
"global_load_dwordx4 v[152:155], v235, s[38:39] sc1\n"
"s_add_u32 s38, s38, 131072\n"
"s_addc_u32 s39, s39, 0\n"
"v_exp_f32 v29, v29\n"
"v_exp_f32 v30, v30\n"
"v_exp_f32 v31, v31\n"
"v_exp_f32 v32, v32\n"
"v_exp_f32 v33, v33\n"
"v_exp_f32 v34, v34\n"
"v_smfmac_f32_32x32x32_bf16 v[192:207], v[80:83], v[24:31], v240\n"
"s_waitcnt lgkmcnt(0)\n"
"global_load_dwordx4 v[156:159], v235, s[38:39] sc1\n"
"s_add_u32 s38, s38, 131072\n"
"s_addc_u32 s39, s39, 0\n"
"v_exp_f32 v35, v35\n"
"v_exp_f32 v36, v36\n"
"v_exp_f32 v37, v37\n"
"v_mfma_f32_32x32x16_bf16 v[16:31], v[64:67], v[48:51], 0\n"
"v_exp_f32 v38, v38\n"
"v_exp_f32 v39, v39\n"
"v_exp_f32 v40, v40\n"
"v_mfma_f32_32x32x8_bf16 v[16:31], v[68:69], v[58:59], v[16:31]\n"
"v_exp_f32 v41, v41\n"
"v_exp_f32 v42, v42\n"
"v_exp_f32 v43, v43\n"
"v_exp_f32 v44, v44\n"
"v_smfmac_f32_32x32x32_bf16 v[208:223], v[72:75], v[32:39], v240\n"
"global_load_dwordx4 v[160:163], v235, s[38:39] sc1\n"
"s_add_u32 s38, s38, 131072\n"
"s_addc_u32 s39, s39, 0\n"
"v_exp_f32 v45, v45\n"
"v_exp_f32 v46, v46\n"
"v_exp_f32 v47, v47\n"
"v_exp_f32 v16, v16\n"
"v_exp_f32 v17, v17\n"
"v_exp_f32 v18, v18\n"
"v_smfmac_f32_32x32x32_bf16 v[208:223], v[80:83], v[40:47], v240\n"
"ds_read_b128 v[72:75], v233 offset:27648\n"
"ds_read_b128 v[80:83], v233 offset:28416\n"
"global_load_dwordx4 v[164:167], v235, s[38:39] sc1\n"
"s_add_u32 s38, s38, 131072\n"
"s_addc_u32 s39, s39, 0\n"
"v_exp_f32 v19, v19\n"
"v_exp_f32 v20, v20\n"
"v_exp_f32 v21, v21\n"
"v_mfma_f32_32x32x16_bf16 v[32:47], v[64:67], v[52:55], 0\n"
"ds_read_b128 v[64:67], v232 offset:27648\n"
"v_exp_f32 v22, v22\n"
"v_exp_f32 v23, v23\n"
"v_exp_f32 v24, v24\n"
"v_mfma_f32_32x32x8_bf16 v[32:47], v[68:69], v[62:63], v[32:47]\n"
"v_exp_f32 v25, v25\n"
"v_exp_f32 v26, v26\n"
"v_exp_f32 v27, v27\n"
"v_exp_f32 v28, v28\n"
"v_smfmac_f32_32x32x32_bf16 v[192:207], v[76:79], v[16:23], v240\n"
"global_load_dwordx4 v[168:171], v235, s[38:39] sc1\n"
"s_add_u32 s38, s38, 131072\n"
"s_addc_u32 s39, s39, 0\n"
"v_exp_f32 v29, v29\n"
"v_exp_f32 v30, v30\n"
"v_exp_f32 v31, v31\n"
"v_exp_f32 v32, v32\n"
"v_exp_f32 v33, v33\n"
"v_exp_f32 v34, v34\n"
"v_smfmac_f32_32x32x32_bf16 v[192:207], v[84:87], v[24:31], v240\n"
"s_waitcnt lgkmcnt(0)\n"
"global_load_dwordx4 v[172:175], v235, s[38:39] sc1\n"
"s_add_u32 s38, s38, 131072\n"
"s_addc_u32 s39, s39, 0\n"
"v_exp_f32 v35, v35\n"
"v_exp_f32 v36, v36\n"
"v_exp_f32 v37, v37\n"
"v_mfma_f32_32x32x16_bf16 v[16:31], v[64:67], v[48:51], 0\n"
"s_setprio 2\n"
"v_exp_f32 v38, v38\n"
"v_exp_f32 v39, v39\n"
"v_exp_f32 v40, v40\n"
"v_mfma_f32_32x32x8_bf16 v[16:31], v[70:71], v[56:57], v[16:31]\n"
"v_exp_f32 v41, v41\n"
"v_exp_f32 v42, v42\n"
"v_exp_f32 v43, v43\n"
"v_exp_f32 v44, v44\n"
"v_smfmac_f32_32x32x32_bf16 v[208:223], v[76:79], v[32:39], v240\n"
"global_load_dwordx4 v[176:179], v235, s[38:39] sc1\n"
"s_add_u32 s38, s38, 131072\n"
"s_addc_u32 s39, s39, 0\n"
"v_exp_f32 v45, v45\n"
"v_exp_f32 v46, v46\n"
"v_exp_f32 v47, v47\n"
"v_exp_f32 v16, v16\n"
"v_exp_f32 v17, v17\n"
"v_exp_f32 v18, v18\n"
"v_smfmac_f32_32x32x32_bf16 v[208:223], v[84:87], v[40:47], v240\n"
"ds_read_b128 v[76:79], v233 offset:29184\n"
"ds_read_b128 v[84:87], v233 offset:29952\n"
"s_cmp_lg_u32 s23, 0\n"
"s_cbranch_scc1 onew_76_%=\n"
"s_add_u32 s44, s18, s78\n"
"s_addc_u32 s45, s19, 0\n"
"v_min_u32 v4, 33, v0\n"
"v_mul_u32_u24 v4, 8, v4\n"
"global_load_dwordx2 v[246:247], v4, s[44:45] sc1\n"
"onew_76_%=:\n"
"v_exp_f32 v19, v19\n"
"v_exp_f32 v20, v20\n"
"v_exp_f32 v21, v21\n"
"v_mfma_f32_32x32x16_bf16 v[32:47], v[64:67], v[52:55], 0\n"
"ds_read_b128 v[64:67], v232 offset:28160\n"
"s_setprio 1\n"
"v_exp_f32 v22, v22\n"
"v_exp_f32 v23, v23\n"
"v_exp_f32 v24, v24\n"
"v_mfma_f32_32x32x8_bf16 v[32:47], v[70:71], v[60:61], v[32:47]\n"
"v_exp_f32 v25, v25\n"
"v_exp_f32 v26, v26\n"
"v_exp_f32 v27, v27\n"
"v_exp_f32 v28, v28\n"
"v_smfmac_f32_32x32x32_bf16 v[192:207], v[72:75], v[16:23], v240\n"
"global_load_dwordx4 v[180:183], v235, s[38:39] sc1\n"
"s_add_u32 s38, s38, 131072\n"
"s_addc_u32 s39, s39, 0\n"
"v_exp_f32 v29, v29\n"
"v_exp_f32 v30, v30\n"
"v_exp_f32 v31, v31\n"
"v_exp_f32 v32, v32\n"
"v_exp_f32 v33, v33\n"
"v_exp_f32 v34, v34\n"
"v_smfmac_f32_32x32x32_bf16 v[192:207], v[80:83], v[24:31], v240\n"
"s_waitcnt lgkmcnt(0)\n"
"global_load_dwordx4 v[184:187], v235, s[38:39] sc1\n"
"s_add_u32 s38, s38, 131072\n"
"s_addc_u32 s39, s39, 0\n"
"v_exp_f32 v35, v35\n"
"v_exp_f32 v36, v36\n"
"v_exp_f32 v37, v37\n"
"v_mfma_f32_32x32x16_bf16 v[16:31], v[64:67], v[48:51], 0\n"
"s_setprio 0\n"
"v_exp_f32 v38, v38\n"
"v_exp_f32 v39, v39\n"
"v_exp_f32 v40, v40\n"
"v_mfma_f32_32x32x8_bf16 v[16:31], v[70:71], v[58:59], v[16:31]\n"
"v_exp_f32 v41, v41\n"
"v_exp_f32 v42, v42\n"
"v_exp_f32 v43, v43\n"
"v_exp_f32 v44, v44\n"
"v_smfmac_f32_32x32x32_bf16 v[208:223], v[72:75], v[32:39], v240\n"
"global_load_dwordx4 v[188:191], v235, s[38:39] sc1\n"
"s_add_u32 s38, s38, 131072\n"
"s_addc_u32 s39, s39, 0\n"
"v_exp_f32 v45, v45\n"
"v_exp_f32 v46, v46\n"
"v_exp_f32 v47, v47\n"
"s_cmp_lg_u32 s23, 0\n"
"s_cbranch_scc1 onew_77_%=\n"
"s_waitcnt vmcnt(3)\n"
"v_cmp_ne_u64 vcc, s[24:25], v[246:247]\n"
"s_nop 0\n"
"s_cmp_eq_u64 vcc, 0\n"
"s_cbranch_scc0 retry_78_%=\n"
"ok_79_%=:\n"
"onew_77_%=:\n"
"s_waitcnt vmcnt(12)\n"
"s_barrier\n"
"s_setprio 3\n"
"v_exp_f32 v16, v16\n"
"v_exp_f32 v17, v17\n"
"v_exp_f32 v18, v18\n"
"v_smfmac_f32_32x32x32_bf16 v[208:223], v[80:83], v[40:47], v240\n"
"ds_read_b128 v[72:75], v233 offset:0\n"
"ds_read_b128 v[80:83], v233 offset:768\n"
"s_add_u32 m0, s33, 139264\n"
"s_mov_b64 exec, s[48:49]\n"
"global_load_lds_dwordx4 v229, s[34:35] offset:0 sc1\n"
"s_mov_b64 exec, -1\n"
"v_exp_f32 v19, v19\n"
"v_exp_f32 v20, v20\n"
"v_exp_f32 v21, v21\n"
"v_mfma_f32_32x32x16_bf16 v[32:47], v[64:67], v[52:55], 0\n"
"ds_read_b128 v[64:67], v232 offset:0\n"
"v_exp_f32 v22, v22\n"
"v_exp_f32 v23, v23\n"
"v_exp_f32 v24, v24\n"
"v_mfma_f32_32x32x8_bf16 v[32:47], v[70:71], v[62:63], v[32:47]\n"
"v_exp_f32 v25, v25\n"
"v_exp_f32 v26, v26\n"
"v_exp_f32 v27, v27\n"
"v_exp_f32 v28, v28\n"
"v_smfmac_f32_32x32x32_bf16 v[192:207], v[76:79], v[16:23], v240\n"
"ds_read2st64_b64 v[68:71], v234 offset0:0 offset1:6\n"
"s_add_u32 m0, s33, 139264\n"
"s_mov_b64 exec, s[50:51]\n"
"global_load_lds_dwordx4 v230, s[34:35] offset:1024 sc1\n"
"s_mov_b64 exec, -1\n"
"v_exp_f32 v29, v29\n"
"v_exp_f32 v30, v30\n"
"v_exp_f32 v31, v31\n"
"v_exp_f32 v32, v32\n"
"v_exp_f32 v33, v33\n"
"v_exp_f32 v34, v34\n"
"v_smfmac_f32_32x32x32_bf16 v[192:207], v[84:87], v[24:31], v240\n"
"s_waitcnt lgkmcnt(0)\n"
"s_add_u32 m0, s33, 139264\n"
"s_mov_b64 exec, s[52:53]\n"
"global_load_lds_dwordx4 v231, s[34:35] offset:2048 sc1\n"
"s_mov_b64 exec, -1\n"
"s_add_u32 s34, s34, s54\n"
"s_addc_u32 s35, s35, 0\n"
"v_exp_f32 v35, v35\n"
"v_exp_f32 v36, v36\n"
"v_exp_f32 v37, v37\n"
"v_mfma_f32_32x32x16_bf16 v[16:31], v[64:67], v[48:51], 0\n"
"v_exp_f32 v38, v38\n"
"v_exp_f32 v39, v39\n"
"v_exp_f32 v40, v40\n"
"v_mfma_f32_32x32x8_bf16 v[16:31], v[68:69], v[56:57], v[16:31]\n"
"v_exp_f32 v41, v41\n"
"v_exp_f32 v42, v42\n"
"v_exp_f32 v43, v43\n"
"v_exp_f32 v44, v44\n"
"v_smfmac_f32_32x32x32_bf16 v[208:223], v[76:79], v[32:39], v240\n"
"s_add_u32 m0, s30, 0\n"
"s_add_u32 s36, s18, s72\n"
"s_addc_u32 s37, s19, 0\n"
"global_load_lds_dwordx4 v226, s[36:37] sc1\n"
"v_exp_f32 v45, v45\n"
"v_exp_f32 v46, v46\n"
"v_exp_f32 v47, v47\n"
"v_exp_f32 v16, v16\n"
"v_exp_f32 v17, v17\n"
"v_exp_f32 v18, v18\n"
"v_smfmac_f32_32x32x32_bf16 v[208:223], v[84:87], v[40:47], v240\n"
"ds_read_b128 v[76:79], v233 offset:1536\n"
"ds_read_b128 v[84:87], v233 offset:2304\n"
"s_add_u32 m0, s30, 8192\n"
"s_add_u32 s36, s36, 8192\n"
"s_addc_u32 s37, s37, 0\n"
"global_load_lds_dwordx4 v226, s[36:37] sc1\n"
"v_exp_f32 v19, v19\n"
"v_exp_f32 v20, v20\n"
"v_exp_f32 v21, v21\n"
"v_mfma_f32_32x32x16_bf16 v[32:47], v[64:67], v[52:55], 0\n"
"ds_read_b128 v[64:67], v232 offset:512\n"
"v_exp_f32 v22, v22\n"
"v_exp_f32 v23, v23\n"
"v_exp_f32 v24, v24\n"
"v_mfma_f32_32x32x8_bf16 v[32:47], v[68:69], v[60:61], v[32:47]\n"
"v_exp_f32 v25, v25\n"
"v_exp_f32 v26, v26\n"
"v_exp_f32 v27, v27\n"
"v_exp_f32 v28, v28\n"
"v_smfmac_f32_32x32x32_bf16 v[192:207], v[72:75], v[16:23], v240\n"
"s_add_u32 m0, s30, 16384\n"
"s_add_u32 s36, s36, 8192\n"
"s_addc_u32 s37, s37, 0\n"
"global_load_lds_dwordx4 v226, s[36:37] sc1\n"
"v_exp_f32 v29, v29\n"
"v_exp_f32 v30, v30\n"
"v_exp_f32 v31, v31\n"
"v_exp_f32 v32, v32\n"
"v_exp_f32 v33, v33\n"
"v_exp_f32 v34, v34\n"
"v_smfmac_f32_32x32x32_bf16 v[192:207], v[80:83], v[24:31], v240\n"
"s_waitcnt lgkmcnt(0)\n"
"s_add_u32 m0, s30, 24576\n"
"s_add_u32 s36, s36, 8192\n"
"s_addc_u32 s37, s37, 0\n"
"global_load_lds_dwordx4 v226, s[36:37] sc1\n"
"v_exp_f32 v35, v35\n"
"v_exp_f32 v36, v36\n"
"v_exp_f32 v37, v37\n"
"v_mfma_f32_32x32x16_bf16 v[16:31], v[64:67], v[48:51], 0\n"
"v_exp_f32 v38, v38\n"
"v_exp_f32 v39, v39\n"
"v_exp_f32 v40, v40\n"
"v_mfma_f32_32x32x8_bf16 v[16:31], v[68:69], v[58:59], v[16:31]\n"
"v_exp_f32 v41, v41\n"
"v_exp_f32 v42, v42\n"
"v_exp_f32 v43, v43\n"
"v_exp_f32 v44, v44\n"
"v_smfmac_f32_32x32x32_bf16 v[208:223], v[72:75], v[32:39], v240\n"
"s_add_u32 m0, s30, 32768\n"
"s_add_u32 s36, s36, 8192\n"
"s_addc_u32 s37, s37, 0\n"
"global_load_lds_dwordx4 v226, s[36:37] sc1\n"
"v_exp_f32 v45, v45\n"
"v_exp_f32 v46, v46\n"
"v_exp_f32 v47, v47\n"
"v_exp_f32 v16, v16\n"
"v_exp_f32 v17, v17\n"
"v_exp_f32 v18, v18\n"
"v_smfmac_f32_32x32x32_bf16 v[208:223], v[80:83], v[40:47], v240\n"
"ds_read_b128 v[72:75], v233 offset:3072\n"
"ds_read_b128 v[80:83], v233 offset:3840\n"
"s_add_u32 m0, s30, 40960\n"
"s_add_u32 s36, s36, 8192\n"
"s_addc_u32 s37, s37, 0\n"
"global_load_lds_dwordx4 v226, s[36:37] sc1\n"
"v_exp_f32 v19, v19\n"
"v_exp_f32 v20, v20\n"
"v_exp_f32 v21, v21\n"
"v_mfma_f32_32x32x16_bf16 v[32:47], v[64:67], v[52:55], 0\n"
"ds_read_b128 v[64:67], v232 offset:3072\n"
"v_exp_f32 v22, v22\n"
"v_exp_f32 v23, v23\n"
"v_exp_f32 v24, v24\n"
"v_mfma_f32_32x32x8_bf16 v[32:47], v[68:69], v[62:63], v[32:47]\n"
"v_exp_f32 v25, v25\n"
"v_exp_f32 v26, v26\n"
"v_exp_f32 v27, v27\n"
"v_exp_f32 v28, v28\n"
"v_smfmac_f32_32x32x32_bf16 v[192:207], v[76:79], v[16:23], v240\n"
"s_add_u32 m0, s30, 49152\n"
"s_add_u32 s36, s36, 8192\n"
"s_addc_u32 s37, s37, 0\n"
"global_load_lds_dwordx4 v226, s[36:37] sc1\n"
"v_exp_f32 v29, v29\n"
"v_exp_f32 v30, v30\n"
"v_exp_f32 v31, v31\n"
"v_exp_f32 v32, v32\n"
"v_exp_f32 v33, v33\n"
"v_exp_f32 v34, v34\n"
"v_smfmac_f32_32x32x32_bf16 v[192:207], v[84:87], v[24:31], v240\n"
"s_waitcnt lgkmcnt(0)\n"
"s_add_u32 m0, s30, 57344\n"
"s_add_u32 s36, s36, 8192\n"
"s_addc_u32 s37, s37, 0\n"
"global_load_lds_dwordx4 v226, s[36:37] sc1\n"
"v_exp_f32 v35, v35\n"
"v_exp_f32 v36, v36\n"
"v_exp_f32 v37, v37\n"
"v_mfma_f32_32x32x16_bf16 v[16:31], v[64:67], v[48:51], 0\n"
"s_setprio 2\n"
"v_exp_f32 v38, v38\n"
"v_exp_f32 v39, v39\n"
"v_exp_f32 v40, v40\n"
"v_mfma_f32_32x32x8_bf16 v[16:31], v[70:71], v[56:57], v[16:31]\n"
"v_exp_f32 v41, v41\n"
"v_exp_f32 v42, v42\n"
"v_exp_f32 v43, v43\n"
"v_exp_f32 v44, v44\n"
"v_smfmac_f32_32x32x32_bf16 v[208:223], v[76:79], v[32:39], v240\n"
"s_add_u32 m0, s30, 65536\n"
"s_add_u32 s36, s36, 8192\n"
"s_addc_u32 s37, s37, 0\n"
"global_load_lds_dwordx4 v226, s[36:37] sc1\n"
"v_exp_f32 v45, v45\n"
"v_exp_f32 v46, v46\n"
"v_exp_f32 v47, v47\n"
"v_exp_f32 v16, v16\n"
"v_exp_f32 v17, v17\n"
"v_exp_f32 v18, v18\n"
"v_smfmac_f32_32x32x32_bf16 v[208:223], v[84:87], v[40:47], v240\n"
"ds_read_b128 v[76:79], v233 offset:4608\n"
"ds_read_b128 v[84:87], v233 offset:5376\n"
"s_add_u32 m0, s30, 73728\n"
"s_add_u32 s36, s36, 8192\n"
"s_addc_u32 s37, s37, 0\n"
"global_load_lds_dwordx4 v226, s[36:37] sc1\n"
"v_exp_f32 v19, v19\n"
"v_exp_f32 v20, v20\n"
"v_exp_f32 v21, v21\n"
"v_mfma_f32_32x32x16_bf16 v[32:47], v[64:67], v[52:55], 0\n"
"ds_read_b128 v[64:67], v232 offset:3584\n"
"s_setprio 1\n"
"v_exp_f32 v22, v22\n"
"v_exp_f32 v23, v23\n"
"v_exp_f32 v24, v24\n"
"v_mfma_f32_32x32x8_bf16 v[32:47], v[70:71], v[60:61], v[32:47]\n"
"v_exp_f32 v25, v25\n"
"v_exp_f32 v26, v26\n"
"v_exp_f32 v27, v27\n"
"v_exp_f32 v28, v28\n"
"v_smfmac_f32_32x32x32_bf16 v[192:207], v[72:75], v[16:23], v240\n"
"s_add_u32 m0, s30, 81920\n"
"s_add_u32 s36, s36, 8192\n"
"s_addc_u32 s37, s37, 0\n"
"global_load_lds_dwordx4 v226, s[36:37] sc1\n"
"v_exp_f32 v29, v29\n"
"v_exp_f32 v30, v30\n"
"v_exp_f32 v31, v31\n"
"v_exp_f32 v32, v32\n"
"v_exp_f32 v33, v33\n"
"v_exp_f32 v34, v34\n"
"v_smfmac_f32_32x32x32_bf16 v[192:207], v[80:83], v[24:31], v240\n"
"s_waitcnt lgkmcnt(0)\n"
"s_add_u32 m0, s30, 90112\n"
"s_add_u32 s36, s36, 8192\n"
"s_addc_u32 s37, s37, 0\n"
"global_load_lds_dwordx4 v226, s[36:37] sc1\n"
"v_exp_f32 v35, v35\n"
"v_exp_f32 v36, v36\n"
"v_exp_f32 v37, v37\n"
"v_mfma_f32_32x32x16_bf16 v[16:31], v[64:67], v[48:51], 0\n"
"s_setprio 0\n"
"v_exp_f32 v38, v38\n"
"v_exp_f32 v39, v39\n"
"v_exp_f32 v40, v40\n"
"v_mfma_f32_32x32x8_bf16 v[16:31], v[70:71], v[58:59], v[16:31]\n"
"v_exp_f32 v41, v41\n"
"v_exp_f32 v42, v42\n"
"v_exp_f32 v43, v43\n"
"v_exp_f32 v44, v44\n"
"v_smfmac_f32_32x32x32_bf16 v[208:223], v[72:75], v[32:39], v240\n"
"s_add_u32 m0, s30, 98304\n"
"s_add_u32 s36, s36, 8192\n"
"s_addc_u32 s37, s37, 0\n"
"global_load_lds_dwordx4 v226, s[36:37] sc1\n"
"v_exp_f32 v45, v45\n"
"v_exp_f32 v46, v46\n"
"v_exp_f32 v47, v47\n"
"s_waitcnt vmcnt(13)\n"
"s_barrier\n"
"s_setprio 3\n"
"v_exp_f32 v16, v16\n"
"v_exp_f32 v17, v17\n"
"v_exp_f32 v18, v18\n"
"v_smfmac_f32_32x32x32_bf16 v[208:223], v[80:83], v[40:47], v240\n"
"ds_read_b128 v[72:75], v233 offset:24576\n"
"ds_read_b128 v[80:83], v233 offset:25344\n"
"s_add_u32 m0, s30, 106496\n"
"s_add_u32 s36, s36, 8192\n"
"s_addc_u32 s37, s37, 0\n"
"global_load_lds_dwordx4 v226, s[36:37] sc1\n"
"v_exp_f32 v19, v19\n"
"v_exp_f32 v20, v20\n"
"v_exp_f32 v21, v21\n"
"v_mfma_f32_32x32x16_bf16 v[32:47], v[64:67], v[52:55], 0\n"
"ds_read_b128 v[64:67], v232 offset:24576\n"
"v_exp_f32 v22, v22\n"
"v_exp_f32 v23, v23\n"
"v_exp_f32 v24, v24\n"
"v_mfma_f32_32x32x8_bf16 v[32:47], v[70:71], v[62:63], v[32:47]\n"
"v_exp_f32 v25, v25\n"
"v_exp_f32 v26, v26\n"
"v_exp_f32 v27, v27\n"
"v_exp_f32 v28, v28\n"
"v_smfmac_f32_32x32x32_bf16 v[192:207], v[76:79], v[16:23], v240\n"
"ds_read2st64_b64 v[68:71], v234 offset0:48 offset1:54\n"
"v_exp_f32 v29, v29\n"
"v_exp_f32 v30, v30\n"
"v_exp_f32 v31, v31\n"
"v_exp_f32 v32, v32\n"
"v_exp_f32 v33, v33\n"
"v_exp_f32 v34, v34\n"
"v_smfmac_f32_32x32x32_bf16 v[192:207], v[84:87], v[24:31], v240\n"
"s_waitcnt lgkmcnt(0)\n"
"v_exp_f32 v35, v35\n"
"v_exp_f32 v36, v36\n"
"v_exp_f32 v37, v37\n"
"v_mfma_f32_32x32x16_bf16 v[16:31], v[64:67], v[48:51], 0\n"
"v_exp_f32 v38, v38\n"
"v_exp_f32 v39, v39\n"
"v_exp_f32 v40, v40\n"
"v_mfma_f32_32x32x8_bf16 v[16:31], v[68:69], v[56:57], v[16:31]\n"
"v_exp_f32 v41, v41\n"
"v_exp_f32 v42, v42\n"
"v_exp_f32 v43, v43\n"
"v_exp_f32 v44, v44\n"
"v_smfmac_f32_32x32x32_bf16 v[208:223], v[76:79], v[32:39], v240\n"
"v_exp_f32 v45, v45\n"
"v_exp_f32 v46, v46\n"
"v_exp_f32 v47, v47\n"
"v_exp_f32 v16, v16\n"
"v_exp_f32 v17, v17\n"
"v_exp_f32 v18, v18\n"
"v_smfmac_f32_32x32x32_bf16 v[208:223], v[84:87], v[40:47], v240\n"
"ds_read_b128 v[76:79], v233 offset:26112\n"
"ds_read_b128 v[84:87], v233 offset:26880\n"
"v_exp_f32 v19, v19\n"
"v_exp_f32 v20, v20\n"
"v_exp_f32 v21, v21\n"
"v_mfma_f32_32x32x16_bf16 v[32:47], v[64:67], v[52:55], 0\n"
"ds_read_b128 v[64:67], v232 offset:25088\n"
"v_exp_f32 v22, v22\n"
"v_exp_f32 v23, v23\n"
"v_exp_f32 v24, v24\n"
"v_mfma_f32_32x32x8_bf16 v[32:47], v[68:69], v[60:61], v[32:47]\n"
"v_exp_f32 v25, v25\n"
"v_exp_f32 v26, v26\n"
"v_exp_f32 v27, v27\n"
"v_exp_f32 v28, v28\n"
"v_smfmac_f32_32x32x32_bf16 v[192:207], v[72:75], v[16:23], v240\n"
"v_exp_f32 v29, v29\n"
"v_exp_f32 v30, v30\n"
"v_exp_f32 v31, v31\n"
"v_exp_f32 v32, v32\n"
"v_exp_f32 v33, v33\n"
"v_exp_f32 v34, v34\n"
"v_smfmac_f32_32x32x32_bf16 v[192:207], v[80:83], v[24:31], v240\n"
"s_waitcnt lgkmcnt(0)\n"
"v_exp_f32 v35, v35\n"
"v_exp_f32 v36, v36\n"
"v_exp_f32 v37, v37\n"
"v_mfma_f32_32x32x16_bf16 v[16:31], v[64:67], v[48:51], 0\n"
"v_exp_f32 v38, v38\n"
"v_exp_f32 v39, v39\n"
"v_exp_f32 v40, v40\n"
"v_mfma_f32_32x32x8_bf16 v[16:31], v[68:69], v[58:59], v[16:31]\n"
"v_exp_f32 v41, v41\n"
"v_exp_f32 v42, v42\n"
"v_exp_f32 v43, v43\n"
"v_exp_f32 v44, v44\n"
"v_smfmac_f32_32x32x32_bf16 v[208:223], v[72:75], v[32:39], v240\n"
"v_exp_f32 v45, v45\n"
"v_exp_f32 v46, v46\n"
"v_exp_f32 v47, v47\n"
"v_exp_f32 v16, v16\n"
"v_exp_f32 v17, v17\n"
"v_exp_f32 v18, v18\n"
"v_smfmac_f32_32x32x32_bf16 v[208:223], v[80:83], v[40:47], v240\n"
"ds_read_b128 v[72:75], v233 offset:27648\n"
"ds_read_b128 v[80:83], v233 offset:28416\n"
"v_exp_f32 v19, v19\n"
"v_exp_f32 v20, v20\n"
"v_exp_f32 v21, v21\n"
"v_mfma_f32_32x32x16_bf16 v[32:47], v[64:67], v[52:55], 0\n"
"ds_read_b128 v[64:67], v232 offset:27648\n"
"v_exp_f32 v22, v22\n"
"v_exp_f32 v23, v23\n"
"v_exp_f32 v24, v24\n"
"v_mfma_f32_32x32x8_bf16 v[32:47], v[68:69], v[62:63], v[32:47]\n"
"v_exp_f32 v25, v25\n"
"v_exp_f32 v26, v26\n"
"v_exp_f32 v27, v27\n"
"v_exp_f32 v28, v28\n"
"v_smfmac_f32_32x32x32_bf16 v[192:207], v[76:79], v[16:23], v240\n"
"ds_read_b128 v[92:95], v232 offset:28160\n"
"v_exp_f32 v29, v29\n"
"v_exp_f32 v30, v30\n"
"v_exp_f32 v31, v31\n"
"v_exp_f32 v32, v32\n"
"v_exp_f32 v33, v33\n"
"v_exp_f32 v34, v34\n"
"v_smfmac_f32_32x32x32_bf16 v[192:207], v[84:87], v[24:31], v240\n"
"s_waitcnt lgkmcnt(0)\n"
"v_exp_f32 v35, v35\n"
"v_exp_f32 v36, v36\n"
"v_exp_f32 v37, v37\n"
"v_mfma_f32_32x32x16_bf16 v[16:31], v[64:67], v[48:51], 0\n"
"s_setprio 2\n"
"v_exp_f32 v38, v38\n"
"v_exp_f32 v39, v39\n"
"v_exp_f32 v40, v40\n"
"v_mfma_f32_32x32x8_bf16 v[16:31], v[70:71], v[56:57], v[16:31]\n"
"v_exp_f32 v41, v41\n"
"v_exp_f32 v42, v42\n"
"v_exp_f32 v43, v43\n"
"v_exp_f32 v44, v44\n"
"v_smfmac_f32_32x32x32_bf16 v[208:223], v[76:79], v[32:39], v240\n"
"v_exp_f32 v45, v45\n"
"v_exp_f32 v46, v46\n"
"v_exp_f32 v47, v47\n"
"v_exp_f32 v16, v16\n"
"v_exp_f32 v17, v17\n"
"v_exp_f32 v18, v18\n"
"v_smfmac_f32_32x32x32_bf16 v[208:223], v[84:87], v[40:47], v240\n"
"ds_read_b128 v[76:79], v233 offset:29184\n"
"ds_read_b128 v[84:87], v233 offset:29952\n"
"s_add_u32 s34, s18, s63\n"
"s_addc_u32 s35, s19, 0\n"
"s_add_u32 s38, s18, s64\n"
"s_addc_u32 s39, s19, 0\n"
"v_exp_f32 v19, v19\n"
"v_exp_f32 v20, v20\n"
"v_exp_f32 v21, v21\n"
"v_mfma_f32_32x32x16_bf16 v[32:47], v[92:95], v[48:51], 0\n"
"v_exp_f32 v22, v22\n"
"v_exp_f32 v23, v23\n"
"v_exp_f32 v24, v24\n"
"v_mfma_f32_32x32x8_bf16 v[32:47], v[70:71], v[58:59], v[32:47]\n"
"v_exp_f32 v25, v25\n"
"v_exp_f32 v26, v26\n"
"v_exp_f32 v27, v27\n"
"v_exp_f32 v28, v28\n"
"v_smfmac_f32_32x32x32_bf16 v[192:207], v[72:75], v[16:23], v240\n"
"v_exp_f32 v29, v29\n"
"v_exp_f32 v30, v30\n"
"v_exp_f32 v31, v31\n"
"v_exp_f32 v32, v32\n"
"v_exp_f32 v33, v33\n"
"v_exp_f32 v34, v34\n"
"v_smfmac_f32_32x32x32_bf16 v[192:207], v[80:83], v[24:31], v240\n"
"s_waitcnt lgkmcnt(0)\n"
"v_exp_f32 v35, v35\n"
"v_exp_f32 v36, v36\n"
"v_exp_f32 v37, v37\n"
"v_mfma_f32_32x32x16_bf16 v[16:31], v[64:67], v[52:55], 0\n"
"s_setprio 1\n"
"v_exp_f32 v38, v38\n"
"v_exp_f32 v39, v39\n"
"v_exp_f32 v40, v40\n"
"v_mfma_f32_32x32x8_bf16 v[16:31], v[70:71], v[60:61], v[16:31]\n"
"v_exp_f32 v41, v41\n"
"v_exp_f32 v42, v42\n"
"v_exp_f32 v43, v43\n"
"v_exp_f32 v44, v44\n"
"v_smfmac_f32_32x32x32_bf16 v[192:207], v[76:79], v[32:39], v240\n"
"v_exp_f32 v45, v45\n"
"v_exp_f32 v46, v46\n"
"v_exp_f32 v47, v47\n"
"v_exp_f32 v16, v16\n"
"v_exp_f32 v17, v17\n"
"v_exp_f32 v18, v18\n"
"v_smfmac_f32_32x32x32_bf16 v[192:207], v[84:87], v[40:47], v240\n"
"v_exp_f32 v19, v19\n"
"v_exp_f32 v20, v20\n"
"v_exp_f32 v21, v21\n"
"v_mfma_f32_32x32x16_bf16 v[32:47], v[92:95], v[52:55], 0\n"
"s_setprio 0\n"
"v_exp_f32 v22, v22\n"
"v_exp_f32 v23, v23\n"
"v_exp_f32 v24, v24\n"
"v_mfma_f32_32x32x8_bf16 v[32:47], v[70:71], v[62:63], v[32:47]\n"
"v_exp_f32 v25, v25\n"
"v_exp_f32 v26, v26\n"
"v_exp_f32 v27, v27\n"
"v_exp_f32 v28, v28\n"
"v_smfmac_f32_32x32x32_bf16 v[208:223], v[72:75], v[16:23], v240\n"
"ds_write_b128 v244, v[192:195] offset:0\n"
"ds_write_b128 v244, v[196:199] offset:1024\n"
"ds_write_b128 v244, v[200:203] offset:2048\n"
"v_exp_f32 v29, v29\n"
"v_exp_f32 v30, v30\n"
"v_exp_f32 v31, v31\n"
"s_waitcnt vmcnt(0) lgkmcnt(0)\n"
"s_barrier\n"
"v_exp_f32 v32, v32\n"
"v_exp_f32 v33, v33\n"
"v_exp_f32 v34, v34\n"
"v_smfmac_f32_32x32x32_bf16 v[208:223], v[80:83], v[24:31], v240\n"
"ds_read_b128 v[24:27], v245 offset:2048\n"
"ds_read_b128 v[16:19], v245 offset:0\n"
"ds_read_b128 v[20:23], v245 offset:1024\n"
"s_add_u32 m0, s30, 139264\n"
"s_add_u32 s36, s36, 32768\n"
"s_addc_u32 s37, s37, 0\n"
"global_load_lds_dwordx4 v226, s[36:37] sc1\n"
"v_exp_f32 v35, v35\n"
"v_exp_f32 v36, v36\n"
"v_exp_f32 v37, v37\n"
"v_exp_f32 v38, v38\n"
"v_exp_f32 v39, v39\n"
"v_exp_f32 v40, v40\n"
"v_exp_f32 v41, v41\n"
"v_exp_f32 v42, v42\n"
"v_exp_f32 v43, v43\n"
"v_exp_f32 v44, v44\n"
"v_smfmac_f32_32x32x32_bf16 v[208:223], v[76:79], v[32:39], v240\n"
"ds_read_b128 v[192:195], v227 offset:0\n"
"ds_read_b128 v[196:199], v227 offset:1024\n"
"ds_read_b128 v[200:203], v227 offset:2048\n"
"ds_read_b128 v[204:207], v227 offset:3072\n"
"ds_read_b128 v[88:91], v227 offset:4096\n"
"s_add_u32 m0, s30, 147456\n"
"s_add_u32 s36, s36, 8192\n"
"s_addc_u32 s37, s37, 0\n"
"global_load_lds_dwordx4 v226, s[36:37] sc1\n"
"v_exp_f32 v45, v45\n"
"v_exp_f32 v46, v46\n"
"v_exp_f32 v47, v47\n"
"v_lshlrev_b32 v3, 4, v1\n"
"v_lshl_add_u32 v4, v2, 15, v3\n"
"v_lshlrev_b32 v11, 3, v1\n"
"v_smfmac_f32_32x32x32_bf16 v[208:223], v[84:87], v[40:47], v240\n"
"s_add_u32 m0, s30, 155648\n"
"s_add_u32 s36, s36, 8192\n"
"s_addc_u32 s37, s37, 0\n"
"global_load_lds_dwordx4 v226, s[36:37] sc1\n"
"s_mov_b32 s40, 0x30800000\n"
"s_mov_b32 s41, 0x71800000\n"
"s_mov_b32 s44, 0x7b800000\n"
"s_waitcnt lgkmcnt(5)\n"
"s_nop 3\n"
"v_add_f32 v8, v216, v24\n"
"v_add_f32 v9, v216, v24\n"
"v_add_f32 v216, v216, v24\n"
"v_add_f32 v217, v217, v25\n"
"v_add_f32 v218, v218, v26\n"
"v_add_f32 v219, v219, v27\n"
"v_permlane32_swap_b32 v8, v9\n"
"v_add_f32 v208, v208, v16\n"
"v_add_f32 v209, v209, v17\n"
"v_add_f32 v210, v210, v18\n"
"v_add_f32 v211, v211, v19\n"
"v_add_f32 v212, v212, v20\n"
"v_add_f32 v213, v213, v21\n"
"v_add_f32 v214, v214, v22\n"
"v_add_f32 v215, v215, v23\n"
"v_cmp_le_f32 vcc, s40, v9\n"
"v_cmp_ge_f32 s[42:43], s41, v9\n"
"v_maximum3_f32 v5, abs(v217), abs(v218), abs(v219)\n"
"v_maximum3_f32 v6, abs(v216), abs(v208), abs(v209)\n"
"s_and_b64 vcc, vcc, s[42:43]\n"
"v_maximum3_f32 v6, abs(v210), abs(v211), v6\n"
"v_maximum3_f32 v5, abs(v212), abs(v213), v5\n"
"v_maximum3_f32 v6, abs(v214), abs(v215), v6\n"
"v_maximum3_f32 v5, v5, v6, v6\n"
"v_cmp_ge_f32 s[42:43], s44, v5\n"
"s_nop 0\n"
"s_and_b64 vcc, vcc, s[42:43]\n"
"s_cmp_eq_u64 vcc, -1\n"
"s_cbranch_scc0 exact_9_%=\n"
"norm_80_%=:\n"
"v_rcp_f32 v9, v9\n"
"s_nop 0\n"
"v_mul_f32 v208, v208, v9\n"
"v_mul_f32 v209, v209, v9\n"
"v_mul_f32 v210, v210, v9\n"
"v_mul_f32 v211, v211, v9\n"
"v_mul_f32 v212, v212, v9\n"
"v_mul_f32 v213, v213, v9\n"
"v_mul_f32 v214, v214, v9\n"
"v_mul_f32 v215, v215, v9\n"
"v_mul_f32 v216, v216, v9\n"
"v_mul_f32 v217, v217, v9\n"
"v_mul_f32 v218, v218, v9\n"
"v_mul_f32 v219, v219, v9\n"
"v_cvt_pk_bf16_f32 v48, v208, v209\n"
"v_cvt_pk_bf16_f32 v49, v210, v211\n"
"v_cvt_pk_bf16_f32 v50, v212, v213\n"
"v_cvt_pk_bf16_f32 v51, v214, v215\n"
"v_cvt_pk_bf16_f32 v52, v216, v217\n"
"v_cvt_pk_bf16_f32 v53, v218, v219\n"
"v_permlane32_swap_b32 v48, v50\n"
"v_permlane32_swap_b32 v49, v51\n"
"global_store_dwordx4 v4, v[48:51], s[34:35] sc1\n"
"s_mov_b32 exec_hi, 0\n"
"global_store_dwordx2 v11, v[52:53], s[38:39] sc1\n"
"s_mov_b32 exec_hi, -1\n"
"s_waitcnt lgkmcnt(3)\n"
"v_mfma_f32_16x16x32_bf16 v[16:19], v[192:195], v[96:99], 0\n"
"ds_read_b128 v[56:59], v227 offset:10240\n"
"s_add_u32 s38, s18, s65\n"
"s_addc_u32 s39, s19, 0\n"
"s_sub_u32 s41, s71, 16777224\n"
"s_lshr_b32 s41, s41, 1\n"
"s_lshl_b32 s40, s47, 18\n"
"s_add_u32 s40, s41, s40\n"
"s_add_u32 s45, s40, 16252928\n"
"s_add_u32 s34, s18, s45\n"
"s_addc_u32 s35, s19, 0\n"
"s_add_u32 s44, s18, s70\n"
"s_addc_u32 s45, s19, 0\n"
"v_and_b32 v9, 15, v0\n"
"v_mfma_f32_16x16x32_bf16 v[20:23], v[196:199], v[96:99], 0\n"
"ds_read_b128 v[60:63], v227 offset:11264\n"
"v_lshlrev_b32 v9, 3, v9\n"
"s_waitcnt lgkmcnt(3)\n"
"v_mfma_f32_16x16x32_bf16 v[24:27], v[200:203], v[96:99], 0\n"
"ds_read_b128 v[64:67], v227 offset:12288\n"
"v_sub_u32 v9, v235, v9\n"
"v_mfma_f32_16x16x32_bf16 v[28:31], v[204:207], v[96:99], 0\n"
"ds_read_b128 v[68:71], v227 offset:13312\n"
"v_subrev_u32 v244, 90112, v244\n"
"s_waitcnt lgkmcnt(3)\n"
"v_mfma_f32_16x16x32_bf16 v[32:35], v[88:91], v[96:99], 0\n"
"ds_read_b128 v[72:75], v227 offset:14336\n"
"v_subrev_u32 v245, 90112, v245\n"
"s_barrier\n"
"v_mfma_f32_16x16x32_bf16 v[16:19], v[56:59], v[100:103], v[16:19]\n"
"ds_read_b128 v[192:195], v227 offset:20480\n"
"s_add_u32 m0, s30, 114688\n"
"s_sub_u32 s36, s36, 40960\n"
"s_subb_u32 s37, s37, 0\n"
"global_load_lds_dwordx4 v226, s[36:37] sc1\n"
"v_rcp_f32 v6, v241\n"
"s_waitcnt lgkmcnt(3)\n"
"v_mfma_f32_16x16x32_bf16 v[20:23], v[60:63], v[100:103], v[20:23]\n"
"ds_read_b128 v[196:199], v227 offset:21504\n"
"v_mul_f32 v7, 0xbfb8aa3b, v241\n"
"v_mfma_f32_16x16x32_bf16 v[24:27], v[64:67], v[100:103], v[24:27]\n"
"ds_read_b128 v[200:203], v227 offset:22528\n"
"v_mul_u32_u24 v8, 8, v0\n"
"s_waitcnt lgkmcnt(3)\n"
"v_mfma_f32_16x16x32_bf16 v[28:31], v[68:71], v[100:103], v[28:31]\n"
"ds_read_b128 v[204:207], v227 offset:23552\n"
"v_permlane16_swap_b32 v250, v252\n"
"v_mfma_f32_16x16x32_bf16 v[32:35], v[72:75], v[100:103], v[32:35]\n"
"ds_read_b128 v[88:91], v227 offset:24576\n"
"s_add_u32 m0, s30, 122880\n"
"s_add_u32 s36, s36, 8192\n"
"s_addc_u32 s37, s37, 0\n"
"global_load_lds_dwordx4 v226, s[36:37] sc1\n"
"v_permlane16_swap_b32 v251, v253\n"
"s_waitcnt lgkmcnt(3)\n"
"v_mfma_f32_16x16x32_bf16 v[16:19], v[192:195], v[104:107], v[16:19]\n"
"ds_read_b128 v[56:59], v227 offset:30720\n"
"v_lshlrev_b32 v212, 16, v250\n"
"v_mfma_f32_16x16x32_bf16 v[20:23], v[196:199], v[104:107], v[20:23]\n"
"ds_read_b128 v[60:63], v227 offset:31744\n"
"v_and_b32 v213, 0xffff0000, v250\n"
"s_waitcnt lgkmcnt(3)\n"
"v_mfma_f32_16x16x32_bf16 v[24:27], v[200:203], v[104:107], v[24:27]\n"
"ds_read_b128 v[64:67], v227 offset:32768\n"
"v_lshlrev_b32 v214, 16, v251\n"
"v_mfma_f32_16x16x32_bf16 v[28:31], v[204:207], v[104:107], v[28:31]\n"
"ds_read_b128 v[68:71], v227 offset:33792\n"
"s_add_u32 m0, s30, 131072\n"
"s_add_u32 s36, s36, 8192\n"
"s_addc_u32 s37, s37, 0\n"
"global_load_lds_dwordx4 v226, s[36:37] sc1\n"
"v_and_b32 v215, 0xffff0000, v251\n"
"s_waitcnt lgkmcnt(3)\n"
"v_mfma_f32_16x16x32_bf16 v[32:35], v[88:91], v[104:107], v[32:35]\n"
"ds_read_b128 v[72:75], v227 offset:34816\n"
"v_lshlrev_b32 v216, 16, v252\n"
"v_mfma_f32_16x16x32_bf16 v[16:19], v[56:59], v[108:111], v[16:19]\n"
"ds_read_b128 v[192:195], v227 offset:40960\n"
"v_and_b32 v217, 0xffff0000, v252\n"
"s_waitcnt lgkmcnt(3)\n"
"v_mfma_f32_16x16x32_bf16 v[20:23], v[60:63], v[108:111], v[20:23]\n"
"ds_read_b128 v[196:199], v227 offset:41984\n"
"v_lshlrev_b32 v218, 16, v253\n"
"v_mfma_f32_16x16x32_bf16 v[24:27], v[64:67], v[108:111], v[24:27]\n"
"ds_read_b128 v[200:203], v227 offset:43008\n"
"s_add_u32 m0, s30, 0\n"
"s_add_u32 s36, s36, 32768\n"
"s_addc_u32 s37, s37, 0\n"
"global_load_lds_dwordx4 v226, s[36:37] sc1\n"
"v_and_b32 v219, 0xffff0000, v253\n"
"s_waitcnt lgkmcnt(3)\n"
"v_mfma_f32_16x16x32_bf16 v[28:31], v[68:71], v[108:111], v[28:31]\n"
"ds_read_b128 v[204:207], v227 offset:44032\n"
"v_lshlrev_b32 v220, 16, v254\n"
"v_mfma_f32_16x16x32_bf16 v[32:35], v[72:75], v[108:111], v[32:35]\n"
"ds_read_b128 v[88:91], v227 offset:45056\n"
"v_and_b32 v221, 0xffff0000, v254\n"
"s_waitcnt lgkmcnt(3)\n"
"v_mfma_f32_16x16x32_bf16 v[16:19], v[192:195], v[112:115], v[16:19]\n"
"ds_read_b128 v[56:59], v227 offset:51200\n"
"v_lshlrev_b32 v222, 16, v255\n"
"v_mfma_f32_16x16x32_bf16 v[20:23], v[196:199], v[112:115], v[20:23]\n"
"ds_read_b128 v[60:63], v227 offset:52224\n"
"v_and_b32 v223, 0xffff0000, v255\n"
"s_waitcnt lgkmcnt(3)\n"
"v_mfma_f32_16x16x32_bf16 v[24:27], v[200:203], v[112:115], v[24:27]\n"
"ds_read_b128 v[64:67], v227 offset:53248\n"
"v_mfma_f32_16x16x32_bf16 v[28:31], v[204:207], v[112:115], v[28:31]\n"
"ds_read_b128 v[68:71], v227 offset:54272\n"
"s_waitcnt lgkmcnt(3)\n"
"v_mfma_f32_16x16x32_bf16 v[32:35], v[88:91], v[112:115], v[32:35]\n"
"ds_read_b128 v[72:75], v227 offset:55296\n"
"v_mfma_f32_16x16x32_bf16 v[16:19], v[56:59], v[116:119], v[16:19]\n"
"ds_read_b128 v[192:195], v227 offset:61440\n"
"s_waitcnt lgkmcnt(3)\n"
"v_mfma_f32_16x16x32_bf16 v[20:23], v[60:63], v[116:119], v[20:23]\n"
"ds_read_b128 v[196:199], v227 offset:62464\n"
"v_mfma_f32_16x16x32_bf16 v[24:27], v[64:67], v[116:119], v[24:27]\n"
"ds_read_b128 v[200:203], v227 offset:63488\n"
"s_waitcnt lgkmcnt(3)\n"
"v_mfma_f32_16x16x32_bf16 v[28:31], v[68:71], v[116:119], v[28:31]\n"
"ds_read_b128 v[204:207], v227 offset:64512\n"
"v_mfma_f32_16x16x32_bf16 v[32:35], v[72:75], v[116:119], v[32:35]\n"
"v_add_u32 v227, 61440, v227\n"
"ds_read_b128 v[88:91], v227 offset:4096\n"
"s_waitcnt vmcnt(4)\n"
"v_lshl_add_u32 v3, v0, 3, s68\n"
"s_mov_b64 exec, 0x1\n"
"global_store_dwordx2 v3, v[242:243], s[18:19] sc1\n"
"s_mov_b64 exec, -1\n"
"s_barrier\n"
"s_waitcnt lgkmcnt(3)\n"
"v_mfma_f32_16x16x32_bf16 v[16:19], v[192:195], v[120:123], v[16:19]\n"
"ds_read_b128 v[56:59], v227 offset:10240\n"
"v_mfma_f32_16x16x32_bf16 v[20:23], v[196:199], v[120:123], v[20:23]\n"
"ds_read_b128 v[60:63], v227 offset:11264\n"
"s_waitcnt lgkmcnt(3)\n"
"v_mfma_f32_16x16x32_bf16 v[24:27], v[200:203], v[120:123], v[24:27]\n"
"ds_read_b128 v[64:67], v227 offset:12288\n"
"v_mfma_f32_16x16x32_bf16 v[28:31], v[204:207], v[120:123], v[28:31]\n"
"ds_read_b128 v[68:71], v227 offset:13312\n"
"s_add_u32 m0, s30, 8192\n"
"s_add_u32 s36, s36, 8192\n"
"s_addc_u32 s37, s37, 0\n"
"global_load_lds_dwordx4 v226, s[36:37] sc1\n"
"s_waitcnt lgkmcnt(3)\n"
"v_mfma_f32_16x16x32_bf16 v[32:35], v[88:91], v[120:123], v[32:35]\n"
"ds_read_b128 v[72:75], v227 offset:14336\n"
"v_mfma_f32_16x16x32_bf16 v[16:19], v[56:59], v[124:127], v[16:19]\n"
"ds_read_b128 v[192:195], v227 offset:20480\n"
"s_waitcnt lgkmcnt(3)\n"
"v_mfma_f32_16x16x32_bf16 v[20:23], v[60:63], v[124:127], v[20:23]\n"
"ds_read_b128 v[196:199], v227 offset:21504\n"
"v_mfma_f32_16x16x32_bf16 v[24:27], v[64:67], v[124:127], v[24:27]\n"
"ds_read_b128 v[200:203], v227 offset:22528\n"
"s_add_u32 m0, s30, 16384\n"
"s_add_u32 s36, s36, 8192\n"
"s_addc_u32 s37, s37, 0\n"
"global_load_lds_dwordx4 v226, s[36:37] sc1\n"
"s_waitcnt lgkmcnt(3)\n"
"v_mfma_f32_16x16x32_bf16 v[28:31], v[68:71], v[124:127], v[28:31]\n"
"ds_read_b128 v[204:207], v227 offset:23552\n"
"v_mfma_f32_16x16x32_bf16 v[32:35], v[72:75], v[124:127], v[32:35]\n"
"ds_read_b128 v[88:91], v227 offset:24576\n"
"s_waitcnt lgkmcnt(3)\n"
"v_mfma_f32_16x16x32_bf16 v[16:19], v[192:195], v[128:131], v[16:19]\n"
"ds_read_b128 v[56:59], v227 offset:30720\n"
"v_mfma_f32_16x16x32_bf16 v[20:23], v[196:199], v[128:131], v[20:23]\n"
"ds_read_b128 v[60:63], v227 offset:31744\n"
"s_add_u32 m0, s30, 24576\n"
"s_add_u32 s36, s36, 8192\n"
"s_addc_u32 s37, s37, 0\n"
"global_load_lds_dwordx4 v226, s[36:37] sc1\n"
"s_waitcnt lgkmcnt(3)\n"
"v_mfma_f32_16x16x32_bf16 v[24:27], v[200:203], v[128:131], v[24:27]\n"
"ds_read_b128 v[64:67], v227 offset:32768\n"
"v_mfma_f32_16x16x32_bf16 v[28:31], v[204:207], v[128:131], v[28:31]\n"
"ds_read_b128 v[68:71], v227 offset:33792\n"
"s_waitcnt lgkmcnt(3)\n"
"v_mfma_f32_16x16x32_bf16 v[32:35], v[88:91], v[128:131], v[32:35]\n"
"ds_read_b128 v[72:75], v227 offset:34816\n"
"v_mfma_f32_16x16x32_bf16 v[16:19], v[56:59], v[132:135], v[16:19]\n"
"ds_read_b128 v[192:195], v227 offset:40960\n"
"s_add_u32 m0, s30, 32768\n"
"s_add_u32 s36, s36, 8192\n"
"s_addc_u32 s37, s37, 0\n"
"global_load_lds_dwordx4 v226, s[36:37] sc1\n"
"s_waitcnt lgkmcnt(3)\n"
"v_mfma_f32_16x16x32_bf16 v[20:23], v[60:63], v[132:135], v[20:23]\n"
"ds_read_b128 v[196:199], v227 offset:41984\n"
"v_mfma_f32_16x16x32_bf16 v[24:27], v[64:67], v[132:135], v[24:27]\n"
"ds_read_b128 v[200:203], v227 offset:43008\n"
"s_waitcnt lgkmcnt(3)\n"
"v_mfma_f32_16x16x32_bf16 v[28:31], v[68:71], v[132:135], v[28:31]\n"
"ds_read_b128 v[204:207], v227 offset:44032\n"
"v_mfma_f32_16x16x32_bf16 v[32:35], v[72:75], v[132:135], v[32:35]\n"
"ds_read_b128 v[88:91], v227 offset:45056\n"
"s_add_u32 m0, s30, 40960\n"
"s_add_u32 s36, s36, 8192\n"
"s_addc_u32 s37, s37, 0\n"
"global_load_lds_dwordx4 v226, s[36:37] sc1\n"
"s_waitcnt vmcnt(7)\n"
"s_barrier\n"
"s_waitcnt lgkmcnt(3)\n"
"v_mfma_f32_16x16x32_bf16 v[16:19], v[192:195], v[136:139], v[16:19]\n"
"ds_read_b128 v[56:59], v227 offset:51200\n"
"v_mfma_f32_16x16x32_bf16 v[20:23], v[196:199], v[136:139], v[20:23]\n"
"ds_read_b128 v[60:63], v227 offset:52224\n"
"s_waitcnt lgkmcnt(3)\n"
"v_mfma_f32_16x16x32_bf16 v[24:27], v[200:203], v[136:139], v[24:27]\n"
"ds_read_b128 v[64:67], v227 offset:53248\n"
"v_mfma_f32_16x16x32_bf16 v[28:31], v[204:207], v[136:139], v[28:31]\n"
"ds_read_b128 v[68:71], v227 offset:54272\n"
"s_add_u32 m0, s30, 49152\n"
"s_add_u32 s36, s36, 8192\n"
"s_addc_u32 s37, s37, 0\n"
"global_load_lds_dwordx4 v226, s[36:37] sc1\n"
"s_waitcnt lgkmcnt(3)\n"
"v_mfma_f32_16x16x32_bf16 v[32:35], v[88:91], v[136:139], v[32:35]\n"
"ds_read_b128 v[72:75], v227 offset:55296\n"
"v_mfma_f32_16x16x32_bf16 v[16:19], v[56:59], v[140:143], v[16:19]\n"
"ds_read_b128 v[192:195], v227 offset:61440\n"
"s_waitcnt lgkmcnt(3)\n"
"v_mfma_f32_16x16x32_bf16 v[20:23], v[60:63], v[140:143], v[20:23]\n"
"ds_read_b128 v[196:199], v227 offset:62464\n"
"v_mfma_f32_16x16x32_bf16 v[24:27], v[64:67], v[140:143], v[24:27]\n"
"ds_read_b128 v[200:203], v227 offset:63488\n"
"s_add_u32 m0, s30, 57344\n"
"s_add_u32 s36, s36, 8192\n"
"s_addc_u32 s37, s37, 0\n"
"global_load_lds_dwordx4 v226, s[36:37] sc1\n"
"s_waitcnt lgkmcnt(3)\n"
"v_mfma_f32_16x16x32_bf16 v[28:31], v[68:71], v[140:143], v[28:31]\n"
"ds_read_b128 v[204:207], v227 offset:64512\n"
"v_mfma_f32_16x16x32_bf16 v[32:35], v[72:75], v[140:143], v[32:35]\n"
"v_add_u32 v227, 61440, v227\n"
"ds_read_b128 v[88:91], v227 offset:4096\n"
"s_waitcnt lgkmcnt(3)\n"
"v_mfma_f32_16x16x32_bf16 v[16:19], v[192:195], v[144:147], v[16:19]\n"
"ds_read_b128 v[56:59], v227 offset:10240\n"
"v_mfma_f32_16x16x32_bf16 v[20:23], v[196:199], v[144:147], v[20:23]\n"
"ds_read_b128 v[60:63], v227 offset:11264\n"
"s_add_u32 m0, s30, 65536\n"
"s_add_u32 s36, s36, 8192\n"
"s_addc_u32 s37, s37, 0\n"
"global_load_lds_dwordx4 v226, s[36:37] sc1\n"
"s_waitcnt lgkmcnt(3)\n"
"v_mfma_f32_16x16x32_bf16 v[24:27], v[200:203], v[144:147], v[24:27]\n"
"ds_read_b128 v[64:67], v227 offset:12288\n"
"v_mfma_f32_16x16x32_bf16 v[28:31], v[204:207], v[144:147], v[28:31]\n"
"ds_read_b128 v[68:71], v227 offset:13312\n"
"s_waitcnt lgkmcnt(3)\n"
"v_mfma_f32_16x16x32_bf16 v[32:35], v[88:91], v[144:147], v[32:35]\n"
"ds_read_b128 v[72:75], v227 offset:14336\n"
"v_mfma_f32_16x16x32_bf16 v[16:19], v[56:59], v[148:151], v[16:19]\n"
"ds_read_b128 v[192:195], v227 offset:20480\n"
"s_waitcnt lgkmcnt(3)\n"
"v_mfma_f32_16x16x32_bf16 v[20:23], v[60:63], v[148:151], v[20:23]\n"
"ds_read_b128 v[196:199], v227 offset:21504\n"
"v_mfma_f32_16x16x32_bf16 v[24:27], v[64:67], v[148:151], v[24:27]\n"
"ds_read_b128 v[200:203], v227 offset:22528\n"
"s_add_u32 m0, s30, 73728\n"
"s_add_u32 s36, s36, 8192\n"
"s_addc_u32 s37, s37, 0\n"
"global_load_lds_dwordx4 v226, s[36:37] sc1\n"
"s_waitcnt lgkmcnt(3)\n"
"v_mfma_f32_16x16x32_bf16 v[28:31], v[68:71], v[148:151], v[28:31]\n"
"ds_read_b128 v[204:207], v227 offset:23552\n"
"v_mfma_f32_16x16x32_bf16 v[32:35], v[72:75], v[148:151], v[32:35]\n"
"ds_read_b128 v[88:91], v227 offset:24576\n"
"s_waitcnt lgkmcnt(3)\n"
"v_mfma_f32_16x16x32_bf16 v[16:19], v[192:195], v[152:155], v[16:19]\n"
"ds_read_b128 v[56:59], v227 offset:30720\n"
"s_add_u32 m0, s30, 81920\n"
"s_add_u32 s36, s36, 8192\n"
"s_addc_u32 s37, s37, 0\n"
"global_load_lds_dwordx4 v226, s[36:37] sc1\n"
"v_mfma_f32_16x16x32_bf16 v[20:23], v[196:199], v[152:155], v[20:23]\n"
"ds_read_b128 v[60:63], v227 offset:31744\n"
"s_waitcnt lgkmcnt(3)\n"
"v_mfma_f32_16x16x32_bf16 v[24:27], v[200:203], v[152:155], v[24:27]\n"
"ds_read_b128 v[64:67], v227 offset:32768\n"
"v_mfma_f32_16x16x32_bf16 v[28:31], v[204:207], v[152:155], v[28:31]\n"
"ds_read_b128 v[68:71], v227 offset:33792\n"
"s_add_u32 m0, s30, 90112\n"
"s_add_u32 s36, s36, 8192\n"
"s_addc_u32 s37, s37, 0\n"
"global_load_lds_dwordx4 v226, s[36:37] sc1\n"
"s_waitcnt lgkmcnt(3)\n"
"v_mfma_f32_16x16x32_bf16 v[32:35], v[88:91], v[152:155], v[32:35]\n"
"ds_read_b128 v[72:75], v227 offset:34816\n"
"s_waitcnt vmcnt(7)\n"
"s_barrier\n"
"v_mfma_f32_16x16x32_bf16 v[16:19], v[56:59], v[156:159], v[16:19]\n"
"v_subrev_u32 v227, 122880, v227\n"
"ds_read_b128 v[192:195], v227 offset:0\n"
"s_waitcnt lgkmcnt(3)\n"
"v_mfma_f32_16x16x32_bf16 v[20:23], v[60:63], v[156:159], v[20:23]\n"
"ds_read_b128 v[196:199], v227 offset:1024\n"
"s_add_u32 m0, s30, 98304\n"
"s_add_u32 s36, s36, 8192\n"
"s_addc_u32 s37, s37, 0\n"
"global_load_lds_dwordx4 v226, s[36:37] sc1\n"
"v_mfma_f32_16x16x32_bf16 v[24:27], v[64:67], v[156:159], v[24:27]\n"
"ds_read_b128 v[200:203], v227 offset:2048\n"
"s_waitcnt lgkmcnt(3)\n"
"v_mfma_f32_16x16x32_bf16 v[28:31], v[68:71], v[156:159], v[28:31]\n"
"ds_read_b128 v[204:207], v227 offset:3072\n"
"v_mfma_f32_16x16x32_bf16 v[32:35], v[72:75], v[156:159], v[32:35]\n"
"ds_read_b128 v[88:91], v227 offset:4096\n"
"s_add_u32 m0, s30, 106496\n"
"s_add_u32 s36, s36, 8192\n"
"s_addc_u32 s37, s37, 0\n"
"global_load_lds_dwordx4 v226, s[36:37] sc1\n"
"s_waitcnt lgkmcnt(3)\n"
"v_mfma_f32_16x16x32_bf16 v[16:19], v[192:195], v[160:163], v[16:19]\n"
"ds_read_b128 v[56:59], v227 offset:10240\n"
"v_mfma_f32_16x16x32_bf16 v[20:23], v[196:199], v[160:163], v[20:23]\n"
"ds_read_b128 v[60:63], v227 offset:11264\n"
"s_waitcnt lgkmcnt(3)\n"
"v_mfma_f32_16x16x32_bf16 v[24:27], v[200:203], v[160:163], v[24:27]\n"
"ds_read_b128 v[64:67], v227 offset:12288\n"
"s_add_u32 m0, s30, 114688\n"
"s_add_u32 s36, s36, 8192\n"
"s_addc_u32 s37, s37, 0\n"
"global_load_lds_dwordx4 v226, s[36:37] sc1\n"
"v_mfma_f32_16x16x32_bf16 v[28:31], v[204:207], v[160:163], v[28:31]\n"
"ds_read_b128 v[68:71], v227 offset:13312\n"
"s_waitcnt lgkmcnt(3)\n"
"v_mfma_f32_16x16x32_bf16 v[32:35], v[88:91], v[160:163], v[32:35]\n"
"ds_read_b128 v[72:75], v227 offset:14336\n"
"v_mfma_f32_16x16x32_bf16 v[16:19], v[56:59], v[164:167], v[16:19]\n"
"ds_read_b128 v[192:195], v227 offset:20480\n"
"s_add_u32 m0, s30, 122880\n"
"s_add_u32 s36, s36, 8192\n"
"s_addc_u32 s37, s37, 0\n"
"global_load_lds_dwordx4 v226, s[36:37] sc1\n"
"s_waitcnt lgkmcnt(3)\n"
"v_mfma_f32_16x16x32_bf16 v[20:23], v[60:63], v[164:167], v[20:23]\n"
"ds_read_b128 v[196:199], v227 offset:21504\n"
"v_mfma_f32_16x16x32_bf16 v[24:27], v[64:67], v[164:167], v[24:27]\n"
"ds_read_b128 v[200:203], v227 offset:22528\n"
"s_waitcnt lgkmcnt(3)\n"
"v_mfma_f32_16x16x32_bf16 v[28:31], v[68:71], v[164:167], v[28:31]\n"
"ds_read_b128 v[204:207], v227 offset:23552\n"
"s_add_u32 m0, s30, 131072\n"
"s_add_u32 s36, s36, 8192\n"
"s_addc_u32 s37, s37, 0\n"
"global_load_lds_dwordx4 v226, s[36:37] sc1\n"
"v_mfma_f32_16x16x32_bf16 v[32:35], v[72:75], v[164:167], v[32:35]\n"
"ds_read_b128 v[88:91], v227 offset:24576\n"
"s_waitcnt lgkmcnt(3)\n"
"v_mfma_f32_16x16x32_bf16 v[16:19], v[192:195], v[168:171], v[16:19]\n"
"ds_read_b128 v[56:59], v227 offset:30720\n"
"v_mfma_f32_16x16x32_bf16 v[20:23], v[196:199], v[168:171], v[20:23]\n"
"ds_read_b128 v[60:63], v227 offset:31744\n"
"s_add_u32 m0, s30, 139264\n"
"s_add_u32 s36, s36, 8192\n"
"s_addc_u32 s37, s37, 0\n"
"global_load_lds_dwordx4 v226, s[36:37] sc1\n"
"s_waitcnt lgkmcnt(3)\n"
"v_mfma_f32_16x16x32_bf16 v[24:27], v[200:203], v[168:171], v[24:27]\n"
"ds_read_b128 v[64:67], v227 offset:32768\n"
"v_mfma_f32_16x16x32_bf16 v[28:31], v[204:207], v[168:171], v[28:31]\n"
"ds_read_b128 v[68:71], v227 offset:33792\n"
"s_waitcnt lgkmcnt(3)\n"
"v_mfma_f32_16x16x32_bf16 v[32:35], v[88:91], v[168:171], v[32:35]\n"
"ds_read_b128 v[72:75], v227 offset:34816\n"
"s_waitcnt vmcnt(9)\n"
"s_barrier\n"
"v_mfma_f32_16x16x32_bf16 v[16:19], v[56:59], v[172:175], v[16:19]\n"
"ds_read_b128 v[192:195], v227 offset:40960\n"
"s_waitcnt lgkmcnt(3)\n"
"v_mfma_f32_16x16x32_bf16 v[20:23], v[60:63], v[172:175], v[20:23]\n"
"ds_read_b128 v[196:199], v227 offset:41984\n"
"v_mfma_f32_16x16x32_bf16 v[24:27], v[64:67], v[172:175], v[24:27]\n"
"ds_read_b128 v[200:203], v227 offset:43008\n"
"s_add_u32 m0, s30, 147456\n"
"s_add_u32 s36, s36, 8192\n"
"s_addc_u32 s37, s37, 0\n"
"global_load_lds_dwordx4 v226, s[36:37] sc1\n"
"s_waitcnt lgkmcnt(3)\n"
"v_mfma_f32_16x16x32_bf16 v[28:31], v[68:71], v[172:175], v[28:31]\n"
"ds_read_b128 v[204:207], v227 offset:44032\n"
"v_mfma_f32_16x16x32_bf16 v[32:35], v[72:75], v[172:175], v[32:35]\n"
"ds_read_b128 v[88:91], v227 offset:45056\n"
"s_waitcnt lgkmcnt(3)\n"
"v_mfma_f32_16x16x32_bf16 v[16:19], v[192:195], v[176:179], v[16:19]\n"
"ds_read_b128 v[56:59], v227 offset:51200\n"
"s_cmp_lg_u32 s23, 0\n"
"s_cbranch_scc1 onew_82_%=\n"
"global_load_dwordx2 v[246:247], v8, s[44:45] sc1\n"
"onew_82_%=:\n"
"v_mfma_f32_16x16x32_bf16 v[20:23], v[196:199], v[176:179], v[20:23]\n"
"ds_read_b128 v[60:63], v227 offset:52224\n"
"s_waitcnt lgkmcnt(3)\n"
"v_mfma_f32_16x16x32_bf16 v[24:27], v[200:203], v[176:179], v[24:27]\n"
"ds_read_b128 v[64:67], v227 offset:53248\n"
"v_mfma_f32_16x16x32_bf16 v[28:31], v[204:207], v[176:179], v[28:31]\n"
"ds_read_b128 v[68:71], v227 offset:54272\n"
"s_add_u32 m0, s30, 155648\n"
"s_add_u32 s36, s36, 8192\n"
"s_addc_u32 s37, s37, 0\n"
"global_load_lds_dwordx4 v226, s[36:37] sc1\n"
"s_waitcnt lgkmcnt(3)\n"
"v_mfma_f32_16x16x32_bf16 v[32:35], v[88:91], v[176:179], v[32:35]\n"
"ds_read_b128 v[72:75], v227 offset:55296\n"
"v_mfma_f32_16x16x32_bf16 v[16:19], v[56:59], v[180:183], v[16:19]\n"
"ds_read_b128 v[192:195], v227 offset:61440\n"
"s_waitcnt lgkmcnt(3)\n"
"v_mfma_f32_16x16x32_bf16 v[20:23], v[60:63], v[180:183], v[20:23]\n"
"ds_read_b128 v[196:199], v227 offset:62464\n"
"s_add_u32 m0, s30, 0\n"
"s_add_u32 s36, s36, 8192\n"
"s_addc_u32 s37, s37, 0\n"
"global_load_lds_dwordx4 v226, s[36:37] sc1\n"
"v_mfma_f32_16x16x32_bf16 v[24:27], v[64:67], v[180:183], v[24:27]\n"
"ds_read_b128 v[200:203], v227 offset:63488\n"
"s_waitcnt lgkmcnt(3)\n"
"v_mfma_f32_16x16x32_bf16 v[28:31], v[68:71], v[180:183], v[28:31]\n"
"ds_read_b128 v[204:207], v227 offset:64512\n"
"v_mfma_f32_16x16x32_bf16 v[32:35], v[72:75], v[180:183], v[32:35]\n"
"v_add_u32 v227, 61440, v227\n"
"ds_read_b128 v[88:91], v227 offset:4096\n"
"s_add_u32 m0, s30, 8192\n"
"s_add_u32 s36, s36, 8192\n"
"s_addc_u32 s37, s37, 0\n"
"global_load_lds_dwordx4 v226, s[36:37] sc1\n"
"s_cmp_lg_u32 s23, 0\n"
"s_cbranch_scc1 onew_83_%=\n"
"s_waitcnt vmcnt(3)\n"
"v_cmp_ne_u64 vcc, s[24:25], v[246:247]\n"
"s_nop 0\n"
"s_cmp_eq_u64 vcc, 0\n"
"s_cbranch_scc0 retry_84_%=\n"
"ok_85_%=:\n"
"onew_83_%=:\n"
"s_waitcnt vmcnt(9)\n"
"s_barrier\n"
"s_waitcnt lgkmcnt(3)\n"
"v_mfma_f32_16x16x32_bf16 v[16:19], v[192:195], v[184:187], v[16:19]\n"
"ds_read_b128 v[56:59], v227 offset:10240\n"
"v_mfma_f32_16x16x32_bf16 v[20:23], v[196:199], v[184:187], v[20:23]\n"
"ds_read_b128 v[60:63], v227 offset:11264\n"
"s_waitcnt lgkmcnt(3)\n"
"v_mfma_f32_16x16x32_bf16 v[24:27], v[200:203], v[184:187], v[24:27]\n"
"ds_read_b128 v[64:67], v227 offset:12288\n"
"global_load_dwordx4 v[96:99], v235, s[38:39] sc1\n"
"s_add_u32 s38, s38, 131072\n"
"s_addc_u32 s39, s39, 0\n"
"v_mfma_f32_16x16x32_bf16 v[28:31], v[204:207], v[184:187], v[28:31]\n"
"ds_read_b128 v[68:71], v227 offset:13312\n"
"s_waitcnt lgkmcnt(3)\n"
"v_mfma_f32_16x16x32_bf16 v[32:35], v[88:91], v[184:187], v[32:35]\n"
"ds_read_b128 v[72:75], v227 offset:14336\n"
"v_mfma_f32_16x16x32_bf16 v[16:19], v[56:59], v[188:191], v[16:19]\n"
"ds_read_b128 v[192:195], v227 offset:22528\n"
"global_load_dwordx4 v[100:103], v235, s[38:39] sc1\n"
"s_add_u32 s38, s38, 131072\n"
"s_addc_u32 s39, s39, 0\n"
"s_waitcnt lgkmcnt(3)\n"
"v_mfma_f32_16x16x32_bf16 v[20:23], v[60:63], v[188:191], v[20:23]\n"
"ds_read_b128 v[196:199], v227 offset:23552\n"
"v_mfma_f32_16x16x32_bf16 v[24:27], v[64:67], v[188:191], v[24:27]\n"
"ds_read_b128 v[200:203], v227 offset:24576\n"
"s_waitcnt lgkmcnt(3)\n"
"v_mfma_f32_16x16x32_bf16 v[28:31], v[68:71], v[188:191], v[28:31]\n"
"ds_read_b128 v[204:207], v227 offset:32768\n"
"global_load_dwordx4 v[104:107], v235, s[38:39] sc1\n"
"s_add_u32 s38, s38, 131072\n"
"s_addc_u32 s39, s39, 0\n"
"v_mfma_f32_16x16x32_bf16 v[32:35], v[72:75], v[188:191], v[32:35]\n"
"ds_read_b128 v[88:91], v227 offset:33792\n"
"ds_write_b128 v244, v[24:27] offset:0\n"
"s_nop 0\n"
"ds_write_b128 v244, v[28:31] offset:1024\n"
"s_nop 3\n"
"ds_write_b128 v244, v[32:35] offset:2048\n"
"s_waitcnt vmcnt(10)\n"
"s_waitcnt lgkmcnt(0)\n"
"s_barrier\n"
"ds_read_b128 v[76:79], v245 offset:2048\n"
"ds_read_b128 v[80:83], v245 offset:1024\n"
"ds_read_b128 v[84:87], v245 offset:0\n"
"global_load_dwordx4 v[108:111], v235, s[38:39] sc1\n"
"s_add_u32 s38, s38, 131072\n"
"s_addc_u32 s39, s39, 0\n"
"s_waitcnt lgkmcnt(0)\n"
"v_add_f32 v16, v16, v76\n"
"v_add_f32 v17, v17, v77\n"
"v_add_f32 v18, v18, v78\n"
"v_add_f32 v19, v19, v79\n"
"v_add_f32 v20, v20, v80\n"
"v_add_f32 v21, v21, v81\n"
"v_add_f32 v22, v22, v82\n"
"v_add_f32 v23, v23, v83\n"
"v_add_f32 v24, v24, v84\n"
"v_add_f32 v25, v25, v85\n"
"v_add_f32 v26, v26, v86\n"
"v_add_f32 v27, v27, v87\n"
"global_load_dwordx4 v[112:115], v235, s[38:39] sc1\n"
"s_add_u32 s38, s38, 131072\n"
"s_addc_u32 s39, s39, 0\n"
"v_mul_f32 v76, v7, v16\n"
"v_mul_f32 v77, v7, v17\n"
"v_mul_f32 v78, v7, v18\n"
"v_mul_f32 v79, v7, v19\n"
"v_mul_f32 v80, v7, v20\n"
"v_mul_f32 v81, v7, v21\n"
"v_mul_f32 v82, v7, v22\n"
"v_mul_f32 v83, v7, v23\n"
"v_mul_f32 v84, v7, v24\n"
"v_mul_f32 v85, v7, v25\n"
"v_mul_f32 v86, v7, v26\n"
"v_mul_f32 v87, v7, v27\n"
"s_add_u32 m0, s30, 16384\n"
"s_add_u32 s36, s36, 8192\n"
"s_addc_u32 s37, s37, 0\n"
"global_load_lds_dwordx4 v226, s[36:37] sc1\n"
"v_exp_f32 v76, v76\n"
"v_exp_f32 v77, v77\n"
"v_exp_f32 v78, v78\n"
"v_exp_f32 v79, v79\n"
"v_exp_f32 v80, v80\n"
"v_exp_f32 v81, v81\n"
"v_exp_f32 v82, v82\n"
"v_exp_f32 v83, v83\n"
"v_exp_f32 v84, v84\n"
"v_exp_f32 v85, v85\n"
"v_exp_f32 v86, v86\n"
"v_exp_f32 v87, v87\n"
"global_load_dwordx4 v[116:119], v235, s[38:39] sc1\n"
"s_add_u32 s38, s38, 131072\n"
"s_addc_u32 s39, s39, 0\n"
"s_waitcnt vmcnt(6)\n"
"v_mfma_f32_16x16x32_bf16 v[44:47], v[192:195], v[96:99], 0\n"
"ds_read_b128 v[56:59], v227 offset:34816\n"
"v_fma_f32 v76, v76, v6, v6\n"
"v_mfma_f32_16x16x32_bf16 v[48:51], v[196:199], v[96:99], 0\n"
"ds_read_b128 v[60:63], v227 offset:43008\n"
"global_load_dwordx4 v[120:123], v235, s[38:39] sc1\n"
"s_add_u32 s38, s38, 131072\n"
"s_addc_u32 s39, s39, 0\n"
"v_fma_f32 v77, v77, v6, v6\n"
"v_mfma_f32_16x16x32_bf16 v[52:55], v[200:203], v[96:99], 0\n"
"ds_read_b128 v[64:67], v227 offset:44032\n"
"v_fma_f32 v78, v78, v6, v6\n"
"s_waitcnt vmcnt(6)\n"
"v_mfma_f32_16x16x32_bf16 v[44:47], v[204:207], v[100:103], v[44:47]\n"
"ds_read_b128 v[68:71], v227 offset:45056\n"
"v_fma_f32 v79, v79, v6, v6\n"
"v_mfma_f32_16x16x32_bf16 v[48:51], v[88:91], v[100:103], v[48:51]\n"
"ds_read_b128 v[72:75], v227 offset:53248\n"
"global_load_dwordx4 v[124:127], v235, s[38:39] sc1\n"
"v_fma_f32 v80, v80, v6, v6\n"
"s_waitcnt lgkmcnt(3)\n"
"v_mfma_f32_16x16x32_bf16 v[52:55], v[56:59], v[100:103], v[52:55]\n"
"ds_read_b128 v[192:195], v227 offset:54272\n"
"v_fma_f32 v81, v81, v6, v6\n"
"s_waitcnt vmcnt(13)\n"
"s_barrier\n"
"s_waitcnt vmcnt(6)\n"
"v_mfma_f32_16x16x32_bf16 v[44:47], v[60:63], v[104:107], v[44:47]\n"
"ds_read_b128 v[196:199], v227 offset:55296\n"
"v_fma_f32 v82, v82, v6, v6\n"
"s_waitcnt lgkmcnt(3)\n"
"v_mfma_f32_16x16x32_bf16 v[48:51], v[64:67], v[104:107], v[48:51]\n"
"ds_read_b128 v[200:203], v227 offset:63488\n"
"s_mov_b64 s[38:39], s[34:35]\n"
"global_load_dwordx2 v[128:129], v9, s[38:39] sc1\n"
"s_add_u32 s38, s34, 16384\n"
"s_addc_u32 s39, s35, 0\n"
"global_load_dwordx2 v[130:131], v9, s[38:39] sc1\n"
"v_fma_f32 v83, v83, v6, v6\n"
"v_mfma_f32_16x16x32_bf16 v[52:55], v[68:71], v[104:107], v[52:55]\n"
"ds_read_b128 v[204:207], v227 offset:64512\n"
"v_fma_f32 v84, v84, v6, v6\n"
"s_waitcnt vmcnt(7)\n"
"s_waitcnt lgkmcnt(3)\n"
"v_mfma_f32_16x16x32_bf16 v[44:47], v[72:75], v[108:111], v[44:47]\n"
"v_add_u32 v227, 61440, v227\n"
"ds_read_b128 v[88:91], v227 offset:4096\n"
"v_fma_f32 v85, v85, v6, v6\n"
"v_mfma_f32_16x16x32_bf16 v[48:51], v[192:195], v[108:111], v[48:51]\n"
"ds_read_b128 v[56:59], v227 offset:12288\n"
"s_add_u32 s38, s34, 131072\n"
"s_addc_u32 s39, s35, 0\n"
"global_load_dwordx2 v[132:133], v9, s[38:39] sc1\n"
"s_add_u32 s38, s34, 147456\n"
"s_addc_u32 s39, s35, 0\n"
"global_load_dwordx2 v[134:135], v9, s[38:39] sc1\n"
"v_fma_f32 v86, v86, v6, v6\n"
"s_waitcnt lgkmcnt(3)\n"
"v_mfma_f32_16x16x32_bf16 v[52:55], v[196:199], v[108:111], v[52:55]\n"
"ds_read_b128 v[60:63], v227 offset:13312\n"
"v_fma_f32 v87, v87, v6, v6\n"
"s_waitcnt vmcnt(7)\n"
"s_barrier\n"
"s_waitcnt vmcnt(8)\n"
"v_mfma_f32_16x16x32_bf16 v[44:47], v[200:203], v[112:115], v[44:47]\n"
"ds_read_b128 v[64:67], v227 offset:14336\n"
"v_rcp_f32 v76, v76\n"
"s_waitcnt lgkmcnt(3)\n"
"v_mfma_f32_16x16x32_bf16 v[48:51], v[204:207], v[112:115], v[48:51]\n"
"ds_read_b128 v[68:71], v227 offset:22528\n"
"v_rcp_f32 v77, v77\n"
"v_mfma_f32_16x16x32_bf16 v[52:55], v[88:91], v[112:115], v[52:55]\n"
"ds_read_b128 v[72:75], v227 offset:23552\n"
"v_rcp_f32 v78, v78\n"
"s_waitcnt vmcnt(6)\n"
"s_waitcnt lgkmcnt(3)\n"
"v_mfma_f32_16x16x32_bf16 v[44:47], v[56:59], v[116:119], v[44:47]\n"
"ds_read_b128 v[192:195], v227 offset:24576\n"
"v_rcp_f32 v79, v79\n"
"v_mfma_f32_16x16x32_bf16 v[48:51], v[60:63], v[116:119], v[48:51]\n"
"ds_read_b128 v[196:199], v227 offset:32768\n"
"v_rcp_f32 v80, v80\n"
"s_waitcnt lgkmcnt(3)\n"
"v_mfma_f32_16x16x32_bf16 v[52:55], v[64:67], v[116:119], v[52:55]\n"
"ds_read_b128 v[200:203], v227 offset:33792\n"
"v_rcp_f32 v81, v81\n"
"s_waitcnt vmcnt(5)\n"
"v_mfma_f32_16x16x32_bf16 v[44:47], v[68:71], v[120:123], v[44:47]\n"
"ds_read_b128 v[204:207], v227 offset:34816\n"
"v_rcp_f32 v82, v82\n"
"s_waitcnt lgkmcnt(3)\n"
"v_mfma_f32_16x16x32_bf16 v[48:51], v[72:75], v[120:123], v[48:51]\n"
"v_subrev_u32 v227, 122880, v227\n"
"ds_read_b128 v[88:91], v227 offset:2048\n"
"v_rcp_f32 v83, v83\n"
"v_mfma_f32_16x16x32_bf16 v[52:55], v[192:195], v[120:123], v[52:55]\n"
"ds_read_b128 v[56:59], v227 offset:3072\n"
"v_rcp_f32 v84, v84\n"
"s_waitcnt vmcnt(4)\n"
"s_waitcnt lgkmcnt(3)\n"
"v_mfma_f32_16x16x32_bf16 v[44:47], v[196:199], v[124:127], v[44:47]\n"
"ds_read_b128 v[60:63], v227 offset:4096\n"
"v_rcp_f32 v85, v85\n"
"v_mfma_f32_16x16x32_bf16 v[48:51], v[200:203], v[124:127], v[48:51]\n"
"ds_read_b128 v[64:67], v227 offset:12288\n"
"v_rcp_f32 v86, v86\n"
"s_waitcnt lgkmcnt(3)\n"
"v_mfma_f32_16x16x32_bf16 v[52:55], v[204:207], v[124:127], v[52:55]\n"
"ds_read_b128 v[68:71], v227 offset:13312\n"
"v_rcp_f32 v87, v87\n"
"s_waitcnt vmcnt(2)\n"
"v_mfma_f32_16x16x32_bf16 v[44:47], v[88:91], v[128:131], v[44:47]\n"
"ds_read_b128 v[72:75], v227 offset:14336\n"
"v_mul_f32 v16, v16, v76\n"
"s_waitcnt lgkmcnt(3)\n"
"v_mfma_f32_16x16x32_bf16 v[48:51], v[56:59], v[128:131], v[48:51]\n"
"v_add_u32 v227, 81920, v227\n"
"ds_read_b128 v[192:195], v227 offset:0\n"
"v_mul_f32 v17, v17, v77\n"
"v_mfma_f32_16x16x32_bf16 v[52:55], v[60:63], v[128:131], v[52:55]\n"
"ds_read_b128 v[196:199], v227 offset:1024\n"
"v_mul_f32 v18, v18, v78\n"
"s_waitcnt vmcnt(0)\n"
"s_waitcnt lgkmcnt(3)\n"
"v_mfma_f32_16x16x32_bf16 v[44:47], v[64:67], v[132:135], v[44:47]\n"
"ds_read_b128 v[200:203], v227 offset:10240\n"
"v_mul_f32 v19, v19, v79\n"
"v_mfma_f32_16x16x32_bf16 v[48:51], v[68:71], v[132:135], v[48:51]\n"
"ds_read_b128 v[204:207], v227 offset:11264\n"
"v_mul_f32 v20, v20, v80\n"
"s_waitcnt lgkmcnt(3)\n"
"v_mfma_f32_16x16x32_bf16 v[52:55], v[72:75], v[132:135], v[52:55]\n"
"ds_read_b128 v[88:91], v227 offset:20480\n"
"v_mul_f32 v21, v21, v81\n"
"v_mfma_f32_16x16x32_bf16 v[36:39], v[192:195], v[96:99], 0\n"
"ds_read_b128 v[56:59], v227 offset:21504\n"
"v_mul_f32 v22, v22, v82\n"
"s_waitcnt lgkmcnt(3)\n"
"v_mfma_f32_16x16x32_bf16 v[40:43], v[196:199], v[96:99], 0\n"
"ds_read_b128 v[60:63], v227 offset:30720\n"
"v_mul_f32 v23, v23, v83\n"
"v_mfma_f32_16x16x32_bf16 v[36:39], v[200:203], v[100:103], v[36:39]\n"
"ds_read_b128 v[64:67], v227 offset:31744\n"
"v_mul_f32 v24, v24, v84\n"
"ds_write_b128 v244, v[44:47] offset:24576\n"
"ds_write_b128 v244, v[48:51] offset:25600\n"
"ds_write_b128 v244, v[52:55] offset:26624\n"
"s_waitcnt lgkmcnt(6)\n"
"v_mfma_f32_16x16x32_bf16 v[40:43], v[204:207], v[100:103], v[40:43]\n"
"ds_read_b128 v[68:71], v227 offset:40960\n"
"v_mul_f32 v25, v25, v85\n"
"v_mfma_f32_16x16x32_bf16 v[36:39], v[88:91], v[104:107], v[36:39]\n"
"ds_read_b128 v[72:75], v227 offset:41984\n"
"v_mul_f32 v26, v26, v86\n"
"s_waitcnt lgkmcnt(6)\n"
"v_mfma_f32_16x16x32_bf16 v[40:43], v[56:59], v[104:107], v[40:43]\n"
"ds_read_b128 v[192:195], v227 offset:51200\n"
"v_mul_f32 v27, v27, v87\n"
"v_mfma_f32_16x16x32_bf16 v[36:39], v[60:63], v[108:111], v[36:39]\n"
"ds_read_b128 v[196:199], v227 offset:52224\n"
"s_waitcnt lgkmcnt(3)\n"
"v_mfma_f32_16x16x32_bf16 v[40:43], v[64:67], v[108:111], v[40:43]\n"
"ds_read_b128 v[200:203], v227 offset:61440\n"
"s_barrier\n"
"ds_read_b128 v[140:143], v245 offset:26624\n"
"ds_read_b128 v[144:147], v245 offset:25600\n"
"ds_read_b128 v[148:151], v245 offset:24576\n"
"s_or_b32 s40, s22, s23\n"
"s_cbranch_scc1 noep_86_%=\n"
"v_mov_b32 v3, 16924672\n"
"s_mov_b64 exec, 1\n"
"global_store_dwordx2 v3, v[242:243], s[18:19] sc1\n"
"s_mov_b64 exec, -1\n"
"noep_86_%=:\n"
"v_mfma_f32_16x16x32_bf16 v[36:39], v[68:71], v[112:115], v[36:39]\n"
"ds_read_b128 v[204:207], v227 offset:62464\n"
"s_waitcnt lgkmcnt(6)\n"
"v_mfma_f32_16x16x32_bf16 v[40:43], v[72:75], v[112:115], v[40:43]\n"
"v_add_u32 v227, 71680, v227\n"
"ds_read_b128 v[88:91], v227 offset:0\n"
"v_mfma_f32_16x16x32_bf16 v[36:39], v[192:195], v[116:119], v[36:39]\n"
"ds_read_b128 v[56:59], v227 offset:1024\n"
"s_waitcnt lgkmcnt(6)\n"
"v_mfma_f32_16x16x32_bf16 v[40:43], v[196:199], v[116:119], v[40:43]\n"
"v_subrev_u32 v227, 153600, v227\n"
"ds_read_b128 v[60:63], v227 offset:0\n"
"v_mfma_f32_16x16x32_bf16 v[36:39], v[200:203], v[120:123], v[36:39]\n"
"ds_read_b128 v[64:67], v227 offset:1024\n"
"s_waitcnt lgkmcnt(5)\n"
"s_waitcnt lgkmcnt(3)\n"
"v_mfma_f32_16x16x32_bf16 v[40:43], v[204:207], v[120:123], v[40:43]\n"
"ds_read_b128 v[68:71], v227 offset:10240\n"
"v_fmac_f32 v220, v44, v24\n"
"v_fmac_f32 v221, v45, v25\n"
"v_fmac_f32 v222, v46, v26\n"
"v_mfma_f32_16x16x32_bf16 v[36:39], v[88:91], v[124:127], v[36:39]\n"
"ds_read_b128 v[72:75], v227 offset:11264\n"
"v_fmac_f32 v223, v47, v27\n"
"v_fmac_f32 v220, v148, v24\n"
"v_fmac_f32 v221, v149, v25\n"
"s_waitcnt lgkmcnt(3)\n"
"v_mfma_f32_16x16x32_bf16 v[40:43], v[56:59], v[124:127], v[40:43]\n"
"v_fmac_f32 v222, v150, v26\n"
"v_fmac_f32 v223, v151, v27\n"
"v_fmac_f32 v212, v140, v16\n"
"v_mfma_f32_16x16x32_bf16 v[36:39], v[60:63], v[128:131], v[36:39]\n"
"v_fmac_f32 v213, v141, v17\n"
"v_fmac_f32 v214, v142, v18\n"
"v_fmac_f32 v215, v143, v19\n"
"s_waitcnt lgkmcnt(1)\n"
"v_mfma_f32_16x16x32_bf16 v[40:43], v[64:67], v[128:131], v[40:43]\n"
"v_fmac_f32 v216, v144, v20\n"
"v_fmac_f32 v217, v145, v21\n"
"v_fmac_f32 v218, v146, v22\n"
"v_mfma_f32_16x16x32_bf16 v[36:39], v[68:71], v[132:135], v[36:39]\n"
"v_fmac_f32 v219, v147, v23\n"
"v_cvt_pk_bf16_f32 v80, v220, v221\n"
"v_cvt_pk_bf16_f32 v81, v222, v223\n"
"s_waitcnt lgkmcnt(0)\n"
"v_mfma_f32_16x16x32_bf16 v[40:43], v[72:75], v[132:135], v[40:43]\n"
"s_cmp_eq_u32 s47, 0\n"
"s_cselect_b32 exec_lo, -1, 0\n"
"s_cselect_b32 exec_hi, 0, -1\n"
"global_store_dwordx2 v238, v[80:81], s[20:21]\n"
"s_mov_b64 exec, -1\n"
"v_fmac_f32 v212, v36, v16\n"
"v_fmac_f32 v213, v37, v17\n"
"v_fmac_f32 v214, v38, v18\n"
"v_fmac_f32 v215, v39, v19\n"
"v_fmac_f32 v216, v40, v20\n"
"v_fmac_f32 v217, v41, v21\n"
"v_fmac_f32 v218, v42, v22\n"
"v_fmac_f32 v219, v43, v23\n"
"v_cvt_pk_bf16_f32 v76, v212, v213\n"
"v_cvt_pk_bf16_f32 v77, v214, v215\n"
"v_cvt_pk_bf16_f32 v78, v216, v217\n"
"v_cvt_pk_bf16_f32 v79, v218, v219\n"
"s_nop 0\n"
"v_permlane16_swap_b32 v76, v78\n"
"v_permlane16_swap_b32 v77, v79\n"
"global_store_dwordx4 v228, v[76:79], s[20:21]\n"
"s_branch end_8_%=\n"
"retry_19_%=:\n"
"s_sleep 12\n"
"global_load_dwordx4 v[44:47], v41, s[18:19] sc1\n"
"s_waitcnt vmcnt(0)\n"
"v_cmp_ne_u32 vcc, s24, v45\n"
"v_cmp_ne_u32 s[42:43], s25, v47\n"
"s_nop 0\n"
"s_or_b64 vcc, vcc, s[42:43]\n"
"s_cmp_eq_u64 vcc, 0\n"
"s_cbranch_scc1 ok_20_%=\n"
"s_sub_u32 s31, s31, 1\n"
"s_cbranch_scc0 retry_19_%=\n"
"s_mov_b32 s31, 0\n"
"s_branch ok_20_%=\n"
"retry_22_%=:\n"
"s_sleep 12\n"
"global_load_dwordx4 v[20:23], v13, s[18:19] sc1\n"
"s_waitcnt vmcnt(0)\n"
"v_cmp_ne_u32 vcc, s24, v21\n"
"v_cmp_ne_u32 s[42:43], s25, v23\n"
"s_nop 0\n"
"s_or_b64 vcc, vcc, s[42:43]\n"
"s_cmp_eq_u64 vcc, 0\n"
"s_cbranch_scc1 ok_23_%=\n"
"s_sub_u32 s31, s31, 1\n"
"s_cbranch_scc0 retry_22_%=\n"
"s_mov_b32 s31, 0\n"
"s_branch ok_23_%=\n"
"retry_34_%=:\n"
"s_sleep 12\n"
"global_load_dwordx4 v[20:23], v13, s[18:19] sc1\n"
"s_waitcnt vmcnt(0)\n"
"v_cmp_ne_u32 vcc, s24, v21\n"
"v_cmp_ne_u32 s[42:43], s25, v23\n"
"s_nop 0\n"
"s_or_b64 vcc, vcc, s[42:43]\n"
"s_cmp_eq_u64 vcc, 0\n"
"s_cbranch_scc1 ok_35_%=\n"
"s_sub_u32 s31, s31, 1\n"
"s_cbranch_scc0 retry_34_%=\n"
"s_mov_b32 s31, 0\n"
"s_branch ok_35_%=\n"
"retry_44_%=:\n"
"s_sleep 12\n"
"global_load_dwordx4 v[44:47], v41, s[18:19] sc1\n"
"s_waitcnt vmcnt(0)\n"
"v_cmp_ne_u32 vcc, s24, v45\n"
"v_cmp_ne_u32 s[42:43], s25, v47\n"
"s_nop 0\n"
"s_or_b64 vcc, vcc, s[42:43]\n"
"s_cmp_eq_u64 vcc, 0\n"
"s_cbranch_scc1 ok_45_%=\n"
"s_sub_u32 s31, s31, 1\n"
"s_cbranch_scc0 retry_44_%=\n"
"s_mov_b32 s31, 0\n"
"s_branch ok_45_%=\n"
"retry_47_%=:\n"
"s_sleep 12\n"
"global_load_dwordx4 v[20:23], v13, s[18:19] sc1\n"
"s_waitcnt vmcnt(0)\n"
"v_cmp_ne_u32 vcc, s24, v21\n"
"v_cmp_ne_u32 s[42:43], s25, v23\n"
"s_nop 0\n"
"s_or_b64 vcc, vcc, s[42:43]\n"
"s_cmp_eq_u64 vcc, 0\n"
"s_cbranch_scc1 ok_48_%=\n"
"s_sub_u32 s31, s31, 1\n"
"s_cbranch_scc0 retry_47_%=\n"
"s_mov_b32 s31, 0\n"
"s_branch ok_48_%=\n"
"retry_50_%=:\n"
"s_sleep 12\n"
"global_load_dwordx4 v[44:47], v12, s[18:19] sc1\n"
"s_waitcnt vmcnt(0)\n"
"v_cmp_ne_u32 vcc, s24, v45\n"
"v_cmp_ne_u32 s[42:43], s25, v47\n"
"s_nop 0\n"
"s_or_b64 vcc, vcc, s[42:43]\n"
"s_cmp_eq_u64 vcc, 0\n"
"s_cbranch_scc1 ok_51_%=\n"
"s_sub_u32 s31, s31, 1\n"
"s_cbranch_scc0 retry_50_%=\n"
"s_mov_b32 s31, 0\n"
"s_branch ok_51_%=\n"
"retry_56_%=:\n"
"s_sleep 12\n"
"global_load_dwordx4 v[44:47], v12, s[18:19] sc1\n"
"s_waitcnt vmcnt(0)\n"
"v_cmp_ne_u32 vcc, s24, v45\n"
"v_cmp_ne_u32 s[42:43], s25, v47\n"
"s_nop 0\n"
"s_or_b64 vcc, vcc, s[42:43]\n"
"s_cmp_eq_u64 vcc, 0\n"
"s_cbranch_scc1 ok_57_%=\n"
"s_sub_u32 s31, s31, 1\n"
"s_cbranch_scc0 retry_56_%=\n"
"s_mov_b32 s31, 0\n"
"s_branch ok_57_%=\n"
"retry_60_%=:\n"
"s_sleep 12\n"
"global_load_dwordx2 v[28:29], v252, s[18:19] sc1\n"
"global_load_dwordx2 v[30:31], v251, s[18:19] sc1\n"
"s_waitcnt vmcnt(0)\n"
"v_cmp_ne_u64 vcc, s[24:25], v[28:29]\n"
"v_cmp_ne_u64 s[42:43], s[24:25], v[30:31]\n"
"s_nop 0\n"
"s_or_b64 vcc, vcc, s[42:43]\n"
"s_nop 0\n"
"s_cmp_eq_u64 vcc, 0\n"
"s_cbranch_scc1 ok_61_%=\n"
"s_sub_u32 s31, s31, 1\n"
"s_cbranch_scc0 retry_60_%=\n"
"s_mov_b32 s31, 0\n"
"s_branch ok_61_%=\n"
"retry_73_%=:\n"
"s_sleep 12\n"
"global_load_dwordx2 v[246:247], v239, s[44:45] sc1\n"
"s_waitcnt vmcnt(0)\n"
"v_cmp_ne_u64 vcc, s[24:25], v[246:247]\n"
"s_nop 0\n"
"s_cmp_eq_u64 vcc, 0\n"
"s_cbranch_scc1 ok_74_%=\n"
"s_sub_u32 s31, s31, 1\n"
"s_cbranch_scc0 retry_73_%=\n"
"s_mov_b32 s31, 0\n"
"s_branch ok_74_%=\n"
"retry_78_%=:\n"
"s_sleep 12\n"
"global_load_dwordx2 v[246:247], v4, s[44:45] sc1\n"
"s_waitcnt vmcnt(0)\n"
"v_cmp_ne_u64 vcc, s[24:25], v[246:247]\n"
"s_nop 0\n"
"s_cmp_eq_u64 vcc, 0\n"
"s_cbranch_scc1 ok_79_%=\n"
"s_sub_u32 s31, s31, 1\n"
"s_cbranch_scc0 retry_78_%=\n"
"s_mov_b32 s31, 0\n"
"s_branch ok_79_%=\n"
"retry_84_%=:\n"
"s_sleep 12\n"
"global_load_dwordx2 v[246:247], v8, s[44:45] sc1\n"
"s_waitcnt vmcnt(0)\n"
"v_cmp_ne_u64 vcc, s[24:25], v[246:247]\n"
"s_nop 0\n"
"s_cmp_eq_u64 vcc, 0\n"
"s_cbranch_scc1 ok_85_%=\n"
"s_sub_u32 s31, s31, 1\n"
"s_cbranch_scc0 retry_84_%=\n"
"s_mov_b32 s31, 0\n"
"s_branch ok_85_%=\n"
"exact_9_%=:\n"
"v_mov_b32 v208, 0\n"
"v_mov_b32 v209, 0\n"
"v_mov_b32 v210, 0\n"
"v_mov_b32 v211, 0\n"
"v_mov_b32 v212, 0\n"
"v_mov_b32 v213, 0\n"
"v_mov_b32 v214, 0\n"
"v_mov_b32 v215, 0\n"
"v_mov_b32 v216, 0\n"
"v_mov_b32 v217, 0\n"
"v_mov_b32 v218, 0\n"
"v_mov_b32 v219, 0\n"
"v_mov_b32 v220, 0\n"
"v_mov_b32 v221, 0\n"
"v_mov_b32 v222, 0\n"
"v_mov_b32 v223, 0\n"
"v_mov_b32 v12, 0xff800000\n"
"s_mov_b32 s29, 0\n"
"s_and_b32 s40, s22, 7\n"
"s_lshl_b32 s40, s40, 2\n"
"s_lshr_b32 s41, s22, 3\n"
"s_and_b32 s41, s41, 3\n"
"s_add_u32 s55, s40, s41\n"
"s_and_b32 s56, s55, 1\n"
"s_lshr_b32 s41, s55, 1\n"
"s_mul_i32 s41, s41, 5\n"
"s_add_u32 s57, s41, 2\n"
"s_mul_i32 s41, s56, 3\n"
"s_add_u32 s41, s57, s41\n"
"s_sub_u32 s58, s41, 2\n"
"s_lshl_b32 s41, s58, 15\n"
"s_add_u32 s41, s41, 2621440\n"
"v_add_u32 v13, s41, v4\n"
"s_lshl_b32 s41, s57, 15\n"
"s_lshl_b32 s40, s56, 3\n"
"s_add_u32 s40, s41, s40\n"
"s_add_u32 s40, s40, 2621440\n"
"v_add_u32 v14, s40, v3\n"
"v_min_u32 v5, 19, v1\n"
"v_mul_u32_u24 v6, 10240, v2\n"
"v_lshl_add_u32 v6, v5, 4, v6\n"
"s_mul_i32 s40, s55, 320\n"
"s_add_u32 s40, s40, 5242880\n"
"v_add_u32 v15, s40, v6\n"
"v_cmp_eq_u32 vcc, 20, v1\n"
"v_mov_b32 v5, 0x3f803f80\n"
"v_cmp_gt_u32 s[42:43], 20, v1\n"
"v_cndmask_b32 v10, 0, v5, vcc\n"
"xloop_87_%=:\n"
"global_load_dwordx4 v[64:67], v13, s[18:19] sc1\n"
"global_load_dwordx2 v[68:69], v14, s[18:19] sc1\n"
"global_load_dwordx4 v[72:75], v15, s[18:19] sc1\n"
"v_add_u32 v15, 20480, v15\n"
"global_load_dwordx4 v[80:83], v15, s[18:19] sc1\n"
"v_add_u32 v15, 20480, v15\n"
"v_add_u32 v13, 512, v13\n"
"v_add_u32 v14, 512, v14\n"
"s_waitcnt vmcnt(0)\n"
"v_mfma_f32_32x32x16_bf16 v[16:31], v[64:67], v[52:55], 0\n"
"v_mfma_f32_32x32x8_bf16 v[16:31], v[68:69], v[60:61], v[16:31]\n"
"v_cndmask_b32 v72, v10, v72, s[42:43]\n"
"v_cndmask_b32 v73, v10, v73, s[42:43]\n"
"v_cndmask_b32 v74, v10, v74, s[42:43]\n"
"v_cndmask_b32 v75, v10, v75, s[42:43]\n"
"v_cndmask_b32 v80, v10, v80, s[42:43]\n"
"v_cndmask_b32 v81, v10, v81, s[42:43]\n"
"v_cndmask_b32 v82, v10, v82, s[42:43]\n"
"v_cndmask_b32 v83, v10, v83, s[42:43]\n"
"s_nop 3\n"
"v_max3_f32 v5, v16, v17, v12\n"
"v_max3_f32 v5, v18, v19, v5\n"
"v_max3_f32 v5, v20, v21, v5\n"
"v_max3_f32 v5, v22, v23, v5\n"
"v_max3_f32 v5, v24, v25, v5\n"
"v_max3_f32 v5, v26, v27, v5\n"
"v_max3_f32 v5, v28, v29, v5\n"
"v_max3_f32 v5, v30, v31, v5\n"
"v_mov_b32 v8, v5\n"
"s_nop 1\n"
"v_permlane32_swap_b32 v5, v8\n"
"s_nop 1\n"
"v_max_f32 v5, v5, v8\n"
"v_sub_f32 v8, v12, v5\n"
"v_exp_f32 v8, v8\n"
"v_mov_b32 v12, v5\n"
"v_sub_f32 v16, v16, v5\n"
"v_sub_f32 v17, v17, v5\n"
"v_sub_f32 v18, v18, v5\n"
"v_sub_f32 v19, v19, v5\n"
"v_sub_f32 v20, v20, v5\n"
"v_sub_f32 v21, v21, v5\n"
"v_sub_f32 v22, v22, v5\n"
"v_sub_f32 v23, v23, v5\n"
"v_sub_f32 v24, v24, v5\n"
"v_sub_f32 v25, v25, v5\n"
"v_sub_f32 v26, v26, v5\n"
"v_sub_f32 v27, v27, v5\n"
"v_sub_f32 v28, v28, v5\n"
"v_sub_f32 v29, v29, v5\n"
"v_sub_f32 v30, v30, v5\n"
"v_sub_f32 v31, v31, v5\n"
"v_exp_f32 v16, v16\n"
"v_exp_f32 v17, v17\n"
"v_exp_f32 v18, v18\n"
"v_exp_f32 v19, v19\n"
"v_exp_f32 v20, v20\n"
"v_exp_f32 v21, v21\n"
"v_exp_f32 v22, v22\n"
"v_exp_f32 v23, v23\n"
"v_exp_f32 v24, v24\n"
"v_exp_f32 v25, v25\n"
"v_exp_f32 v26, v26\n"
"v_exp_f32 v27, v27\n"
"v_exp_f32 v28, v28\n"
"v_exp_f32 v29, v29\n"
"v_exp_f32 v30, v30\n"
"v_exp_f32 v31, v31\n"
"v_mul_f32 v208, v208, v8\n"
"v_mul_f32 v209, v209, v8\n"
"v_mul_f32 v210, v210, v8\n"
"v_mul_f32 v211, v211, v8\n"
"v_mul_f32 v212, v212, v8\n"
"v_mul_f32 v213, v213, v8\n"
"v_mul_f32 v214, v214, v8\n"
"v_mul_f32 v215, v215, v8\n"
"v_mul_f32 v216, v216, v8\n"
"v_mul_f32 v217, v217, v8\n"
"v_mul_f32 v218, v218, v8\n"
"v_mul_f32 v219, v219, v8\n"
"v_mul_f32 v220, v220, v8\n"
"v_mul_f32 v221, v221, v8\n"
"v_mul_f32 v222, v222, v8\n"
"v_mul_f32 v223, v223, v8\n"
"s_nop 1\n"
"v_smfmac_f32_32x32x32_bf16 v[208:223], v[72:75], v[16:23], v240\n"
"v_smfmac_f32_32x32x32_bf16 v[208:223], v[80:83], v[24:31], v240\n"
"s_add_u32 s29, s29, 1\n"
"s_cmp_lt_u32 s29, 64\n"
"s_cbranch_scc1 xloop_87_%=\n"
"s_nop 11\n"
"v_mov_b32 v8, v216\n"
"v_mov_b32 v9, v216\n"
"s_nop 1\n"
"v_permlane32_swap_b32 v8, v9\n"
"s_nop 1\n"
"s_branch norm_80_%=\n"
"end_8_%=:\n"
      :
      : "s"(__builtin_amdgcn_kernarg_segment_ptr()), "s"(blockIdx.x), "s"(w),
        "s"((unsigned)(size_t)(__attribute__((address_space(3))) char*)lds), "s"(&k_tables)
      : "v0", "v1", "v2", "v3", "v4", "v5", "v6", "v7", "v8", "v9", "v10", "v11", "v12", "v13", "v14", "v15", "v16", "v17", "v18", "v19", "v20", "v21", "v22", "v23",
      "v24", "v25", "v26", "v27", "v28", "v29", "v30", "v31", "v32", "v33", "v34", "v35", "v36", "v37", "v38", "v39", "v40", "v41", "v42", "v43", "v44", "v45", "v46", "v47",
      "v48", "v49", "v50", "v51", "v52", "v53", "v54", "v55", "v56", "v57", "v58", "v59", "v60", "v61", "v62", "v63", "v64", "v65", "v66", "v67", "v68", "v69", "v70", "v71",
      "v72", "v73", "v74", "v75", "v76", "v77", "v78", "v79", "v80", "v81", "v82", "v83", "v84", "v85", "v86", "v87", "v88", "v89", "v90", "v91", "v92", "v93", "v94", "v95",
      "v96", "v97", "v98", "v99", "v100", "v101", "v102", "v103", "v104", "v105", "v106", "v107", "v108", "v109", "v110", "v111", "v112", "v113", "v114", "v115", "v116", "v117", "v118", "v119",
      "v120", "v121", "v122", "v123", "v124", "v125", "v126", "v127", "v128", "v129", "v130", "v131", "v132", "v133", "v134", "v135", "v136", "v137", "v138", "v139", "v140", "v141", "v142", "v143",
      "v144", "v145", "v146", "v147", "v148", "v149", "v150", "v151", "v152", "v153", "v154", "v155", "v156", "v157", "v158", "v159", "v160", "v161", "v162", "v163", "v164", "v165", "v166", "v167",
      "v168", "v169", "v170", "v171", "v172", "v173", "v174", "v175", "v176", "v177", "v178", "v179", "v180", "v181", "v182", "v183", "v184", "v185", "v186", "v187", "v188", "v189", "v190", "v191",
      "v192", "v193", "v194", "v195", "v196", "v197", "v198", "v199", "v200", "v201", "v202", "v203", "v204", "v205", "v206", "v207", "v208", "v209", "v210", "v211", "v212", "v213", "v214", "v215",
      "v216", "v217", "v218", "v219", "v220", "v221", "v222", "v223", "v224", "v225", "v226", "v227", "v228", "v229", "v230", "v231", "v232", "v233", "v234", "v235", "v236", "v237", "v238", "v239",
      "v240", "v241", "v242", "v243", "v244", "v245", "v246", "v247", "v248", "v249", "v250", "v251", "v252", "v253", "v254", "v255", "s8", "s9", "s10", "s11", "s12", "s13", "s14", "s15",
      "s16", "s17", "s18", "s19", "s20", "s21", "s22", "s23", "s24", "s25", "s26", "s27", "s28", "s29", "s30", "s31", "s33", "s34", "s35", "s36", "s37", "s38", "s39", "s40",
      "s41", "s42", "s43", "s44", "s45", "s46", "s47", "s48", "s49", "s50", "s51", "s52", "s53", "s54", "s55", "s56", "s57", "s58", "s59", "s60", "s61", "s62", "s63", "s64",
      "s65", "s66", "s67", "s68", "s69", "s70", "s71", "s72", "s73", "s74", "s75", "s76", "s77", "s78", "s79", "s80", "s81", "s82", "s83", "s84", "s85", "s86", "s87", "s88",
      "s89", "s90", "s91", "s92", "s93", "s94", "s95", "s96", "s97", "s98", "s99", "vcc", "scc", "memory");
}

extern "C" void kernel_launch(void* const* d_in, const int* in_sizes, int n_in, void* d_out, int out_size,
                              void* d_ws, size_t ws_size, hipStream_t stream) {
  fused_block<<<256, 512, 0, stream>>>(d_in[0], d_in[1], d_in[2], d_in[4], d_in[5], d_ws, d_out);
}
```
